```python
import torch

def _softmax(x, axis=-1):
    return torch.softmax(x, dim=axis)
def _gelu(x):
    return torch.nn.functional.gelu(x, approximate="tanh")
def _silu(x):
    return torch.nn.functional.silu(x)
def _sigmoid(x):
    return torch.sigmoid(x)
def _layernorm(x, axis=-1, eps=1e-5):
    m = x.mean(dim=axis, keepdim=True)
    v = x.var(dim=axis, keepdim=True, unbiased=False)
    return (x - m) / torch.sqrt(v + eps)
def _scatter_add(tgt, idx, src, axis=0):
    return tgt.clone().index_add_(axis, idx, src)
def _segment_sum(src, seg, n):
    out = torch.zeros((n,) + src.shape[1:], dtype=src.dtype)
    return out.index_add_(0, seg, src)
def _pad(x, pw):
    flat = tuple(v for pair in reversed(pw) for v in pair)
    return torch.nn.functional.pad(x, flat)

kernel_name = 'gated_mix__sliding_block_cached__mixer_channel_mlp__decode'

def setup_inputs():

    _w = (lambda a: (lambda t: t.to(torch.bfloat16) if t.is_floating_point() else t)(torch.as_tensor(a)))
    def _A_setup():
        B, Sq, D = 4, 1, 512
        Hq, Hkv, Sk, Dh = 32, 8, 4096, 128
        assert Hq % Hkv == 0, (Hq, Hkv)
        return {
            "x": _w((lambda *s: torch.randn(*s, dtype=torch.bfloat16) * 0.05)(B, Sq, D)),
            "k_cache": _w((lambda *s: torch.randn(*s, dtype=torch.bfloat16) * 6.4)(B, Hkv, Sk, Dh)),
            "v_cache": _w((lambda *s: torch.randn(*s, dtype=torch.bfloat16) * 0.4)(B, Hkv, Sk, Dh)),
            "wq": _w((lambda *s: torch.randn(*s, dtype=torch.bfloat16) * 6.4)(D, Hq * Dh) * (D ** -0.5)),
            "wo": _w((lambda *s: torch.randn(*s, dtype=torch.bfloat16) * 0.4)(Hq * Dh, D) * ((Hq * Dh) ** -0.5)),
        }
    _A_in = _A_setup()

    def _B_setup():
        B, S, D, F = 4, 1, 512, 1792
        return {
            "x": _w((lambda *s: torch.randn(*s, dtype=torch.bfloat16) * 0.05)(B, S, D)),
            "w1": _w((lambda *s: torch.randn(*s, dtype=torch.bfloat16) * 0.8)(D, F) * (D ** -0.5)),
            "b1": _w((lambda *s: torch.randn(*s, dtype=torch.bfloat16) * 0.8)(F)),
            "w2": _w((lambda *s: torch.randn(*s, dtype=torch.bfloat16) * 0.05)(F, D) * (F ** -0.5)),
            "b2": _w((lambda *s: torch.randn(*s, dtype=torch.bfloat16) * 0.05)(D)),
        }
    _B_in = _B_setup()

    return {
        "A_x": _A_in["x"],
        "A_k_cache": _A_in["k_cache"],
        "A_v_cache": _A_in["v_cache"],
        "A_wq": _A_in["wq"],
        "A_wo": _A_in["wo"],
        "B_x": _B_in["x"],
        "B_w1": _B_in["w1"],
        "B_b1": _B_in["b1"],
        "B_w2": _B_in["w2"],
        "B_b2": _B_in["b2"],
        "gate_w": _w((lambda *s: torch.randn(*s, dtype=torch.bfloat16) * 0.05)(512) * 16.0),
    }

def reference(A_x, A_k_cache, A_v_cache, A_wq, A_wo, B_x, B_w1, B_b1, B_w2, B_b2, gate_w):
    def _A_ref(x, k_cache, v_cache, wq, wo):
        B, Sq, D = x.shape
        Hq, Dh = 32, 128
        Hkv, Sk = k_cache.shape[1], k_cache.shape[2]
        W = min(4096, max(256, Sk // 4))
        rep = Hq // Hkv
        q = (lambda x, *d: x.permute(*d))((x @ wq).reshape(B, Sq, Hq, Dh), 0, 2, 1, 3)
        k = (lambda x, n, axis: torch.repeat_interleave(x, n, dim=axis))(k_cache[:, :, Sk - W:, :], rep, axis=1)
        v = (lambda x, n, axis: torch.repeat_interleave(x, n, dim=axis))(v_cache[:, :, Sk - W:, :], rep, axis=1)
        scores = torch.einsum("bhqd,bhkd->bhqk", q, k) * (Dh ** -0.5)
        p = _softmax(scores, axis=-1)
        o = torch.einsum("bhqk,bhkd->bhqd", p, v)
        return (lambda x, *d: x.permute(*d))(o, 0, 2, 1, 3).reshape(B, Sq, Hq * Dh) @ wo

    def _B_ref(x, w1, b1, w2, b2):
        y = _layernorm(x, axis=-1)
        h = _gelu(torch.einsum("bsd,df->bsf", y, w1) + b1)
        return torch.einsum("bsf,fd->bsd", h, w2) + b2

    x = A_x
    g = _sigmoid((x * gate_w).sum(-1, keepdims=True))
    a = _A_ref(x, A_k_cache, A_v_cache, A_wq, A_wo)
    b = _B_ref(x, B_w1, B_b1, B_w2, B_b2)
    return g * a + (1 - g) * b
```

```cpp
#include <hip/hip_runtime.h>
#include <hip/hip_bf16.h>
#include <stdint.h>

#define GAS __attribute__((address_space(1)))
typedef unsigned short u16;
typedef unsigned v4u __attribute__((ext_vector_type(4)));
typedef unsigned v2u __attribute__((ext_vector_type(2)));
typedef float f32x4 __attribute__((ext_vector_type(4)));
typedef float f32x2 __attribute__((ext_vector_type(2)));
typedef __bf16 bf16x8 __attribute__((ext_vector_type(8)));
typedef short s16x4 __attribute__((ext_vector_type(4)));
typedef GAS unsigned gu32;

constexpr int DM = 512, SK = 4096, DH = 128, WINDOW = 1024, FF = 1792, NQ = 4096;
constexpr unsigned WS_QG = 4096;
constexpr unsigned WS_P2 = WS_QG + 65536;
constexpr unsigned WS_ML = WS_P2 + (1u << 20);
constexpr unsigned WS_P3 = WS_ML + 16384;
constexpr unsigned WS_END = WS_P3 + (2u << 20);
constexpr unsigned SPIN_CAP = 1u << 17;
constexpr int AUX_SC1 = 16;
constexpr float LOG2E = 1.4426950408889634f;

__device__ __forceinline__ float bfr(float f) { return (float)(__bf16)f; }
__device__ __forceinline__ unsigned bfbits(float f) { return (unsigned)__builtin_bit_cast(u16, (__bf16)f); }
__device__ __forceinline__ unsigned pack2(float lo, float hi) { return bfbits(lo) | (bfbits(hi) << 16); }
__device__ __forceinline__ float lo2f(unsigned u) { return __builtin_bit_cast(float, u << 16); }
__device__ __forceinline__ float hi2f(unsigned u) { return __builtin_bit_cast(float, u & 0xffff0000u); }
__device__ __forceinline__ unsigned perm_lo(unsigned a, unsigned b) { return __builtin_amdgcn_perm(b, a, 0x05040100u); }
__device__ __forceinline__ unsigned perm_hi(unsigned a, unsigned b) { return __builtin_amdgcn_perm(b, a, 0x07060302u); }

template <int CTRL> __device__ __forceinline__ float dpp(float x) {
  return __builtin_bit_cast(float, __builtin_amdgcn_mov_dpp(__builtin_bit_cast(int, x), CTRL, 0xf, 0xf, true));
}
constexpr int XOR1 = 0xB1, XOR2 = 0x4E, ROR8 = 0x128, ROR4 = 0x124;
__device__ __forceinline__ float x16_max(float x) {
  auto s = __builtin_amdgcn_permlane16_swap(__float_as_uint(x), __float_as_uint(x), false, false);
  return fmaxf(__uint_as_float(s[0]), __uint_as_float(s[1]));
}
__device__ __forceinline__ float x32_max(float x) {
  auto s = __builtin_amdgcn_permlane32_swap(__float_as_uint(x), __float_as_uint(x), false, false);
  return fmaxf(__uint_as_float(s[0]), __uint_as_float(s[1]));
}
__device__ __forceinline__ float x16_sum(float x) {
  auto s = __builtin_amdgcn_permlane16_swap(__float_as_uint(x), __float_as_uint(x), false, false);
  return __uint_as_float(s[0]) + __uint_as_float(s[1]);
}
__device__ __forceinline__ float x32_sum(float x) {
  auto s = __builtin_amdgcn_permlane32_swap(__float_as_uint(x), __float_as_uint(x), false, false);
  return __uint_as_float(s[0]) + __uint_as_float(s[1]);
}
__device__ __forceinline__ float wave_sum(float x) {
  x += dpp<XOR1>(x); x += dpp<XOR2>(x); x += dpp<ROR4>(x); x += dpp<ROR8>(x);
  return x32_sum(x16_sum(x));
}
__device__ __forceinline__ f32x4 mfma32(v4u a, v4u b, f32x4 c) {
  return __builtin_amdgcn_mfma_f32_16x16x32_bf16(__builtin_bit_cast(bf16x8, a), __builtin_bit_cast(bf16x8, b), c, 0, 0, 0);
}
__device__ __forceinline__ f32x4 mfma16(v2u a, v2u b, f32x4 c) {
  return __builtin_amdgcn_mfma_f32_16x16x16bf16_1k(__builtin_bit_cast(s16x4, a), __builtin_bit_cast(s16x4, b), c, 0, 0, 0);
}
#define BAR() do { asm volatile("s_waitcnt lgkmcnt(0)" ::: "memory"); __builtin_amdgcn_s_barrier(); asm volatile("" ::: "memory"); } while (0)
#define PIN() __builtin_amdgcn_sched_barrier(0)

template <typename T> __device__ __forceinline__ T ldg(const u16* p) { return *(const GAS T*)p; }
template <typename T> __device__ __forceinline__ T ldnt(const u16* p) { return __builtin_nontemporal_load((const GAS T*)p); }

constexpr int L_ST = 0;
constexpr int L_RQ = 16;
constexpr int L_RH = L_RQ + 1024;
constexpr int L_HB = L_RH + 1024;
constexpr int L_MO = L_HB + 32;
constexpr int L_ML = L_MO + 2048;
constexpr int L_OB = L_ML + 32;
constexpr int L_END = L_OB + 64;

__global__ void __launch_bounds__(256, 2)
fused_kernel(const u16* __restrict__ x, const u16* __restrict__ kcache, const u16* __restrict__ vcache,
             const u16* __restrict__ wq, const u16* __restrict__ wo, const u16* __restrict__ w1,
             const u16* __restrict__ b1, const u16* __restrict__ w2, const u16* __restrict__ b2,
             const u16* __restrict__ gw, u16* __restrict__ out, unsigned char* ws) {
  __shared__ __attribute__((aligned(16))) float lds[L_END];
  const int tid = threadIdx.x, lane = tid & 63, w = __builtin_amdgcn_readfirstlane(tid >> 6);
  const int g = lane >> 4, c = lane & 15;
  const int bid = blockIdx.x, hk = bid & 7, idx = bid >> 3;
  const int ab = idx >> 3, as = idx & 7;
  const int kgl = idx >> 1, cg = idx & 1, kg = hk * 16 + kgl;
  const bool has_mlp = kgl < 14;
  const int f0 = 224 * hk + 16 * kgl;
  const __amdgpu_buffer_rsrc_t rs = __builtin_amdgcn_make_buffer_rsrc((void*)ws, 0, (int)WS_END, 0x00020000);

  const unsigned E = __hip_atomic_load((gu32*)ws, __ATOMIC_RELAXED, __HIP_MEMORY_SCOPE_AGENT);
  const int kc = c >> 2, ch = c & 3;
  const int krow = 128 * w + 32 * kc + 8 * g;
  const v4u xa = ldg<v4u>(x + (c & 3) * DM + krow);
  const v4u xs = ldg<v4u>(x + w * DM + 8 * lane);
  const v4u gwv = ldg<v4u>(gw + 8 * lane);
  v2u wqv[8], w1v[8];
  {
    const u16* p = wq + (size_t)krow * NQ + hk * 512 + idx * 16 + 4 * ch;
#pragma unroll
    for (int j = 0; j < 8; ++j) wqv[j] = ldg<v2u>(p + j * NQ);
  }
  const __amdgpu_buffer_rsrc_t rw1 = __builtin_amdgcn_make_buffer_rsrc((void*)w1, 0, has_mlp ? DM * FF * 2 : 0, 0x00020000);
  const __amdgpu_buffer_rsrc_t rb1 = __builtin_amdgcn_make_buffer_rsrc((void*)b1, 0, has_mlp ? FF * 2 : 0, 0x00020000);
  const __amdgpu_buffer_rsrc_t rw2 = __builtin_amdgcn_make_buffer_rsrc((void*)w2, 0, has_mlp ? DM * FF * 2 : 0, 0x00020000);
  {
    const int o = (krow * FF + f0 + 4 * ch) * 2;
#pragma unroll
    for (int j = 0; j < 8; ++j) w1v[j] = __builtin_amdgcn_raw_buffer_load_b64(rw1, o + j * FF * 2, 0, 0);
  }
  const v2u b1v = __builtin_amdgcn_raw_buffer_load_b64(rb1, (f0 + 4 * ch) * 2, 0, 0);
  v4u kf[8], vf[8];
  {
    const size_t kvoff = ((size_t)(ab * 8 + hk) * SK + (SK - WINDOW) + 128 * as + 32 * w) * DH;
    const u16* kp = kcache + kvoff + c * DH + 8 * g;
#pragma unroll
    for (int t = 0; t < 2; ++t)
#pragma unroll
      for (int ks = 0; ks < 4; ++ks) kf[t * 4 + ks] = ldnt<v4u>(kp + t * 16 * DH + 32 * ks);
    const u16* vp = vcache + kvoff + 4 * g * DH + 8 * c;
#pragma unroll
    for (int j = 0; j < 8; ++j) vf[j] = ldnt<v4u>(vp + (16 * (j >> 2) + (j & 3)) * DH);
  }
  PIN();

  {
    float xv[8], gv[8];
#pragma unroll
    for (int k = 0; k < 4; ++k) { xv[2 * k] = lo2f(xs[k]); xv[2 * k + 1] = hi2f(xs[k]); gv[2 * k] = lo2f(gwv[k]); gv[2 * k + 1] = hi2f(gwv[k]); }
    float s = 0.f, gs = 0.f;
#pragma unroll
    for (int k = 0; k < 8; ++k) { s += xv[k]; gs += bfr(xv[k] * gv[k]); }
    s = wave_sum(s); gs = wave_sum(gs);
    const float mean = s * (1.0f / DM);
    float vs = 0.f;
#pragma unroll
    for (int k = 0; k < 8; ++k) { const float d = xv[k] - mean; vs = fmaf(d, d, vs); }
    vs = wave_sum(vs);
    if (lane == 0) {
      const float var_b = bfr(vs * (1.0f / DM));
      const float den = bfr(sqrtf(bfr(var_b + 1e-5f)));
      const float gt = bfr(1.0f / (1.0f + __expf(-bfr(gs))));
      *(f32x4*)(lds + L_ST + 4 * w) = f32x4{bfr(mean), den, gt, bfr(1.0f - gt)};
    }
  }
  BAR();
  {
    const f32x4 st = *(const f32x4*)(lds + L_ST + 4 * (c & 3));
    v4u ya;
#pragma unroll
    for (int k = 0; k < 4; ++k) {
      const float y0 = bfr(bfr(lo2f(xa[k]) - st[0]) / st[1]), y1 = bfr(bfr(hi2f(xa[k]) - st[0]) / st[1]);
      ya[k] = pack2(y0, y1);
    }
    f32x4 dq[4], dh[4];
#pragma unroll
    for (int i = 0; i < 4; ++i) {
      v4u fq, fh;
#pragma unroll
      for (int jj = 0; jj < 4; ++jj) {
        fq[jj] = (i & 1) ? perm_hi(wqv[2 * jj][i >> 1], wqv[2 * jj + 1][i >> 1]) : perm_lo(wqv[2 * jj][i >> 1], wqv[2 * jj + 1][i >> 1]);
        fh[jj] = (i & 1) ? perm_hi(w1v[2 * jj][i >> 1], w1v[2 * jj + 1][i >> 1]) : perm_lo(w1v[2 * jj][i >> 1], w1v[2 * jj + 1][i >> 1]);
      }
      dq[i] = mfma32(xa, fq, f32x4{0.f, 0.f, 0.f, 0.f});
      dh[i] = mfma32(ya, fh, f32x4{0.f, 0.f, 0.f, 0.f});
    }
    if (g == kc) {
#pragma unroll
      for (int r = 0; r < 4; ++r) {
        *(f32x4*)(lds + L_RQ + ((w * 4 + kc) * 4 + r) * 16 + 4 * ch) = f32x4{dq[0][r], dq[1][r], dq[2][r], dq[3][r]};
        *(f32x4*)(lds + L_RH + ((w * 4 + kc) * 4 + r) * 16 + 4 * ch) = f32x4{dh[0][r], dh[1][r], dh[2][r], dh[3][r]};
      }
    }
  }
  BAR();
  const unsigned T = E + 1u;
  if (w < 2) {
    const float* red = lds + (w == 0 ? L_RQ : L_RH) + (g * 16 + (c >> 2)) * 16 + 4 * ch;
    f32x4 a = *(const f32x4*)(red);
#pragma unroll
    for (int p = 1; p < 4; ++p) a += *(const f32x4*)(red + p * 64);
#pragma unroll
    for (int i = 0; i < 4; ++i) a[i] = x32_sum(x16_sum(a[i]));
    if (w == 0) {
      if (g == 0) {
        const v4u qv = {pack2(a[0], a[1]), T, pack2(a[2], a[3]), T};
        __builtin_amdgcn_raw_buffer_store_b128(qv, rs, WS_QG + ((c >> 2) * 1024 + hk * 128 + idx * 4 + ch) * 16, 0, AUX_SC1);
      }
    } else if (g == 0) {
      const float bb[4] = {lo2f(b1v[0]), hi2f(b1v[0]), lo2f(b1v[1]), hi2f(b1v[1])};
      float hv[4];
#pragma unroll
      for (int i = 0; i < 4; ++i) {
        const float u = bfr(bfr(a[i]) + bb[i]);
        const float inner = 0.7978845608028654f * (u + 0.044715f * u * u * u);
        hv[i] = 0.5f * u * (1.0f + tanhf(inner));
      }
      *(v2u*)((u16*)(lds + L_HB) + (c >> 2) * 16 + 4 * ch) = v2u{pack2(hv[0], hv[1]), pack2(hv[2], hv[3])};
    }
  }
  asm volatile("" :: "v"(kf[7]));
  PIN();
  v4u qraw[8];
  const unsigned qoff = WS_QG + (ab * 1024 + hk * 128 + (c >> 2) * 32 + 2 * g) * 16;
#pragma unroll
  for (int ks = 0; ks < 4; ++ks) {
    qraw[2 * ks] = __builtin_amdgcn_raw_buffer_load_b128(rs, qoff + ks * 128, 0, AUX_SC1);
    qraw[2 * ks + 1] = __builtin_amdgcn_raw_buffer_load_b128(rs, qoff + ks * 128 + 16, 0, AUX_SC1);
  }
  PIN();
  v2u wov[8], w2v[4];
  {
    const u16* p = wo + (size_t)(32 * kg + 8 * g) * DM + 256 * cg + 64 * w + 4 * c;
#pragma unroll
    for (int j = 0; j < 8; ++j) wov[j] = ldnt<v2u>(p + j * DM);
    const int o2 = ((f0 + 4 * g) * DM + 256 * cg + 64 * w + 4 * c) * 2;
#pragma unroll
    for (int j = 0; j < 4; ++j) w2v[j] = __builtin_amdgcn_raw_buffer_load_b64(rw2, o2 + j * DM * 2, 0, 2);
  }
  PIN();
  for (unsigned spins = 0; spins < SPIN_CAP; ++spins) {
    bool ok = true;
#pragma unroll
    for (int i = 0; i < 8; ++i) ok &= (qraw[i][1] == T) & (qraw[i][3] == T);
    if (__all(ok)) break;
    asm volatile("" ::: "memory");
#pragma unroll
    for (int ks = 0; ks < 4; ++ks) {
      qraw[2 * ks] = __builtin_amdgcn_raw_buffer_load_b128(rs, qoff + ks * 128, 0, AUX_SC1);
      qraw[2 * ks + 1] = __builtin_amdgcn_raw_buffer_load_b128(rs, qoff + ks * 128 + 16, 0, AUX_SC1);
    }
  }
  {
    f32x4 s0 = {0.f, 0.f, 0.f, 0.f}, s1 = {0.f, 0.f, 0.f, 0.f};
#pragma unroll
    for (int ks = 0; ks < 4; ++ks) {
      const v4u qf = {qraw[2 * ks][0], qraw[2 * ks][2], qraw[2 * ks + 1][0], qraw[2 * ks + 1][2]};
      s0 = mfma32(kf[ks], qf, s0);
      s1 = mfma32(kf[4 + ks], qf, s1);
    }
    float sc[8];
#pragma unroll
    for (int j = 0; j < 4; ++j) { sc[j] = bfr(bfr(s0[j]) * 0.08838834764831845f); sc[4 + j] = bfr(bfr(s1[j]) * 0.08838834764831845f); }
    float m = fmaxf(fmaxf(fmaxf(sc[0], sc[1]), fmaxf(sc[2], sc[3])), fmaxf(fmaxf(sc[4], sc[5]), fmaxf(sc[6], sc[7])));
    m = x32_max(x16_max(m));
    float p[8], l = 0.f;
#pragma unroll
    for (int j = 0; j < 8; ++j) { p[j] = __builtin_amdgcn_exp2f((sc[j] - m) * LOG2E); l += p[j]; }
    l = x32_sum(x16_sum(l));
    const v4u pf = {pack2(p[0], p[1]), pack2(p[2], p[3]), pack2(p[4], p[5]), pack2(p[6], p[7])};
    float ov[8];
#pragma unroll
    for (int i = 0; i < 8; ++i) {
      v4u vt;
#pragma unroll
      for (int jj = 0; jj < 4; ++jj)
        vt[jj] = (i & 1) ? perm_hi(vf[2 * jj][i >> 1], vf[2 * jj + 1][i >> 1]) : perm_lo(vf[2 * jj][i >> 1], vf[2 * jj + 1][i >> 1]);
      const f32x4 o = mfma32(pf, vt, f32x4{0.f, 0.f, 0.f, 0.f});
      ov[i] = o[0];
    }
    float* mo = lds + L_MO + (w * 4 + g) * 128 + 8 * c;
    *(f32x4*)(mo) = f32x4{ov[0], ov[1], ov[2], ov[3]};
    *(f32x4*)(mo + 4) = f32x4{ov[4], ov[5], ov[6], ov[7]};
    if (g == 0 && (c & 3) == 0) *(f32x2*)(lds + L_ML + (w * 4 + (c >> 2)) * 2) = f32x2{m, l};
  }
  BAR();
  {
    f32x2 ml[4], ov[4];
#pragma unroll
    for (int u = 0; u < 4; ++u) { ml[u] = *(const f32x2*)(lds + L_ML + (u * 4 + w) * 2); ov[u] = *(const f32x2*)(lds + L_MO + (u * 4 + w) * 128 + 2 * lane); }
    const float M = fmaxf(fmaxf(ml[0][0], ml[1][0]), fmaxf(ml[2][0], ml[3][0]));
    float L = 0.f, o0 = 0.f, o1 = 0.f;
#pragma unroll
    for (int u = 0; u < 4; ++u) {
      const float f = __builtin_amdgcn_exp2f((ml[u][0] - M) * LOG2E);
      L = fmaf(f, ml[u][1], L); o0 = fmaf(f, ov[u][0], o0); o1 = fmaf(f, ov[u][1], o1);
    }
    const int hg = 4 * hk + w;
    const v4u pv = {__float_as_uint(o0), T, __float_as_uint(o1), T};
    __builtin_amdgcn_raw_buffer_store_b128(pv, rs, WS_P2 + (((hg * 4 + (lane >> 4)) * 4 + ab) * 8 + as) * 256 + (lane & 15) * 16, 0, AUX_SC1);
    if (lane == 0) {
      const v4u mv = {__float_as_uint(M), T, __float_as_uint(L), T};
      __builtin_amdgcn_raw_buffer_store_b128(mv, rs, WS_ML + ((hg * 4 + ab) * 8 + as) * 16, 0, AUX_SC1);
    }
  }
  PIN();
  const int h2 = 4 * hk + (kgl >> 2);
  const unsigned p2off = WS_P2 + (((h2 * 4 + (kgl & 3)) * 4 + w) * 8 + (lane >> 3)) * 256 + (lane & 7) * 32;
  const unsigned mloff = WS_ML + ((h2 * 4 + w) * 8 + (lane >> 3)) * 16;
  v4u a0 = __builtin_amdgcn_raw_buffer_load_b128(rs, p2off, 0, AUX_SC1);
  v4u a1 = __builtin_amdgcn_raw_buffer_load_b128(rs, p2off + 16, 0, AUX_SC1);
  v4u am = __builtin_amdgcn_raw_buffer_load_b128(rs, mloff, 0, AUX_SC1);
  PIN();
  v4u wof[4];
  float accm[4];
  const f32x4 stg = *(const f32x4*)(lds + L_ST + 4 * g);
  {
    const v2u ha = *(const v2u*)((const u16*)(lds + L_HB) + (c >> 2) * 16 + 4 * g);
#pragma unroll
    for (int i = 0; i < 4; ++i) {
#pragma unroll
      for (int jj = 0; jj < 4; ++jj)
        wof[i][jj] = (i & 1) ? perm_hi(wov[2 * jj][i >> 1], wov[2 * jj + 1][i >> 1]) : perm_lo(wov[2 * jj][i >> 1], wov[2 * jj + 1][i >> 1]);
      v2u f2;
#pragma unroll
      for (int jj = 0; jj < 2; ++jj)
        f2[jj] = (i & 1) ? perm_hi(w2v[2 * jj][i >> 1], w2v[2 * jj + 1][i >> 1]) : perm_lo(w2v[2 * jj][i >> 1], w2v[2 * jj + 1][i >> 1]);
      const f32x4 d = mfma16(ha, f2, f32x4{0.f, 0.f, 0.f, 0.f});
      accm[i] = has_mlp ? stg[3] * d[0] : 0.f;
    }
  }
  for (unsigned spins = 0; spins < SPIN_CAP; ++spins) {
    const bool ok = (a0[1] == T) & (a0[3] == T) & (a1[1] == T) & (a1[3] == T) & (am[1] == T) & (am[3] == T);
    if (__all(ok)) break;
    asm volatile("" ::: "memory");
    if (!ok) {
      a0 = __builtin_amdgcn_raw_buffer_load_b128(rs, p2off, 0, AUX_SC1);
      a1 = __builtin_amdgcn_raw_buffer_load_b128(rs, p2off + 16, 0, AUX_SC1);
      am = __builtin_amdgcn_raw_buffer_load_b128(rs, mloff, 0, AUX_SC1);
    }
  }
  {
    const float m = __uint_as_float(am[0]), l = __uint_as_float(am[2]);
    float M = fmaxf(m, dpp<ROR8>(m)); M = x32_max(x16_max(M));
    const float f = __builtin_amdgcn_exp2f((m - M) * LOG2E);
    float v[5] = {f * __uint_as_float(a0[0]), f * __uint_as_float(a0[2]), f * __uint_as_float(a1[0]), f * __uint_as_float(a1[2]), f * l};
#pragma unroll
    for (int i = 0; i < 5; ++i) { v[i] += dpp<ROR8>(v[i]); v[i] = x32_sum(x16_sum(v[i])); }
    const float inv = 1.0f / v[4];
    if (lane < 8) *(v2u*)((u16*)(lds + L_OB) + w * 32 + 4 * lane) = v2u{pack2(v[0] * inv, v[1] * inv), pack2(v[2] * inv, v[3] * inv)};
  }
  BAR();
  {
    const v4u oa = *(const v4u*)((const u16*)(lds + L_OB) + (c >> 2) * 32 + 8 * g);
    float val[4];
#pragma unroll
    for (int i = 0; i < 4; ++i) {
      const f32x4 d = mfma32(oa, wof[i], f32x4{0.f, 0.f, 0.f, 0.f});
      val[i] = fmaf(stg[2], d[0], accm[i]);
    }
    const unsigned r = g * 64 + 32 * cg + 8 * w + (c >> 1);
    const unsigned off = WS_P3 + (r * 128 + kg) * 64 + (c & 1) * 16;
    const v4u s0 = {__float_as_uint(val[0]), T, __float_as_uint(val[1]), T};
    const v4u s1 = {__float_as_uint(val[2]), T, __float_as_uint(val[3]), T};
    __builtin_amdgcn_raw_buffer_store_b128(s0, rs, off, 0, AUX_SC1);
    __builtin_amdgcn_raw_buffer_store_b128(s1, rs, off + 32, 0, AUX_SC1);
  }
  if (w == 0) {
    const int ob = bid >> 6, oc = 8 * (bid & 63);
    const int slot = lane & 3, col = oc + ((slot & 1) << 2) + (slot & 2);
    const unsigned b2v = ldg<unsigned>(b2 + col);
    const float omg = lds[L_ST + 4 * ob + 3];
    const unsigned off = WS_P3 + bid * 8192 + lane * 16;
    v4u pv[8];
#pragma unroll
    for (int i = 0; i < 8; ++i) pv[i] = __builtin_amdgcn_raw_buffer_load_b128(rs, off + i * 1024, 0, AUX_SC1);
    for (unsigned spins = 0; spins < SPIN_CAP; ++spins) {
      bool okall = true;
#pragma unroll
      for (int i = 0; i < 8; ++i) okall &= (pv[i][1] == T) & (pv[i][3] == T);
      if (__all(okall)) break;
      asm volatile("" ::: "memory");
#pragma unroll
      for (int i = 0; i < 8; ++i)
        if (!((pv[i][1] == T) & (pv[i][3] == T))) pv[i] = __builtin_amdgcn_raw_buffer_load_b128(rs, off + i * 1024, 0, AUX_SC1);
    }
    float sa = 0.f, sb = 0.f;
#pragma unroll
    for (int i = 0; i < 8; ++i) { sa += __uint_as_float(pv[i][0]); sb += __uint_as_float(pv[i][2]); }
    sa += dpp<ROR4>(sa); sa += dpp<ROR8>(sa); sa = x32_sum(x16_sum(sa));
    sb += dpp<ROR4>(sb); sb += dpp<ROR8>(sb); sb = x32_sum(x16_sum(sb));
    if (lane < 4) {
      const float o0 = fmaf(omg, lo2f(b2v), sa), o1 = fmaf(omg, hi2f(b2v), sb);
      *(GAS unsigned*)(out + ob * DM + col) = pack2(o0, o1);
    }
    if (bid == 0 && lane == 0) __hip_atomic_store((gu32*)ws, T, __ATOMIC_RELAXED, __HIP_MEMORY_SCOPE_AGENT);
  }
}

extern "C" void kernel_launch(void* const* d_in, const int* in_sizes, int n_in,
                              void* d_out, int out_size, void* d_ws, size_t ws_size,
                              hipStream_t stream) {
  (void)in_sizes; (void)n_in; (void)out_size; (void)ws_size;
  fused_kernel<<<256, 256, 0, stream>>>(
      (const u16*)d_in[0], (const u16*)d_in[1], (const u16*)d_in[2], (const u16*)d_in[3], (const u16*)d_in[4],
      (const u16*)d_in[6], (const u16*)d_in[7], (const u16*)d_in[8], (const u16*)d_in[9], (const u16*)d_in[10],
      (u16*)d_out, (unsigned char*)d_ws);
}
```

```cpp
#include <hip/hip_runtime.h>
#include <hip/hip_bf16.h>
#include <stdint.h>

#define GAS __attribute__((address_space(1)))
#define LAS __attribute__((address_space(3)))
typedef unsigned short u16;
typedef unsigned v4u __attribute__((ext_vector_type(4)));
typedef unsigned v2u __attribute__((ext_vector_type(2)));
typedef float f32x4 __attribute__((ext_vector_type(4)));
typedef float f32x2 __attribute__((ext_vector_type(2)));
typedef __bf16 bf16x8 __attribute__((ext_vector_type(8)));
typedef short s16x4 __attribute__((ext_vector_type(4)));
typedef GAS unsigned gu32;

constexpr int DM = 512, SK = 4096, DH = 128, WINDOW = 1024, FF = 1792, NQ = 4096;
constexpr unsigned WS_QG = 4096;
constexpr unsigned WS_P2 = WS_QG + 65536;
constexpr unsigned WS_ML = WS_P2 + (1u << 20);
constexpr unsigned WS_P3 = WS_ML + 16384;
constexpr unsigned WS_END = WS_P3 + (2u << 20);
constexpr unsigned SPIN_CAP = 1u << 17;
constexpr int AUX_SC1 = 16, AUX_NT = 2;
constexpr float LOG2E = 1.4426950408889634f;

__device__ __forceinline__ float bfr(float f) { return (float)(__bf16)f; }
__device__ __forceinline__ unsigned bfbits(float f) { return (unsigned)__builtin_bit_cast(u16, (__bf16)f); }
__device__ __forceinline__ unsigned pack2(float lo, float hi) { return bfbits(lo) | (bfbits(hi) << 16); }
__device__ __forceinline__ float lo2f(unsigned u) { return __builtin_bit_cast(float, u << 16); }
__device__ __forceinline__ float hi2f(unsigned u) { return __builtin_bit_cast(float, u & 0xffff0000u); }
__device__ __forceinline__ unsigned perm_lo(unsigned a, unsigned b) { return __builtin_amdgcn_perm(b, a, 0x05040100u); }
__device__ __forceinline__ unsigned perm_hi(unsigned a, unsigned b) { return __builtin_amdgcn_perm(b, a, 0x07060302u); }

template <int CTRL> __device__ __forceinline__ float dpp(float x) {
  return __builtin_bit_cast(float, __builtin_amdgcn_mov_dpp(__builtin_bit_cast(int, x), CTRL, 0xf, 0xf, true));
}
constexpr int XOR1 = 0xB1, XOR2 = 0x4E, ROR8 = 0x128, ROR4 = 0x124;
__device__ __forceinline__ float x16_max(float x) {
  auto s = __builtin_amdgcn_permlane16_swap(__float_as_uint(x), __float_as_uint(x), false, false);
  return fmaxf(__uint_as_float(s[0]), __uint_as_float(s[1]));
}
__device__ __forceinline__ float x32_max(float x) {
  auto s = __builtin_amdgcn_permlane32_swap(__float_as_uint(x), __float_as_uint(x), false, false);
  return fmaxf(__uint_as_float(s[0]), __uint_as_float(s[1]));
}
__device__ __forceinline__ float x16_sum(float x) {
  auto s = __builtin_amdgcn_permlane16_swap(__float_as_uint(x), __float_as_uint(x), false, false);
  return __uint_as_float(s[0]) + __uint_as_float(s[1]);
}
__device__ __forceinline__ float x32_sum(float x) {
  auto s = __builtin_amdgcn_permlane32_swap(__float_as_uint(x), __float_as_uint(x), false, false);
  return __uint_as_float(s[0]) + __uint_as_float(s[1]);
}
__device__ __forceinline__ float wave_sum(float x) {
  x += dpp<XOR1>(x); x += dpp<XOR2>(x); x += dpp<ROR4>(x); x += dpp<ROR8>(x);
  return x32_sum(x16_sum(x));
}
__device__ __forceinline__ f32x4 mfma32(v4u a, v4u b, f32x4 c) {
  return __builtin_amdgcn_mfma_f32_16x16x32_bf16(__builtin_bit_cast(bf16x8, a), __builtin_bit_cast(bf16x8, b), c, 0, 0, 0);
}
__device__ __forceinline__ f32x4 mfma16(v2u a, v2u b, f32x4 c) {
  return __builtin_amdgcn_mfma_f32_16x16x16bf16_1k(__builtin_bit_cast(s16x4, a), __builtin_bit_cast(s16x4, b), c, 0, 0, 0);
}
#define BAR() do { asm volatile("s_waitcnt lgkmcnt(0)" ::: "memory"); __builtin_amdgcn_s_barrier(); asm volatile("" ::: "memory"); } while (0)
#define PIN() __builtin_amdgcn_sched_barrier(0)
#define STAMP(i)

template <typename T> __device__ __forceinline__ T ldg(const u16* p) { return *(const GAS T*)p; }
template <typename T> __device__ __forceinline__ T ldnt(const u16* p) { return __builtin_nontemporal_load((const GAS T*)p); }

constexpr int L_ST = 0;
constexpr int L_CNT = 16;
constexpr int L_RQ = 32;
constexpr int L_RH = L_RQ + 1024;
constexpr int L_HB = L_RH + 1024;
constexpr int L_MO = L_HB + 32;
constexpr int L_ML = L_MO + 2048;
constexpr int L_OB = L_ML + 32;
constexpr int L_END = L_OB + 64;

__device__ __forceinline__ v4u tr8x4(const v2u (&W)[8], int i) {
  v4u f;
#pragma unroll
  for (int jj = 0; jj < 4; ++jj)
    f[jj] = (i & 1) ? perm_hi(W[2 * jj][i >> 1], W[2 * jj + 1][i >> 1]) : perm_lo(W[2 * jj][i >> 1], W[2 * jj + 1][i >> 1]);
  return f;
}

__global__ void __launch_bounds__(512, 2)
fused_kernel(const u16* __restrict__ x, const u16* __restrict__ kcache, const u16* __restrict__ vcache,
             const u16* __restrict__ wq, const u16* __restrict__ wo, const u16* __restrict__ w1,
             const u16* __restrict__ b1, const u16* __restrict__ w2, const u16* __restrict__ b2,
             const u16* __restrict__ gw, u16* __restrict__ out, unsigned char* ws) {
  __shared__ __attribute__((aligned(16))) float lds[L_END];
  const int tid = threadIdx.x, lane = tid & 63, w = __builtin_amdgcn_readfirstlane(tid >> 6);
  const int g = lane >> 4, c = lane & 15;
  const int bid = blockIdx.x, hk = bid & 7, idx = bid >> 3;
  const int ab = idx >> 3, as = idx & 7;
  const int kgl = idx >> 1, cg = idx & 1, kg = hk * 16 + kgl;
  const __amdgpu_buffer_rsrc_t rs = __builtin_amdgcn_make_buffer_rsrc((void*)ws, 0, (int)WS_END, 0x00020000);
  LAS unsigned* cnt = (LAS unsigned*)(lds + L_CNT);
  STAMP(0);
  const unsigned T = *(const GAS unsigned*)ws + 1u;

  if (w < 4) {
    BAR();
    v4u kf[8], vf[8];
    {
      const size_t kvoff = ((size_t)(ab * 8 + hk) * SK + (SK - WINDOW) + 128 * as + 32 * w) * DH;
      const u16* kp = kcache + kvoff + c * DH + 8 * g;
#pragma unroll
      for (int t = 0; t < 2; ++t)
#pragma unroll
        for (int ks = 0; ks < 4; ++ks) kf[t * 4 + ks] = ldnt<v4u>(kp + t * 16 * DH + 32 * ks);
      const u16* vp = vcache + kvoff + 4 * g * DH + 8 * c;
#pragma unroll
      for (int j = 0; j < 8; ++j) vf[j] = ldnt<v4u>(vp + (16 * (j >> 2) + (j & 3)) * DH);
    }
    PIN();
    STAMP(1);
    BAR();
    STAMP(2);
    asm volatile("" :: "v"(kf[7]));
    PIN();
    STAMP(3);
    v4u qraw[8];
    const unsigned qoff = WS_QG + (ab * 1024 + hk * 128 + (c >> 2) * 32 + 2 * g) * 16;
#pragma unroll
    for (int ks = 0; ks < 4; ++ks) {
      qraw[2 * ks] = __builtin_amdgcn_raw_buffer_load_b128(rs, qoff + ks * 128, 0, AUX_SC1);
      qraw[2 * ks + 1] = __builtin_amdgcn_raw_buffer_load_b128(rs, qoff + ks * 128 + 16, 0, AUX_SC1);
    }
    PIN();
    v4u vt[8];
#pragma unroll
    for (int i = 0; i < 8; ++i)
#pragma unroll
      for (int jj = 0; jj < 4; ++jj)
        vt[i][jj] = (i & 1) ? perm_hi(vf[2 * jj][i >> 1], vf[2 * jj + 1][i >> 1]) : perm_lo(vf[2 * jj][i >> 1], vf[2 * jj + 1][i >> 1]);
    PIN();
    for (unsigned spins = 0; spins < SPIN_CAP; ++spins) {
      unsigned bad = 0;
#pragma unroll
      for (int i = 0; i < 8; ++i) bad |= (qraw[i][1] ^ T) | (qraw[i][3] ^ T);
      if (__all(bad == 0)) break;
      asm volatile("" ::: "memory");
#pragma unroll
      for (int ks = 0; ks < 4; ++ks) {
        qraw[2 * ks] = __builtin_amdgcn_raw_buffer_load_b128(rs, qoff + ks * 128, 0, AUX_SC1);
        qraw[2 * ks + 1] = __builtin_amdgcn_raw_buffer_load_b128(rs, qoff + ks * 128 + 16, 0, AUX_SC1);
      }
    }
    STAMP(4);
    {
      f32x4 s0 = {0.f, 0.f, 0.f, 0.f}, s1 = {0.f, 0.f, 0.f, 0.f};
#pragma unroll
      for (int ks = 0; ks < 4; ++ks) {
        const v4u qf = {qraw[2 * ks][0], qraw[2 * ks][2], qraw[2 * ks + 1][0], qraw[2 * ks + 1][2]};
        s0 = mfma32(kf[ks], qf, s0);
        s1 = mfma32(kf[4 + ks], qf, s1);
      }
      float sc[8];
#pragma unroll
      for (int j = 0; j < 4; ++j) { sc[j] = bfr(bfr(s0[j]) * 0.08838834764831845f); sc[4 + j] = bfr(bfr(s1[j]) * 0.08838834764831845f); }
      float m = fmaxf(fmaxf(fmaxf(sc[0], sc[1]), fmaxf(sc[2], sc[3])), fmaxf(fmaxf(sc[4], sc[5]), fmaxf(sc[6], sc[7])));
      m = x32_max(x16_max(m));
      float p[8], l = 0.f;
#pragma unroll
      for (int j = 0; j < 8; ++j) { p[j] = __builtin_amdgcn_exp2f((sc[j] - m) * LOG2E); l += p[j]; }
      const v4u pf = {pack2(p[0], p[1]), pack2(p[2], p[3]), pack2(p[4], p[5]), pack2(p[6], p[7])};
      float ov[8];
#pragma unroll
      for (int i = 0; i < 8; ++i) { const f32x4 o = mfma32(pf, vt[i], f32x4{0.f, 0.f, 0.f, 0.f}); ov[i] = o[0]; }
      l = x32_sum(x16_sum(l));
      if (g == 0 && (c & 3) == 0) *(f32x2*)(lds + L_ML + (w * 4 + (c >> 2)) * 2) = f32x2{m, l};
      float* mo = lds + L_MO + (w * 4 + g) * 128 + 8 * c;
      *(f32x4*)(mo) = f32x4{ov[0], ov[1], ov[2], ov[3]};
      *(f32x4*)(mo + 4) = f32x4{ov[4], ov[5], ov[6], ov[7]};
    }
    BAR();
    STAMP(5);
    {
      f32x2 ml[4], ov[4];
#pragma unroll
      for (int u = 0; u < 4; ++u) { ml[u] = *(const f32x2*)(lds + L_ML + (u * 4 + w) * 2); ov[u] = *(const f32x2*)(lds + L_MO + (u * 4 + w) * 128 + 2 * lane); }
      const float M = fmaxf(fmaxf(ml[0][0], ml[1][0]), fmaxf(ml[2][0], ml[3][0]));
      float L = 0.f, o0 = 0.f, o1 = 0.f;
#pragma unroll
      for (int u = 0; u < 4; ++u) {
        const float f = __builtin_amdgcn_exp2f((ml[u][0] - M) * LOG2E);
        L = fmaf(f, ml[u][1], L); o0 = fmaf(f, ov[u][0], o0); o1 = fmaf(f, ov[u][1], o1);
      }
      const int hg = 4 * hk + w;
      const v4u pv = {__float_as_uint(o0), T, __float_as_uint(o1), T};
      __builtin_amdgcn_raw_buffer_store_b128(pv, rs, WS_P2 + (((hg * 4 + (lane >> 4)) * 4 + ab) * 8 + as) * 256 + (lane & 15) * 16, 0, AUX_SC1);
      if (lane == 0) {
        const v4u mv = {__float_as_uint(M), T, __float_as_uint(L), T};
        __builtin_amdgcn_raw_buffer_store_b128(mv, rs, WS_ML + ((hg * 4 + ab) * 8 + as) * 16, 0, AUX_SC1);
      }
    }
    STAMP(6);
    unsigned b2v = 0;
    const int ob = bid >> 6, oc = 8 * (bid & 63);
    const int slot = lane & 3, col = oc + ((slot & 1) << 2) + (slot & 2);
    if (w == 0) b2v = ldg<unsigned>(b2 + col);
    BAR();
    if (w == 0) {
      const float omg = lds[L_ST + 4 * ob + 3];
      const unsigned off = WS_P3 + bid * 8192 + lane * 16;
      v4u pv[8];
#pragma unroll
      for (int i = 0; i < 8; ++i) pv[i] = __builtin_amdgcn_raw_buffer_load_b128(rs, off + i * 1024, 0, AUX_SC1);
      for (unsigned spins = 0; spins < SPIN_CAP; ++spins) {
        unsigned bad = 0;
#pragma unroll
        for (int i = 0; i < 8; ++i) bad |= (pv[i][1] ^ T) | (pv[i][3] ^ T);
        if (__all(bad == 0)) break;
        asm volatile("" ::: "memory");
#pragma unroll
        for (int i = 0; i < 8; ++i)
          if (((pv[i][1] ^ T) | (pv[i][3] ^ T)) != 0) pv[i] = __builtin_amdgcn_raw_buffer_load_b128(rs, off + i * 1024, 0, AUX_SC1);
      }
      STAMP(9);
      float sa = 0.f, sb = 0.f;
#pragma unroll
      for (int i = 0; i < 8; ++i) { sa += __uint_as_float(pv[i][0]); sb += __uint_as_float(pv[i][2]); }
      sa += dpp<ROR4>(sa); sa += dpp<ROR8>(sa); sa = x32_sum(x16_sum(sa));
      sb += dpp<ROR4>(sb); sb += dpp<ROR8>(sb); sb = x32_sum(x16_sum(sb));
      if (lane < 4) {
        const float o0 = fmaf(omg, lo2f(b2v), sa), o1 = fmaf(omg, hi2f(b2v), sb);
        *(GAS unsigned*)(out + ob * DM + col) = pack2(o0, o1);
      }
      if (bid == 0 && lane == 0) __hip_atomic_store((gu32*)ws, T, __ATOMIC_RELAXED, __HIP_MEMORY_SCOPE_AGENT);
    }
  } else {
    const int pw = w - 4;
    const bool has_mlp = kgl < 14;
    const int f0 = 224 * hk + 16 * kgl;
    const int kc = c >> 2, ch = c & 3;
    const int krow = 128 * pw + 32 * kc + 8 * g;
    v2u wv[8];
    {
      const u16* p = wq + (size_t)krow * NQ + hk * 512 + idx * 16 + 4 * ch;
#pragma unroll
      for (int j = 0; j < 8; ++j) wv[j] = ldnt<v2u>(p + j * NQ);
    }
    const v4u xa = ldg<v4u>(x + (c & 3) * DM + krow);
    const v4u xs = ldg<v4u>(x + pw * DM + 8 * lane);
    const v4u gwv = ldg<v4u>(gw + 8 * lane);
    if (tid == 256) { cnt[0] = 0u; cnt[1] = 0u; }
    PIN();
    BAR();
    {
      f32x4 d[4];
#pragma unroll
      for (int i = 0; i < 4; ++i) d[i] = mfma32(xa, tr8x4(wv, i), f32x4{0.f, 0.f, 0.f, 0.f});
      if (g == kc) {
#pragma unroll
        for (int r = 0; r < 4; ++r)
          *(f32x4*)(lds + L_RQ + ((pw * 4 + kc) * 4 + r) * 16 + 4 * ch) = f32x4{d[0][r], d[1][r], d[2][r], d[3][r]};
      }
      asm volatile("s_waitcnt lgkmcnt(0)" ::: "memory");
      unsigned old = 0;
      if (lane == 0) old = __hip_atomic_fetch_add(cnt, 1u, __ATOMIC_RELAXED, __HIP_MEMORY_SCOPE_WORKGROUP);
      old = __builtin_amdgcn_readfirstlane(old);
      asm volatile("" ::: "memory");
      if (old == 3u) {
        const float* red = lds + L_RQ + (g * 16 + (c >> 2)) * 16 + 4 * ch;
        f32x4 a = *(const f32x4*)(red);
#pragma unroll
        for (int p = 1; p < 4; ++p) a += *(const f32x4*)(red + p * 64);
#pragma unroll
        for (int i = 0; i < 4; ++i) a[i] = x32_sum(x16_sum(a[i]));
        if (g == 0) {
          const v4u qv = {pack2(a[0], a[1]), T, pack2(a[2], a[3]), T};
          __builtin_amdgcn_raw_buffer_store_b128(qv, rs, WS_QG + ((c >> 2) * 1024 + hk * 128 + idx * 4 + ch) * 16, 0, AUX_SC1);
        }
      }
    }
    STAMP(1);
    {
      float xv[8], gv[8];
#pragma unroll
      for (int k = 0; k < 4; ++k) { xv[2 * k] = lo2f(xs[k]); xv[2 * k + 1] = hi2f(xs[k]); gv[2 * k] = lo2f(gwv[k]); gv[2 * k + 1] = hi2f(gwv[k]); }
      float s = 0.f, gs = 0.f;
#pragma unroll
      for (int k = 0; k < 8; ++k) { s += xv[k]; gs += bfr(xv[k] * gv[k]); }
      s = wave_sum(s); gs = wave_sum(gs);
      const float mean = s * (1.0f / DM);
      float vs = 0.f;
#pragma unroll
      for (int k = 0; k < 8; ++k) { const float d = xv[k] - mean; vs = fmaf(d, d, vs); }
      vs = wave_sum(vs);
      if (lane == 0) {
        const float var_b = bfr(vs * (1.0f / DM));
        const float den = bfr(sqrtf(bfr(var_b + 1e-5f)));
        const float gt = bfr(1.0f / (1.0f + __expf(-bfr(gs))));
        *(f32x4*)(lds + L_ST + 4 * pw) = f32x4{bfr(mean), den, gt, bfr(1.0f - gt)};
      }
    }
    BAR();
    STAMP(2);
    const __amdgpu_buffer_rsrc_t rw1 = __builtin_amdgcn_make_buffer_rsrc((void*)w1, 0, has_mlp ? DM * FF * 2 : 0, 0x00020000);
    const __amdgpu_buffer_rsrc_t rb1 = __builtin_amdgcn_make_buffer_rsrc((void*)b1, 0, has_mlp ? FF * 2 : 0, 0x00020000);
    const __amdgpu_buffer_rsrc_t rw2 = __builtin_amdgcn_make_buffer_rsrc((void*)w2, 0, has_mlp ? DM * FF * 2 : 0, 0x00020000);
    {
      const int o = (krow * FF + f0 + 4 * ch) * 2;
#pragma unroll
      for (int j = 0; j < 8; ++j) wv[j] = __builtin_amdgcn_raw_buffer_load_b64(rw1, o + j * FF * 2, 0, AUX_NT);
    }
    const v2u b1v = __builtin_amdgcn_raw_buffer_load_b64(rb1, (f0 + 4 * ch) * 2, 0, 0);
    v2u wov[8], w2v[4];
    {
      const u16* p = wo + (size_t)(32 * kg + 8 * g) * DM + 256 * cg + 64 * pw + 4 * c;
#pragma unroll
      for (int j = 0; j < 8; ++j) wov[j] = ldnt<v2u>(p + j * DM);
      const int o2 = ((f0 + 4 * g) * DM + 256 * cg + 64 * pw + 4 * c) * 2;
#pragma unroll
      for (int j = 0; j < 4; ++j) w2v[j] = __builtin_amdgcn_raw_buffer_load_b64(rw2, o2 + j * DM * 2, 0, AUX_NT);
    }
    PIN();
    v4u ya;
    {
      const f32x4 st = *(const f32x4*)(lds + L_ST + 4 * (c & 3));
#pragma unroll
      for (int k = 0; k < 4; ++k) {
        const float y0 = bfr(bfr(lo2f(xa[k]) - st[0]) / st[1]), y1 = bfr(bfr(hi2f(xa[k]) - st[0]) / st[1]);
        ya[k] = pack2(y0, y1);
      }
    }
    const f32x4 stg = *(const f32x4*)(lds + L_ST + 4 * g);
    STAMP(3);
    BAR();
    STAMP(5);
    {
      f32x4 d[4];
#pragma unroll
      for (int i = 0; i < 4; ++i) d[i] = mfma32(ya, tr8x4(wv, i), f32x4{0.f, 0.f, 0.f, 0.f});
      if (g == kc) {
#pragma unroll
        for (int r = 0; r < 4; ++r)
          *(f32x4*)(lds + L_RH + ((pw * 4 + kc) * 4 + r) * 16 + 4 * ch) = f32x4{d[0][r], d[1][r], d[2][r], d[3][r]};
      }
      asm volatile("s_waitcnt lgkmcnt(0)" ::: "memory");
      unsigned old = 0;
      if (lane == 0) old = __hip_atomic_fetch_add(cnt + 1, 1u, __ATOMIC_RELAXED, __HIP_MEMORY_SCOPE_WORKGROUP);
      old = __builtin_amdgcn_readfirstlane(old);
      asm volatile("" ::: "memory");
      if (old == 3u) {
        const float* red = lds + L_RH + (g * 16 + (c >> 2)) * 16 + 4 * ch;
        f32x4 a = *(const f32x4*)(red);
#pragma unroll
        for (int p = 1; p < 4; ++p) a += *(const f32x4*)(red + p * 64);
#pragma unroll
        for (int i = 0; i < 4; ++i) a[i] = x32_sum(x16_sum(a[i]));
        const float bb[4] = {lo2f(b1v[0]), hi2f(b1v[0]), lo2f(b1v[1]), hi2f(b1v[1])};
        const float av = g == 0 ? a[0] : g == 1 ? a[1] : g == 2 ? a[2] : a[3];
        const float bv = g == 0 ? bb[0] : g == 1 ? bb[1] : g == 2 ? bb[2] : bb[3];
        const float u = bfr(bfr(av) + bv);
        const float inner = 0.7978845608028654f * (u + 0.044715f * u * u * u);
        const float hv = 0.5f * u * (1.0f + tanhf(inner));
        ((u16*)(lds + L_HB))[(c >> 2) * 16 + 4 * ch + g] = (u16)bfbits(hv);
      }
    }
    const int h2 = 4 * hk + (kgl >> 2);
    const unsigned p2off = WS_P2 + (((h2 * 4 + (kgl & 3)) * 4 + pw) * 8 + (lane >> 3)) * 256 + (lane & 7) * 32;
    const unsigned mloff = WS_ML + ((h2 * 4 + pw) * 8 + (lane >> 3)) * 16;
    v4u wof[4];
    v2u w2f[4];
#pragma unroll
    for (int i = 0; i < 4; ++i) {
      wof[i] = tr8x4(wov, i);
#pragma unroll
      for (int jj = 0; jj < 2; ++jj)
        w2f[i][jj] = (i & 1) ? perm_hi(w2v[2 * jj][i >> 1], w2v[2 * jj + 1][i >> 1]) : perm_lo(w2v[2 * jj][i >> 1], w2v[2 * jj + 1][i >> 1]);
    }
    PIN();
    STAMP(6);
    v4u a0 = __builtin_amdgcn_raw_buffer_load_b128(rs, p2off, 0, AUX_SC1);
    v4u a1 = __builtin_amdgcn_raw_buffer_load_b128(rs, p2off + 16, 0, AUX_SC1);
    v4u am = __builtin_amdgcn_raw_buffer_load_b128(rs, mloff, 0, AUX_SC1);
    for (unsigned spins = 0; spins < SPIN_CAP; ++spins) {
      const bool ok = ((a0[1] ^ T) | (a0[3] ^ T) | (a1[1] ^ T) | (a1[3] ^ T) | (am[1] ^ T) | (am[3] ^ T)) == 0;
      if (__all(ok)) break;
      asm volatile("" ::: "memory");
      if (!ok) {
        a0 = __builtin_amdgcn_raw_buffer_load_b128(rs, p2off, 0, AUX_SC1);
        a1 = __builtin_amdgcn_raw_buffer_load_b128(rs, p2off + 16, 0, AUX_SC1);
        am = __builtin_amdgcn_raw_buffer_load_b128(rs, mloff, 0, AUX_SC1);
      }
    }
    STAMP(7);
    {
      const float m = __uint_as_float(am[0]), l = __uint_as_float(am[2]);
      float M = fmaxf(m, dpp<ROR8>(m)); M = x32_max(x16_max(M));
      const float f = __builtin_amdgcn_exp2f((m - M) * LOG2E);
      float v[5] = {f * __uint_as_float(a0[0]), f * __uint_as_float(a0[2]), f * __uint_as_float(a1[0]), f * __uint_as_float(a1[2]), f * l};
#pragma unroll
      for (int i = 0; i < 5; ++i) { v[i] += dpp<ROR8>(v[i]); v[i] = x32_sum(x16_sum(v[i])); }
      const float inv = 1.0f / v[4];
      if (lane < 8) *(v2u*)((u16*)(lds + L_OB) + pw * 32 + 4 * lane) = v2u{pack2(v[0] * inv, v[1] * inv), pack2(v[2] * inv, v[3] * inv)};
    }
    BAR();
    {
      const v4u oa = *(const v4u*)((const u16*)(lds + L_OB) + (c >> 2) * 32 + 8 * g);
      const v2u ha = *(const v2u*)((const u16*)(lds + L_HB) + (c >> 2) * 16 + 4 * g);
      float val[4];
#pragma unroll
      for (int i = 0; i < 4; ++i) {
        const f32x4 d = mfma32(oa, wof[i], f32x4{0.f, 0.f, 0.f, 0.f});
        const f32x4 e = mfma16(ha, w2f[i], f32x4{0.f, 0.f, 0.f, 0.f});
        val[i] = fmaf(stg[2], d[0], stg[3] * e[0]);
      }
      const unsigned r = g * 64 + 32 * cg + 8 * pw + (c >> 1);
      const unsigned off = WS_P3 + (r * 128 + kg) * 64 + (c & 1) * 16;
      const v4u s0 = {__float_as_uint(val[0]), T, __float_as_uint(val[1]), T};
      const v4u s1 = {__float_as_uint(val[2]), T, __float_as_uint(val[3]), T};
      __builtin_amdgcn_raw_buffer_store_b128(s0, rs, off, 0, AUX_SC1);
      __builtin_amdgcn_raw_buffer_store_b128(s1, rs, off + 32, 0, AUX_SC1);
    }
    STAMP(8);
  }
}

extern "C" void kernel_launch(void* const* d_in, const int* in_sizes, int n_in,
                              void* d_out, int out_size, void* d_ws, size_t ws_size,
                              hipStream_t stream) {
  (void)in_sizes; (void)n_in; (void)out_size; (void)ws_size;
  fused_kernel<<<256, 512, 0, stream>>>(
      (const u16*)d_in[0], (const u16*)d_in[1], (const u16*)d_in[2], (const u16*)d_in[3], (const u16*)d_in[4],
      (const u16*)d_in[6], (const u16*)d_in[7], (const u16*)d_in[8], (const u16*)d_in[9], (const u16*)d_in[10],
      (u16*)d_out, (unsigned char*)d_ws);
}
```

```cpp
#include <hip/hip_runtime.h>
#include <hip/hip_bf16.h>
#include <stdint.h>
#include <stdio.h>

#define GAS __attribute__((address_space(1)))
#define LAS __attribute__((address_space(3)))
typedef unsigned short u16;
typedef unsigned v4u __attribute__((ext_vector_type(4)));
typedef unsigned v2u __attribute__((ext_vector_type(2)));
typedef float f32x4 __attribute__((ext_vector_type(4)));
typedef float f32x2 __attribute__((ext_vector_type(2)));
typedef __bf16 bf16x8 __attribute__((ext_vector_type(8)));
typedef short s16x4 __attribute__((ext_vector_type(4)));
typedef GAS unsigned gu32;

constexpr int DM = 512, SK = 4096, DH = 128, WINDOW = 1024, FF = 1792, NQ = 4096;
constexpr unsigned WS_QG = 4096;
constexpr unsigned WS_P2 = WS_QG + 65536;
constexpr unsigned WS_ML = WS_P2 + (1u << 20);
constexpr unsigned WS_P3 = WS_ML + 16384;
constexpr unsigned WS_END = WS_P3 + (2u << 20);
constexpr unsigned SPIN_CAP = 1u << 17;
constexpr int AUX_SC1 = 16, AUX_NT = 2;
constexpr float LOG2E = 1.4426950408889634f;

__device__ __forceinline__ float bfr(float f) { return (float)(__bf16)f; }
__device__ __forceinline__ unsigned bfbits(float f) { return (unsigned)__builtin_bit_cast(u16, (__bf16)f); }
__device__ __forceinline__ unsigned pack2(float lo, float hi) { return bfbits(lo) | (bfbits(hi) << 16); }
__device__ __forceinline__ float lo2f(unsigned u) { return __builtin_bit_cast(float, u << 16); }
__device__ __forceinline__ float hi2f(unsigned u) { return __builtin_bit_cast(float, u & 0xffff0000u); }
__device__ __forceinline__ unsigned perm_lo(unsigned a, unsigned b) { return __builtin_amdgcn_perm(b, a, 0x05040100u); }
__device__ __forceinline__ unsigned perm_hi(unsigned a, unsigned b) { return __builtin_amdgcn_perm(b, a, 0x07060302u); }

template <int CTRL> __device__ __forceinline__ float dpp(float x) {
  return __builtin_bit_cast(float, __builtin_amdgcn_mov_dpp(__builtin_bit_cast(int, x), CTRL, 0xf, 0xf, true));
}
constexpr int XOR1 = 0xB1, XOR2 = 0x4E, ROR8 = 0x128, ROR4 = 0x124;
__device__ __forceinline__ float x16_max(float x) {
  auto s = __builtin_amdgcn_permlane16_swap(__float_as_uint(x), __float_as_uint(x), false, false);
  return fmaxf(__uint_as_float(s[0]), __uint_as_float(s[1]));
}
__device__ __forceinline__ float x32_max(float x) {
  auto s = __builtin_amdgcn_permlane32_swap(__float_as_uint(x), __float_as_uint(x), false, false);
  return fmaxf(__uint_as_float(s[0]), __uint_as_float(s[1]));
}
__device__ __forceinline__ float x16_sum(float x) {
  auto s = __builtin_amdgcn_permlane16_swap(__float_as_uint(x), __float_as_uint(x), false, false);
  return __uint_as_float(s[0]) + __uint_as_float(s[1]);
}
__device__ __forceinline__ float x32_sum(float x) {
  auto s = __builtin_amdgcn_permlane32_swap(__float_as_uint(x), __float_as_uint(x), false, false);
  return __uint_as_float(s[0]) + __uint_as_float(s[1]);
}
__device__ __forceinline__ float wave_sum(float x) {
  x += dpp<XOR1>(x); x += dpp<XOR2>(x); x += dpp<ROR4>(x); x += dpp<ROR8>(x);
  return x32_sum(x16_sum(x));
}
__device__ __forceinline__ f32x4 mfma32(v4u a, v4u b, f32x4 c) {
  return __builtin_amdgcn_mfma_f32_16x16x32_bf16(__builtin_bit_cast(bf16x8, a), __builtin_bit_cast(bf16x8, b), c, 0, 0, 0);
}
__device__ __forceinline__ f32x4 mfma16(v2u a, v2u b, f32x4 c) {
  return __builtin_amdgcn_mfma_f32_16x16x16bf16_1k(__builtin_bit_cast(s16x4, a), __builtin_bit_cast(s16x4, b), c, 0, 0, 0);
}
#define BAR() do { asm volatile("s_waitcnt lgkmcnt(0)" ::: "memory"); __builtin_amdgcn_s_barrier(); asm volatile("" ::: "memory"); } while (0)
#define PIN() __builtin_amdgcn_sched_barrier(0)
#define STAMP(i)

template <typename T> __device__ __forceinline__ T ldg(const u16* p) { return *(const GAS T*)p; }
template <typename T> __device__ __forceinline__ T ldnt(const u16* p) { return __builtin_nontemporal_load((const GAS T*)p); }

constexpr int L_ST = 0;
constexpr int L_CNT = 16;
constexpr int L_RQ = 32;
constexpr int L_RH = L_RQ + 1024;
constexpr int L_HB = L_RH + 1024;
constexpr int L_MO = L_HB + 32;
constexpr int L_ML = L_MO + 2048;
constexpr int L_OB = L_ML + 32;
constexpr int L_END = L_OB + 64;

__device__ __forceinline__ v4u tr8x4(const v2u (&W)[8], int i) {
  v4u f;
#pragma unroll
  for (int jj = 0; jj < 4; ++jj)
    f[jj] = (i & 1) ? perm_hi(W[2 * jj][i >> 1], W[2 * jj + 1][i >> 1]) : perm_lo(W[2 * jj][i >> 1], W[2 * jj + 1][i >> 1]);
  return f;
}

__global__ void __launch_bounds__(512, 2)
fused_kernel(const u16* __restrict__ x, const u16* __restrict__ kcache, const u16* __restrict__ vcache,
             const u16* __restrict__ wq, const u16* __restrict__ wo, const u16* __restrict__ w1,
             const u16* __restrict__ b1, const u16* __restrict__ w2, const u16* __restrict__ b2,
             const u16* __restrict__ gw, u16* __restrict__ out, unsigned char* ws) {
  __shared__ __attribute__((aligned(16))) float lds[L_END];
  const int tid = threadIdx.x, lane = tid & 63, w = __builtin_amdgcn_readfirstlane(tid >> 6);
  const int g = lane >> 4, c = lane & 15;
  const int bid = blockIdx.x, hk = bid & 7, idx = bid >> 3;
  const int ab = idx >> 3, as = idx & 7;
  const int kgl = idx >> 1, cg = idx & 1, kg = hk * 16 + kgl;
  const __amdgpu_buffer_rsrc_t rs = __builtin_amdgcn_make_buffer_rsrc((void*)ws, 0, (int)WS_END, 0x00020000);
  LAS unsigned* cnt = (LAS unsigned*)(lds + L_CNT);
  STAMP(0);
  const unsigned T = *(const GAS unsigned*)ws + 1u;

  if (w < 4) {
    BAR();
    v4u kf[8], vf[8];
    {
      const size_t kvoff = ((size_t)(ab * 8 + hk) * SK + (SK - WINDOW) + 128 * as + 32 * w) * DH;
      const u16* kp = kcache + kvoff + c * DH + 8 * g;
#pragma unroll
      for (int t = 0; t < 2; ++t)
#pragma unroll
        for (int ks = 0; ks < 4; ++ks) kf[t * 4 + ks] = ldnt<v4u>(kp + t * 16 * DH + 32 * ks);
      const u16* vp = vcache + kvoff + 4 * g * DH + 8 * c;
#pragma unroll
      for (int j = 0; j < 8; ++j) vf[j] = ldnt<v4u>(vp + (16 * (j >> 2) + (j & 3)) * DH);
    }
    PIN();
    STAMP(1);
    BAR();
    STAMP(2);
    asm volatile("" :: "v"(kf[7]));
    PIN();
    STAMP(3);
    v4u qraw[8];
    const unsigned qoff = WS_QG + (ab * 1024 + hk * 128 + (c >> 2) * 32 + 2 * g) * 16;
#pragma unroll
    for (int ks = 0; ks < 4; ++ks) {
      qraw[2 * ks] = __builtin_amdgcn_raw_buffer_load_b128(rs, qoff + ks * 128, 0, AUX_SC1);
      qraw[2 * ks + 1] = __builtin_amdgcn_raw_buffer_load_b128(rs, qoff + ks * 128 + 16, 0, AUX_SC1);
    }
    PIN();
    v4u vt[8];
#pragma unroll
    for (int i = 0; i < 8; ++i)
#pragma unroll
      for (int jj = 0; jj < 4; ++jj)
        vt[i][jj] = (i & 1) ? perm_hi(vf[2 * jj][i >> 1], vf[2 * jj + 1][i >> 1]) : perm_lo(vf[2 * jj][i >> 1], vf[2 * jj + 1][i >> 1]);
    PIN();
    for (unsigned spins = 0; spins < SPIN_CAP; ++spins) {
      unsigned bad = 0;
#pragma unroll
      for (int i = 0; i < 8; ++i) bad |= (qraw[i][1] ^ T) | (qraw[i][3] ^ T);
      if (__all(bad == 0)) break;
      asm volatile("" ::: "memory");
#pragma unroll
      for (int ks = 0; ks < 4; ++ks) {
        qraw[2 * ks] = __builtin_amdgcn_raw_buffer_load_b128(rs, qoff + ks * 128, 0, AUX_SC1);
        qraw[2 * ks + 1] = __builtin_amdgcn_raw_buffer_load_b128(rs, qoff + ks * 128 + 16, 0, AUX_SC1);
      }
    }
    STAMP(4);
    {
      f32x4 s0 = {0.f, 0.f, 0.f, 0.f}, s1 = {0.f, 0.f, 0.f, 0.f};
#pragma unroll
      for (int ks = 0; ks < 4; ++ks) {
        const v4u qf = {qraw[2 * ks][0], qraw[2 * ks][2], qraw[2 * ks + 1][0], qraw[2 * ks + 1][2]};
        s0 = mfma32(kf[ks], qf, s0);
        s1 = mfma32(kf[4 + ks], qf, s1);
      }
      float sc[8];
#pragma unroll
      for (int j = 0; j < 4; ++j) { sc[j] = bfr(bfr(s0[j]) * 0.08838834764831845f); sc[4 + j] = bfr(bfr(s1[j]) * 0.08838834764831845f); }
      float m = fmaxf(fmaxf(fmaxf(sc[0], sc[1]), fmaxf(sc[2], sc[3])), fmaxf(fmaxf(sc[4], sc[5]), fmaxf(sc[6], sc[7])));
      m = x32_max(x16_max(m));
      float p[8], l = 0.f;
#pragma unroll
      for (int j = 0; j < 8; ++j) { p[j] = __builtin_amdgcn_exp2f((sc[j] - m) * LOG2E); l += p[j]; }
      const v4u pf = {pack2(p[0], p[1]), pack2(p[2], p[3]), pack2(p[4], p[5]), pack2(p[6], p[7])};
      float ov[8];
#pragma unroll
      for (int i = 0; i < 8; ++i) { const f32x4 o = mfma32(pf, vt[i], f32x4{0.f, 0.f, 0.f, 0.f}); ov[i] = o[0]; }
      l = x32_sum(x16_sum(l));
      if (g == 0 && (c & 3) == 0) *(f32x2*)(lds + L_ML + (w * 4 + (c >> 2)) * 2) = f32x2{m, l};
      float* mo = lds + L_MO + (w * 4 + g) * 128 + 8 * c;
      *(f32x4*)(mo) = f32x4{ov[0], ov[1], ov[2], ov[3]};
      *(f32x4*)(mo + 4) = f32x4{ov[4], ov[5], ov[6], ov[7]};
    }
    BAR();
    STAMP(5);
    {
      f32x2 ml[4], ov[4];
#pragma unroll
      for (int u = 0; u < 4; ++u) { ml[u] = *(const f32x2*)(lds + L_ML + (u * 4 + w) * 2); ov[u] = *(const f32x2*)(lds + L_MO + (u * 4 + w) * 128 + 2 * lane); }
      const float M = fmaxf(fmaxf(ml[0][0], ml[1][0]), fmaxf(ml[2][0], ml[3][0]));
      float L = 0.f, o0 = 0.f, o1 = 0.f;
#pragma unroll
      for (int u = 0; u < 4; ++u) {
        const float f = __builtin_amdgcn_exp2f((ml[u][0] - M) * LOG2E);
        L = fmaf(f, ml[u][1], L); o0 = fmaf(f, ov[u][0], o0); o1 = fmaf(f, ov[u][1], o1);
      }
      const int hg = 4 * hk + w;
      const v4u pv = {__float_as_uint(o0), T, __float_as_uint(o1), T};
      __builtin_amdgcn_raw_buffer_store_b128(pv, rs, WS_P2 + (((hg * 4 + (lane >> 4)) * 4 + ab) * 8 + as) * 256 + (lane & 15) * 16, 0, AUX_SC1);
      if (lane == 0) {
        const v4u mv = {__float_as_uint(M), T, __float_as_uint(L), T};
        __builtin_amdgcn_raw_buffer_store_b128(mv, rs, WS_ML + ((hg * 4 + ab) * 8 + as) * 16, 0, AUX_SC1);
      }
    }
    STAMP(6);
    unsigned b2v = 0;
    const int ob = bid >> 6, oc = 8 * (bid & 63);
    const int slot = lane & 3, col = oc + ((slot & 1) << 2) + (slot & 2);
    if (w == 0) b2v = ldg<unsigned>(b2 + col);
    BAR();
    if (w == 0) {
      const float omg = lds[L_ST + 4 * ob + 3];
      const unsigned off = WS_P3 + bid * 8192 + lane * 16;
      v4u pv[8];
#pragma unroll
      for (int i = 0; i < 8; ++i) pv[i] = __builtin_amdgcn_raw_buffer_load_b128(rs, off + i * 1024, 0, AUX_SC1);
      for (unsigned spins = 0; spins < SPIN_CAP; ++spins) {
        unsigned bad = 0;
#pragma unroll
        for (int i = 0; i < 8; ++i) bad |= (pv[i][1] ^ T) | (pv[i][3] ^ T);
        if (__all(bad == 0)) break;
        asm volatile("" ::: "memory");
#pragma unroll
        for (int i = 0; i < 8; ++i)
          if (((pv[i][1] ^ T) | (pv[i][3] ^ T)) != 0) pv[i] = __builtin_amdgcn_raw_buffer_load_b128(rs, off + i * 1024, 0, AUX_SC1);
      }
      STAMP(9);
      float sa = 0.f, sb = 0.f;
#pragma unroll
      for (int i = 0; i < 8; ++i) { sa += __uint_as_float(pv[i][0]); sb += __uint_as_float(pv[i][2]); }
      sa += dpp<ROR4>(sa); sa += dpp<ROR8>(sa); sa = x32_sum(x16_sum(sa));
      sb += dpp<ROR4>(sb); sb += dpp<ROR8>(sb); sb = x32_sum(x16_sum(sb));
      if (lane < 4) {
        const float o0 = fmaf(omg, lo2f(b2v), sa), o1 = fmaf(omg, hi2f(b2v), sb);
        *(GAS unsigned*)(out + ob * DM + col) = pack2(o0, o1);
      }
      if (bid == 0 && lane == 0) __hip_atomic_store((gu32*)ws, T, __ATOMIC_RELAXED, __HIP_MEMORY_SCOPE_AGENT);
    }
  } else {
    const int pw = w - 4;
    const bool has_mlp = kgl < 14;
    const int f0 = 224 * hk + 16 * kgl;
    const int kc = c >> 2, ch = c & 3;
    const int krow = 128 * pw + 32 * kc + 8 * g;
    v2u wv[8];
    {
      const u16* p = wq + (size_t)krow * NQ + hk * 512 + idx * 16 + 4 * ch;
#pragma unroll
      for (int j = 0; j < 8; ++j) wv[j] = ldnt<v2u>(p + j * NQ);
    }
    const v4u xa = ldg<v4u>(x + (c & 3) * DM + krow);
    const v4u xs = ldg<v4u>(x + pw * DM + 8 * lane);
    const v4u gwv = ldg<v4u>(gw + 8 * lane);
    if (tid == 256) { cnt[0] = 0u; cnt[1] = 0u; }
    PIN();
    BAR();
    {
      f32x4 d[4];
#pragma unroll
      for (int i = 0; i < 4; ++i) d[i] = mfma32(xa, tr8x4(wv, i), f32x4{0.f, 0.f, 0.f, 0.f});
      if (g == kc) {
#pragma unroll
        for (int r = 0; r < 4; ++r)
          *(f32x4*)(lds + L_RQ + ((pw * 4 + kc) * 4 + r) * 16 + 4 * ch) = f32x4{d[0][r], d[1][r], d[2][r], d[3][r]};
      }
      asm volatile("s_waitcnt lgkmcnt(0)" ::: "memory");
      unsigned old = 0;
      if (lane == 0) old = __hip_atomic_fetch_add(cnt, 1u, __ATOMIC_RELAXED, __HIP_MEMORY_SCOPE_WORKGROUP);
      old = __builtin_amdgcn_readfirstlane(old);
      asm volatile("" ::: "memory");
      if (old == 3u) {
        const float* red = lds + L_RQ + (g * 16 + (c >> 2)) * 16 + 4 * ch;
        f32x4 a = *(const f32x4*)(red);
#pragma unroll
        for (int p = 1; p < 4; ++p) a += *(const f32x4*)(red + p * 64);
#pragma unroll
        for (int i = 0; i < 4; ++i) a[i] = x32_sum(x16_sum(a[i]));
        if (g == 0) {
          const v4u qv = {pack2(a[0], a[1]), T, pack2(a[2], a[3]), T};
          __builtin_amdgcn_raw_buffer_store_b128(qv, rs, WS_QG + ((c >> 2) * 1024 + hk * 128 + idx * 4 + ch) * 16, 0, AUX_SC1);
        }
      }
    }
    STAMP(1);
    {
      float xv[8], gv[8];
#pragma unroll
      for (int k = 0; k < 4; ++k) { xv[2 * k] = lo2f(xs[k]); xv[2 * k + 1] = hi2f(xs[k]); gv[2 * k] = lo2f(gwv[k]); gv[2 * k + 1] = hi2f(gwv[k]); }
      float s = 0.f, gs = 0.f;
#pragma unroll
      for (int k = 0; k < 8; ++k) { s += xv[k]; gs += bfr(xv[k] * gv[k]); }
      s = wave_sum(s); gs = wave_sum(gs);
      const float mean = s * (1.0f / DM);
      float vs = 0.f;
#pragma unroll
      for (int k = 0; k < 8; ++k) { const float d = xv[k] - mean; vs = fmaf(d, d, vs); }
      vs = wave_sum(vs);
      if (lane == 0) {
        const float var_b = bfr(vs * (1.0f / DM));
        const float den = bfr(sqrtf(bfr(var_b + 1e-5f)));
        const float gt = bfr(1.0f / (1.0f + __expf(-bfr(gs))));
        *(f32x4*)(lds + L_ST + 4 * pw) = f32x4{bfr(mean), den, gt, bfr(1.0f - gt)};
      }
    }
    BAR();
    STAMP(2);
    const __amdgpu_buffer_rsrc_t rw1 = __builtin_amdgcn_make_buffer_rsrc((void*)w1, 0, has_mlp ? DM * FF * 2 : 0, 0x00020000);
    const __amdgpu_buffer_rsrc_t rb1 = __builtin_amdgcn_make_buffer_rsrc((void*)b1, 0, has_mlp ? FF * 2 : 0, 0x00020000);
    const __amdgpu_buffer_rsrc_t rw2 = __builtin_amdgcn_make_buffer_rsrc((void*)w2, 0, has_mlp ? DM * FF * 2 : 0, 0x00020000);
    {
      const int o = (krow * FF + f0 + 4 * ch) * 2;
#pragma unroll
      for (int j = 0; j < 8; ++j) wv[j] = __builtin_amdgcn_raw_buffer_load_b64(rw1, o + j * FF * 2, 0, AUX_NT);
    }
    const v2u b1v = __builtin_amdgcn_raw_buffer_load_b64(rb1, (f0 + 4 * ch) * 2, 0, 0);
    v2u wov[8], w2v[4];
    {
      const u16* p = wo + (size_t)(32 * kg + 8 * g) * DM + 256 * cg + 64 * pw + 4 * c;
#pragma unroll
      for (int j = 0; j < 8; ++j) wov[j] = ldnt<v2u>(p + j * DM);
      const int o2 = ((f0 + 4 * g) * DM + 256 * cg + 64 * pw + 4 * c) * 2;
#pragma unroll
      for (int j = 0; j < 4; ++j) w2v[j] = __builtin_amdgcn_raw_buffer_load_b64(rw2, o2 + j * DM * 2, 0, AUX_NT);
    }
    PIN();
    v4u ya;
    {
      const f32x4 st = *(const f32x4*)(lds + L_ST + 4 * (c & 3));
#pragma unroll
      for (int k = 0; k < 4; ++k) {
        const float y0 = bfr(bfr(lo2f(xa[k]) - st[0]) / st[1]), y1 = bfr(bfr(hi2f(xa[k]) - st[0]) / st[1]);
        ya[k] = pack2(y0, y1);
      }
    }
    const f32x4 stg = *(const f32x4*)(lds + L_ST + 4 * g);
    STAMP(3);
    BAR();
    STAMP(5);
    {
      f32x4 d[4];
#pragma unroll
      for (int i = 0; i < 4; ++i) d[i] = mfma32(ya, tr8x4(wv, i), f32x4{0.f, 0.f, 0.f, 0.f});
      if (g == kc) {
#pragma unroll
        for (int r = 0; r < 4; ++r)
          *(f32x4*)(lds + L_RH + ((pw * 4 + kc) * 4 + r) * 16 + 4 * ch) = f32x4{d[0][r], d[1][r], d[2][r], d[3][r]};
      }
      asm volatile("s_waitcnt lgkmcnt(0)" ::: "memory");
      unsigned old = 0;
      if (lane == 0) old = __hip_atomic_fetch_add(cnt + 1, 1u, __ATOMIC_RELAXED, __HIP_MEMORY_SCOPE_WORKGROUP);
      old = __builtin_amdgcn_readfirstlane(old);
      asm volatile("" ::: "memory");
      if (old == 3u) {
        const float* red = lds + L_RH + (g * 16 + (c >> 2)) * 16 + 4 * ch;
        f32x4 a = *(const f32x4*)(red);
#pragma unroll
        for (int p = 1; p < 4; ++p) a += *(const f32x4*)(red + p * 64);
#pragma unroll
        for (int i = 0; i < 4; ++i) a[i] = x32_sum(x16_sum(a[i]));
        const float bb[4] = {lo2f(b1v[0]), hi2f(b1v[0]), lo2f(b1v[1]), hi2f(b1v[1])};
        const float av = g == 0 ? a[0] : g == 1 ? a[1] : g == 2 ? a[2] : a[3];
        const float bv = g == 0 ? bb[0] : g == 1 ? bb[1] : g == 2 ? bb[2] : bb[3];
        const float u = bfr(bfr(av) + bv);
        const float inner = 0.7978845608028654f * (u + 0.044715f * u * u * u);
        const float hv = 0.5f * u * (1.0f + tanhf(inner));
        ((u16*)(lds + L_HB))[(c >> 2) * 16 + 4 * ch + g] = (u16)bfbits(hv);
      }
    }
    const int h2 = 4 * hk + (kgl >> 2);
    const unsigned p2off = WS_P2 + (((h2 * 4 + (kgl & 3)) * 4 + pw) * 8 + (lane >> 3)) * 256 + (lane & 7) * 32;
    const unsigned mloff = WS_ML + ((h2 * 4 + pw) * 8 + (lane >> 3)) * 16;
    v4u wof[4];
    v2u w2f[4];
#pragma unroll
    for (int i = 0; i < 4; ++i) {
      wof[i] = tr8x4(wov, i);
#pragma unroll
      for (int jj = 0; jj < 2; ++jj)
        w2f[i][jj] = (i & 1) ? perm_hi(w2v[2 * jj][i >> 1], w2v[2 * jj + 1][i >> 1]) : perm_lo(w2v[2 * jj][i >> 1], w2v[2 * jj + 1][i >> 1]);
    }
    PIN();
    STAMP(6);
    v4u a0 = __builtin_amdgcn_raw_buffer_load_b128(rs, p2off, 0, AUX_SC1);
    v4u a1 = __builtin_amdgcn_raw_buffer_load_b128(rs, p2off + 16, 0, AUX_SC1);
    v4u am = __builtin_amdgcn_raw_buffer_load_b128(rs, mloff, 0, AUX_SC1);
    for (unsigned spins = 0; spins < SPIN_CAP; ++spins) {
      const bool ok = ((a0[1] ^ T) | (a0[3] ^ T) | (a1[1] ^ T) | (a1[3] ^ T) | (am[1] ^ T) | (am[3] ^ T)) == 0;
      if (__all(ok)) break;
      asm volatile("" ::: "memory");
      if (!ok) {
        a0 = __builtin_amdgcn_raw_buffer_load_b128(rs, p2off, 0, AUX_SC1);
        a1 = __builtin_amdgcn_raw_buffer_load_b128(rs, p2off + 16, 0, AUX_SC1);
        am = __builtin_amdgcn_raw_buffer_load_b128(rs, mloff, 0, AUX_SC1);
      }
    }
    STAMP(7);
    {
      const float m = __uint_as_float(am[0]), l = __uint_as_float(am[2]);
      float M = fmaxf(m, dpp<ROR8>(m)); M = x32_max(x16_max(M));
      const float f = __builtin_amdgcn_exp2f((m - M) * LOG2E);
      float v[5] = {f * __uint_as_float(a0[0]), f * __uint_as_float(a0[2]), f * __uint_as_float(a1[0]), f * __uint_as_float(a1[2]), f * l};
#pragma unroll
      for (int i = 0; i < 5; ++i) { v[i] += dpp<ROR8>(v[i]); v[i] = x32_sum(x16_sum(v[i])); }
      const float inv = 1.0f / v[4];
      if (lane < 8) *(v2u*)((u16*)(lds + L_OB) + pw * 32 + 4 * lane) = v2u{pack2(v[0] * inv, v[1] * inv), pack2(v[2] * inv, v[3] * inv)};
    }
    BAR();
    {
      const v4u oa = *(const v4u*)((const u16*)(lds + L_OB) + (c >> 2) * 32 + 8 * g);
      const v2u ha = *(const v2u*)((const u16*)(lds + L_HB) + (c >> 2) * 16 + 4 * g);
      float val[4];
#pragma unroll
      for (int i = 0; i < 4; ++i) {
        const f32x4 d = mfma32(oa, wof[i], f32x4{0.f, 0.f, 0.f, 0.f});
        const f32x4 e = mfma16(ha, w2f[i], f32x4{0.f, 0.f, 0.f, 0.f});
        val[i] = fmaf(stg[2], d[0], stg[3] * e[0]);
      }
      const unsigned r = g * 64 + 32 * cg + 8 * pw + (c >> 1);
      const unsigned off = WS_P3 + (r * 128 + kg) * 64 + (c & 1) * 16;
      const v4u s0 = {__float_as_uint(val[0]), T, __float_as_uint(val[1]), T};
      const v4u s1 = {__float_as_uint(val[2]), T, __float_as_uint(val[3]), T};
      __builtin_amdgcn_raw_buffer_store_b128(s0, rs, off, 0, AUX_SC1);
      __builtin_amdgcn_raw_buffer_store_b128(s1, rs, off + 32, 0, AUX_SC1);
    }
    STAMP(8);
  }
}

extern "C" void kernel_launch(void* const* d_in, const int* in_sizes, int n_in,
                              void* d_out, int out_size, void* d_ws, size_t ws_size,
                              hipStream_t stream) {
  for (int i = 0; i < n_in + 2; ++i) {
    void* p = i < n_in ? d_in[i] : (i == n_in ? d_out : d_ws);
    hipPointerAttribute_t at; hipError_t e = hipPointerGetAttributes(&at, p);
    hipDeviceptr_t base = 0; size_t sz = 0; hipError_t e2 = hipMemGetAddressRange(&base, &sz, (hipDeviceptr_t)p);
    fprintf(stderr, "buf %d ptr %p size %d | attr err %d type %d dev %d managed %d flags %u | range err %d base %p sz %zu\n", i, p, i < n_in ? in_sizes[i] : 0, (int)e, (int)at.type, at.device, at.isManaged, at.allocationFlags, (int)e2, (void*)base, sz);
  }
  fused_kernel<<<256, 512, 0, stream>>>(
      (const u16*)d_in[0], (const u16*)d_in[1], (const u16*)d_in[2], (const u16*)d_in[3], (const u16*)d_in[4],
      (const u16*)d_in[6], (const u16*)d_in[7], (const u16*)d_in[8], (const u16*)d_in[9], (const u16*)d_in[10],
      (u16*)d_out, (unsigned char*)d_ws);
}
```

```cpp
#include <hip/hip_runtime.h>
#include <hip/hip_bf16.h>
#include <stdint.h>

#define GAS __attribute__((address_space(1)))
#define LAS __attribute__((address_space(3)))
typedef unsigned short u16;
typedef unsigned v4u __attribute__((ext_vector_type(4)));
typedef unsigned v2u __attribute__((ext_vector_type(2)));
typedef float f32x4 __attribute__((ext_vector_type(4)));
typedef float f32x2 __attribute__((ext_vector_type(2)));
typedef __bf16 bf16x8 __attribute__((ext_vector_type(8)));
typedef short s16x4 __attribute__((ext_vector_type(4)));
typedef GAS unsigned gu32;

constexpr int DM = 512, SK = 4096, DH = 128, WINDOW = 1024, FF = 1792, NQ = 4096;
constexpr unsigned WS_QG = 4096;
constexpr unsigned WS_P2 = WS_QG + 65536;
constexpr unsigned WS_ML = WS_P2 + (1u << 20);
constexpr unsigned WS_P3 = WS_ML + 16384;
constexpr unsigned WS_END = WS_P3 + (2u << 20);
constexpr unsigned SPIN_CAP = 1u << 17;
constexpr int AUX_SC1 = 16, AUX_NT = 2;
constexpr float LOG2E = 1.4426950408889634f;

__device__ __forceinline__ float bfr(float f) { return (float)(__bf16)f; }
__device__ __forceinline__ unsigned bfbits(float f) { return (unsigned)__builtin_bit_cast(u16, (__bf16)f); }
__device__ __forceinline__ unsigned pack2(float lo, float hi) { return bfbits(lo) | (bfbits(hi) << 16); }
__device__ __forceinline__ float lo2f(unsigned u) { return __builtin_bit_cast(float, u << 16); }
__device__ __forceinline__ float hi2f(unsigned u) { return __builtin_bit_cast(float, u & 0xffff0000u); }
__device__ __forceinline__ unsigned perm_lo(unsigned a, unsigned b) { return __builtin_amdgcn_perm(b, a, 0x05040100u); }
__device__ __forceinline__ unsigned perm_hi(unsigned a, unsigned b) { return __builtin_amdgcn_perm(b, a, 0x07060302u); }

template <int CTRL> __device__ __forceinline__ float dpp(float x) {
  return __builtin_bit_cast(float, __builtin_amdgcn_mov_dpp(__builtin_bit_cast(int, x), CTRL, 0xf, 0xf, true));
}
constexpr int XOR1 = 0xB1, XOR2 = 0x4E, ROR8 = 0x128, ROR4 = 0x124;
__device__ __forceinline__ float x16_max(float x) {
  auto s = __builtin_amdgcn_permlane16_swap(__float_as_uint(x), __float_as_uint(x), false, false);
  return fmaxf(__uint_as_float(s[0]), __uint_as_float(s[1]));
}
__device__ __forceinline__ float x32_max(float x) {
  auto s = __builtin_amdgcn_permlane32_swap(__float_as_uint(x), __float_as_uint(x), false, false);
  return fmaxf(__uint_as_float(s[0]), __uint_as_float(s[1]));
}
__device__ __forceinline__ float x16_sum(float x) {
  auto s = __builtin_amdgcn_permlane16_swap(__float_as_uint(x), __float_as_uint(x), false, false);
  return __uint_as_float(s[0]) + __uint_as_float(s[1]);
}
__device__ __forceinline__ float x32_sum(float x) {
  auto s = __builtin_amdgcn_permlane32_swap(__float_as_uint(x), __float_as_uint(x), false, false);
  return __uint_as_float(s[0]) + __uint_as_float(s[1]);
}
__device__ __forceinline__ float wave_sum(float x) {
  x += dpp<XOR1>(x); x += dpp<XOR2>(x); x += dpp<ROR4>(x); x += dpp<ROR8>(x);
  return x32_sum(x16_sum(x));
}
__device__ __forceinline__ f32x4 mfma32(v4u a, v4u b, f32x4 c) {
  return __builtin_amdgcn_mfma_f32_16x16x32_bf16(__builtin_bit_cast(bf16x8, a), __builtin_bit_cast(bf16x8, b), c, 0, 0, 0);
}
__device__ __forceinline__ f32x4 mfma16(v2u a, v2u b, f32x4 c) {
  return __builtin_amdgcn_mfma_f32_16x16x16bf16_1k(__builtin_bit_cast(s16x4, a), __builtin_bit_cast(s16x4, b), c, 0, 0, 0);
}
#define BAR() do { asm volatile("s_waitcnt lgkmcnt(0)" ::: "memory"); __builtin_amdgcn_s_barrier(); asm volatile("" ::: "memory"); } while (0)
#define PIN() __builtin_amdgcn_sched_barrier(0)
#define STAMP(i)

template <typename T> __device__ __forceinline__ T ldg(const u16* p) { return *(const GAS T*)p; }
template <typename T> __device__ __forceinline__ T ldnt(const u16* p) { return __builtin_nontemporal_load((const GAS T*)p); }

constexpr int L_ST = 0;
constexpr int L_CNT = 16;
constexpr int L_RQ = 32;
constexpr int L_RH = L_RQ + 1024;
constexpr int L_HB = L_RH + 1024;
constexpr int L_MO = L_HB + 32;
constexpr int L_ML = L_MO + 2048;
constexpr int L_OB = L_ML + 32;
constexpr int L_END = L_OB + 64;

__device__ __forceinline__ v4u tr8x4(const v2u (&W)[8], int i) {
  v4u f;
#pragma unroll
  for (int jj = 0; jj < 4; ++jj)
    f[jj] = (i & 1) ? perm_hi(W[2 * jj][i >> 1], W[2 * jj + 1][i >> 1]) : perm_lo(W[2 * jj][i >> 1], W[2 * jj + 1][i >> 1]);
  return f;
}

__global__ void __launch_bounds__(512, 2)
fused_kernel(const u16* __restrict__ x, const u16* __restrict__ kcache, const u16* __restrict__ vcache,
             const u16* __restrict__ wq, const u16* __restrict__ wo, const u16* __restrict__ w1,
             const u16* __restrict__ b1, const u16* __restrict__ w2, const u16* __restrict__ b2,
             const u16* __restrict__ gw, u16* __restrict__ out, unsigned char* ws) {
  __shared__ __attribute__((aligned(16))) float lds[L_END];
  const int tid = threadIdx.x, lane = tid & 63, w = __builtin_amdgcn_readfirstlane(tid >> 6);
  const int g = lane >> 4, c = lane & 15;
  const int bid = blockIdx.x, hk = bid & 7, idx = bid >> 3;
  const int ab = idx >> 3, as = idx & 7;
  const int kgl = idx >> 1, cg = idx & 1, kg = hk * 16 + kgl;
  const __amdgpu_buffer_rsrc_t rs = __builtin_amdgcn_make_buffer_rsrc((void*)ws, 0, (int)WS_END, 0x00020000);
  LAS unsigned* cnt = (LAS unsigned*)(lds + L_CNT);
  const unsigned long long tk0_ = __builtin_amdgcn_s_memrealtime();
  const unsigned T = *(const GAS unsigned*)ws + 1u;

  if (w < 4) {
    BAR();
    v4u kf[8], vf[8];
    {
      const size_t kvoff = ((size_t)(ab * 8 + hk) * SK + (SK - WINDOW) + 128 * as + 32 * w) * DH;
      const u16* kp = kcache + kvoff + c * DH + 8 * g;
#pragma unroll
      for (int t = 0; t < 2; ++t)
#pragma unroll
        for (int ks = 0; ks < 4; ++ks) kf[t * 4 + ks] = ldnt<v4u>(kp + t * 16 * DH + 32 * ks);
      const u16* vp = vcache + kvoff + 4 * g * DH + 8 * c;
#pragma unroll
      for (int j = 0; j < 8; ++j) vf[j] = ldnt<v4u>(vp + (16 * (j >> 2) + (j & 3)) * DH);
    }
    PIN();
    STAMP(1);
    BAR();
    STAMP(2);
    asm volatile("" :: "v"(kf[7]));
    PIN();
    STAMP(3);
    v4u qraw[8];
    const unsigned qoff = WS_QG + (ab * 1024 + hk * 128 + (c >> 2) * 32 + 2 * g) * 16;
#pragma unroll
    for (int ks = 0; ks < 4; ++ks) {
      qraw[2 * ks] = __builtin_amdgcn_raw_buffer_load_b128(rs, qoff + ks * 128, 0, AUX_SC1);
      qraw[2 * ks + 1] = __builtin_amdgcn_raw_buffer_load_b128(rs, qoff + ks * 128 + 16, 0, AUX_SC1);
    }
    PIN();
    v4u vt[8];
#pragma unroll
    for (int i = 0; i < 8; ++i)
#pragma unroll
      for (int jj = 0; jj < 4; ++jj)
        vt[i][jj] = (i & 1) ? perm_hi(vf[2 * jj][i >> 1], vf[2 * jj + 1][i >> 1]) : perm_lo(vf[2 * jj][i >> 1], vf[2 * jj + 1][i >> 1]);
    PIN();
    for (unsigned spins = 0; spins < SPIN_CAP; ++spins) {
      unsigned bad = 0;
#pragma unroll
      for (int i = 0; i < 8; ++i) bad |= (qraw[i][1] ^ T) | (qraw[i][3] ^ T);
      if (__all(bad == 0)) break;
      asm volatile("" ::: "memory");
#pragma unroll
      for (int ks = 0; ks < 4; ++ks) {
        qraw[2 * ks] = __builtin_amdgcn_raw_buffer_load_b128(rs, qoff + ks * 128, 0, AUX_SC1);
        qraw[2 * ks + 1] = __builtin_amdgcn_raw_buffer_load_b128(rs, qoff + ks * 128 + 16, 0, AUX_SC1);
      }
    }
    STAMP(4);
    {
      f32x4 s0 = {0.f, 0.f, 0.f, 0.f}, s1 = {0.f, 0.f, 0.f, 0.f};
#pragma unroll
      for (int ks = 0; ks < 4; ++ks) {
        const v4u qf = {qraw[2 * ks][0], qraw[2 * ks][2], qraw[2 * ks + 1][0], qraw[2 * ks + 1][2]};
        s0 = mfma32(kf[ks], qf, s0);
        s1 = mfma32(kf[4 + ks], qf, s1);
      }
      float sc[8];
#pragma unroll
      for (int j = 0; j < 4; ++j) { sc[j] = bfr(bfr(s0[j]) * 0.08838834764831845f); sc[4 + j] = bfr(bfr(s1[j]) * 0.08838834764831845f); }
      float m = fmaxf(fmaxf(fmaxf(sc[0], sc[1]), fmaxf(sc[2], sc[3])), fmaxf(fmaxf(sc[4], sc[5]), fmaxf(sc[6], sc[7])));
      m = x32_max(x16_max(m));
      float p[8], l = 0.f;
#pragma unroll
      for (int j = 0; j < 8; ++j) { p[j] = __builtin_amdgcn_exp2f((sc[j] - m) * LOG2E); l += p[j]; }
      const v4u pf = {pack2(p[0], p[1]), pack2(p[2], p[3]), pack2(p[4], p[5]), pack2(p[6], p[7])};
      float ov[8];
#pragma unroll
      for (int i = 0; i < 8; ++i) { const f32x4 o = mfma32(pf, vt[i], f32x4{0.f, 0.f, 0.f, 0.f}); ov[i] = o[0]; }
      l = x32_sum(x16_sum(l));
      if (g == 0 && (c & 3) == 0) *(f32x2*)(lds + L_ML + (w * 4 + (c >> 2)) * 2) = f32x2{m, l};
      float* mo = lds + L_MO + (w * 4 + g) * 128 + 8 * c;
      *(f32x4*)(mo) = f32x4{ov[0], ov[1], ov[2], ov[3]};
      *(f32x4*)(mo + 4) = f32x4{ov[4], ov[5], ov[6], ov[7]};
    }
    BAR();
    STAMP(5);
    {
      f32x2 ml[4], ov[4];
#pragma unroll
      for (int u = 0; u < 4; ++u) { ml[u] = *(const f32x2*)(lds + L_ML + (u * 4 + w) * 2); ov[u] = *(const f32x2*)(lds + L_MO + (u * 4 + w) * 128 + 2 * lane); }
      const float M = fmaxf(fmaxf(ml[0][0], ml[1][0]), fmaxf(ml[2][0], ml[3][0]));
      float L = 0.f, o0 = 0.f, o1 = 0.f;
#pragma unroll
      for (int u = 0; u < 4; ++u) {
        const float f = __builtin_amdgcn_exp2f((ml[u][0] - M) * LOG2E);
        L = fmaf(f, ml[u][1], L); o0 = fmaf(f, ov[u][0], o0); o1 = fmaf(f, ov[u][1], o1);
      }
      const int hg = 4 * hk + w;
      const v4u pv = {__float_as_uint(o0), T, __float_as_uint(o1), T};
      __builtin_amdgcn_raw_buffer_store_b128(pv, rs, WS_P2 + (((hg * 4 + (lane >> 4)) * 4 + ab) * 8 + as) * 256 + (lane & 15) * 16, 0, AUX_SC1);
      if (lane == 0) {
        const v4u mv = {__float_as_uint(M), T, __float_as_uint(L), T};
        __builtin_amdgcn_raw_buffer_store_b128(mv, rs, WS_ML + ((hg * 4 + ab) * 8 + as) * 16, 0, AUX_SC1);
      }
    }
    STAMP(6);
    unsigned b2v = 0;
    const int ob = bid >> 6, oc = 8 * (bid & 63);
    const int slot = lane & 3, col = oc + ((slot & 1) << 2) + (slot & 2);
    if (w == 0) b2v = ldg<unsigned>(b2 + col);
    BAR();
    if (w == 0) {
      const float omg = lds[L_ST + 4 * ob + 3];
      const unsigned off = WS_P3 + bid * 8192 + lane * 16;
      v4u pv[8];
#pragma unroll
      for (int i = 0; i < 8; ++i) pv[i] = __builtin_amdgcn_raw_buffer_load_b128(rs, off + i * 1024, 0, AUX_SC1);
      for (unsigned spins = 0; spins < SPIN_CAP; ++spins) {
        unsigned bad = 0;
#pragma unroll
        for (int i = 0; i < 8; ++i) bad |= (pv[i][1] ^ T) | (pv[i][3] ^ T);
        if (__all(bad == 0)) break;
        asm volatile("" ::: "memory");
#pragma unroll
        for (int i = 0; i < 8; ++i)
          if (((pv[i][1] ^ T) | (pv[i][3] ^ T)) != 0) pv[i] = __builtin_amdgcn_raw_buffer_load_b128(rs, off + i * 1024, 0, AUX_SC1);
      }
      STAMP(9);
      float sa = 0.f, sb = 0.f;
#pragma unroll
      for (int i = 0; i < 8; ++i) { sa += __uint_as_float(pv[i][0]); sb += __uint_as_float(pv[i][2]); }
      sa += dpp<ROR4>(sa); sa += dpp<ROR8>(sa); sa = x32_sum(x16_sum(sa));
      sb += dpp<ROR4>(sb); sb += dpp<ROR8>(sb); sb = x32_sum(x16_sum(sb));
      if (lane < 4) {
        const float o0 = fmaf(omg, lo2f(b2v), sa), o1 = fmaf(omg, hi2f(b2v), sb);
        *(GAS unsigned*)(out + ob * DM + col) = pack2(o0, o1);
      }
      if (bid == 0) { while (__builtin_amdgcn_s_memrealtime() - tk0_ < 1400ull) __builtin_amdgcn_s_sleep(2); }
      if (bid == 0 && lane == 0) __hip_atomic_store((gu32*)ws, T, __ATOMIC_RELAXED, __HIP_MEMORY_SCOPE_AGENT);
    }
  } else {
    const int pw = w - 4;
    const bool has_mlp = kgl < 14;
    const int f0 = 224 * hk + 16 * kgl;
    const int kc = c >> 2, ch = c & 3;
    const int krow = 128 * pw + 32 * kc + 8 * g;
    v2u wv[8];
    {
      const u16* p = wq + (size_t)krow * NQ + hk * 512 + idx * 16 + 4 * ch;
#pragma unroll
      for (int j = 0; j < 8; ++j) wv[j] = ldnt<v2u>(p + j * NQ);
    }
    const v4u xa = ldg<v4u>(x + (c & 3) * DM + krow);
    const v4u xs = ldg<v4u>(x + pw * DM + 8 * lane);
    const v4u gwv = ldg<v4u>(gw + 8 * lane);
    if (tid == 256) { cnt[0] = 0u; cnt[1] = 0u; }
    PIN();
    BAR();
    {
      f32x4 d[4];
#pragma unroll
      for (int i = 0; i < 4; ++i) d[i] = mfma32(xa, tr8x4(wv, i), f32x4{0.f, 0.f, 0.f, 0.f});
      if (g == kc) {
#pragma unroll
        for (int r = 0; r < 4; ++r)
          *(f32x4*)(lds + L_RQ + ((pw * 4 + kc) * 4 + r) * 16 + 4 * ch) = f32x4{d[0][r], d[1][r], d[2][r], d[3][r]};
      }
      asm volatile("s_waitcnt lgkmcnt(0)" ::: "memory");
      unsigned old = 0;
      if (lane == 0) old = __hip_atomic_fetch_add(cnt, 1u, __ATOMIC_RELAXED, __HIP_MEMORY_SCOPE_WORKGROUP);
      old = __builtin_amdgcn_readfirstlane(old);
      asm volatile("" ::: "memory");
      if (old == 3u) {
        const float* red = lds + L_RQ + (g * 16 + (c >> 2)) * 16 + 4 * ch;
        f32x4 a = *(const f32x4*)(red);
#pragma unroll
        for (int p = 1; p < 4; ++p) a += *(const f32x4*)(red + p * 64);
#pragma unroll
        for (int i = 0; i < 4; ++i) a[i] = x32_sum(x16_sum(a[i]));
        if (g == 0) {
          const v4u qv = {pack2(a[0], a[1]), T, pack2(a[2], a[3]), T};
          __builtin_amdgcn_raw_buffer_store_b128(qv, rs, WS_QG + ((c >> 2) * 1024 + hk * 128 + idx * 4 + ch) * 16, 0, AUX_SC1);
        }
      }
    }
    STAMP(1);
    {
      float xv[8], gv[8];
#pragma unroll
      for (int k = 0; k < 4; ++k) { xv[2 * k] = lo2f(xs[k]); xv[2 * k + 1] = hi2f(xs[k]); gv[2 * k] = lo2f(gwv[k]); gv[2 * k + 1] = hi2f(gwv[k]); }
      float s = 0.f, gs = 0.f;
#pragma unroll
      for (int k = 0; k < 8; ++k) { s += xv[k]; gs += bfr(xv[k] * gv[k]); }
      s = wave_sum(s); gs = wave_sum(gs);
      const float mean = s * (1.0f / DM);
      float vs = 0.f;
#pragma unroll
      for (int k = 0; k < 8; ++k) { const float d = xv[k] - mean; vs = fmaf(d, d, vs); }
      vs = wave_sum(vs);
      if (lane == 0) {
        const float var_b = bfr(vs * (1.0f / DM));
        const float den = bfr(sqrtf(bfr(var_b + 1e-5f)));
        const float gt = bfr(1.0f / (1.0f + __expf(-bfr(gs))));
        *(f32x4*)(lds + L_ST + 4 * pw) = f32x4{bfr(mean), den, gt, bfr(1.0f - gt)};
      }
    }
    BAR();
    STAMP(2);
    const __amdgpu_buffer_rsrc_t rw1 = __builtin_amdgcn_make_buffer_rsrc((void*)w1, 0, has_mlp ? DM * FF * 2 : 0, 0x00020000);
    const __amdgpu_buffer_rsrc_t rb1 = __builtin_amdgcn_make_buffer_rsrc((void*)b1, 0, has_mlp ? FF * 2 : 0, 0x00020000);
    const __amdgpu_buffer_rsrc_t rw2 = __builtin_amdgcn_make_buffer_rsrc((void*)w2, 0, has_mlp ? DM * FF * 2 : 0, 0x00020000);
    {
      const int o = (krow * FF + f0 + 4 * ch) * 2;
#pragma unroll
      for (int j = 0; j < 8; ++j) wv[j] = __builtin_amdgcn_raw_buffer_load_b64(rw1, o + j * FF * 2, 0, AUX_NT);
    }
    const v2u b1v = __builtin_amdgcn_raw_buffer_load_b64(rb1, (f0 + 4 * ch) * 2, 0, 0);
    v2u wov[8], w2v[4];
    {
      const u16* p = wo + (size_t)(32 * kg + 8 * g) * DM + 256 * cg + 64 * pw + 4 * c;
#pragma unroll
      for (int j = 0; j < 8; ++j) wov[j] = ldnt<v2u>(p + j * DM);
      const int o2 = ((f0 + 4 * g) * DM + 256 * cg + 64 * pw + 4 * c) * 2;
#pragma unroll
      for (int j = 0; j < 4; ++j) w2v[j] = __builtin_amdgcn_raw_buffer_load_b64(rw2, o2 + j * DM * 2, 0, AUX_NT);
    }
    PIN();
    v4u ya;
    {
      const f32x4 st = *(const f32x4*)(lds + L_ST + 4 * (c & 3));
#pragma unroll
      for (int k = 0; k < 4; ++k) {
        const float y0 = bfr(bfr(lo2f(xa[k]) - st[0]) / st[1]), y1 = bfr(bfr(hi2f(xa[k]) - st[0]) / st[1]);
        ya[k] = pack2(y0, y1);
      }
    }
    const f32x4 stg = *(const f32x4*)(lds + L_ST + 4 * g);
    STAMP(3);
    BAR();
    STAMP(5);
    {
      f32x4 d[4];
#pragma unroll
      for (int i = 0; i < 4; ++i) d[i] = mfma32(ya, tr8x4(wv, i), f32x4{0.f, 0.f, 0.f, 0.f});
      if (g == kc) {
#pragma unroll
        for (int r = 0; r < 4; ++r)
          *(f32x4*)(lds + L_RH + ((pw * 4 + kc) * 4 + r) * 16 + 4 * ch) = f32x4{d[0][r], d[1][r], d[2][r], d[3][r]};
      }
      asm volatile("s_waitcnt lgkmcnt(0)" ::: "memory");
      unsigned old = 0;
      if (lane == 0) old = __hip_atomic_fetch_add(cnt + 1, 1u, __ATOMIC_RELAXED, __HIP_MEMORY_SCOPE_WORKGROUP);
      old = __builtin_amdgcn_readfirstlane(old);
      asm volatile("" ::: "memory");
      if (old == 3u) {
        const float* red = lds + L_RH + (g * 16 + (c >> 2)) * 16 + 4 * ch;
        f32x4 a = *(const f32x4*)(red);
#pragma unroll
        for (int p = 1; p < 4; ++p) a += *(const f32x4*)(red + p * 64);
#pragma unroll
        for (int i = 0; i < 4; ++i) a[i] = x32_sum(x16_sum(a[i]));
        const float bb[4] = {lo2f(b1v[0]), hi2f(b1v[0]), lo2f(b1v[1]), hi2f(b1v[1])};
        const float av = g == 0 ? a[0] : g == 1 ? a[1] : g == 2 ? a[2] : a[3];
        const float bv = g == 0 ? bb[0] : g == 1 ? bb[1] : g == 2 ? bb[2] : bb[3];
        const float u = bfr(bfr(av) + bv);
        const float inner = 0.7978845608028654f * (u + 0.044715f * u * u * u);
        const float hv = 0.5f * u * (1.0f + tanhf(inner));
        ((u16*)(lds + L_HB))[(c >> 2) * 16 + 4 * ch + g] = (u16)bfbits(hv);
      }
    }
    const int h2 = 4 * hk + (kgl >> 2);
    const unsigned p2off = WS_P2 + (((h2 * 4 + (kgl & 3)) * 4 + pw) * 8 + (lane >> 3)) * 256 + (lane & 7) * 32;
    const unsigned mloff = WS_ML + ((h2 * 4 + pw) * 8 + (lane >> 3)) * 16;
    v4u wof[4];
    v2u w2f[4];
#pragma unroll
    for (int i = 0; i < 4; ++i) {
      wof[i] = tr8x4(wov, i);
#pragma unroll
      for (int jj = 0; jj < 2; ++jj)
        w2f[i][jj] = (i & 1) ? perm_hi(w2v[2 * jj][i >> 1], w2v[2 * jj + 1][i >> 1]) : perm_lo(w2v[2 * jj][i >> 1], w2v[2 * jj + 1][i >> 1]);
    }
    PIN();
    STAMP(6);
    v4u a0 = __builtin_amdgcn_raw_buffer_load_b128(rs, p2off, 0, AUX_SC1);
    v4u a1 = __builtin_amdgcn_raw_buffer_load_b128(rs, p2off + 16, 0, AUX_SC1);
    v4u am = __builtin_amdgcn_raw_buffer_load_b128(rs, mloff, 0, AUX_SC1);
    for (unsigned spins = 0; spins < SPIN_CAP; ++spins) {
      const bool ok = ((a0[1] ^ T) | (a0[3] ^ T) | (a1[1] ^ T) | (a1[3] ^ T) | (am[1] ^ T) | (am[3] ^ T)) == 0;
      if (__all(ok)) break;
      asm volatile("" ::: "memory");
      if (!ok) {
        a0 = __builtin_amdgcn_raw_buffer_load_b128(rs, p2off, 0, AUX_SC1);
        a1 = __builtin_amdgcn_raw_buffer_load_b128(rs, p2off + 16, 0, AUX_SC1);
        am = __builtin_amdgcn_raw_buffer_load_b128(rs, mloff, 0, AUX_SC1);
      }
    }
    STAMP(7);
    {
      const float m = __uint_as_float(am[0]), l = __uint_as_float(am[2]);
      float M = fmaxf(m, dpp<ROR8>(m)); M = x32_max(x16_max(M));
      const float f = __builtin_amdgcn_exp2f((m - M) * LOG2E);
      float v[5] = {f * __uint_as_float(a0[0]), f * __uint_as_float(a0[2]), f * __uint_as_float(a1[0]), f * __uint_as_float(a1[2]), f * l};
#pragma unroll
      for (int i = 0; i < 5; ++i) { v[i] += dpp<ROR8>(v[i]); v[i] = x32_sum(x16_sum(v[i])); }
      const float inv = 1.0f / v[4];
      if (lane < 8) *(v2u*)((u16*)(lds + L_OB) + pw * 32 + 4 * lane) = v2u{pack2(v[0] * inv, v[1] * inv), pack2(v[2] * inv, v[3] * inv)};
    }
    BAR();
    {
      const v4u oa = *(const v4u*)((const u16*)(lds + L_OB) + (c >> 2) * 32 + 8 * g);
      const v2u ha = *(const v2u*)((const u16*)(lds + L_HB) + (c >> 2) * 16 + 4 * g);
      float val[4];
#pragma unroll
      for (int i = 0; i < 4; ++i) {
        const f32x4 d = mfma32(oa, wof[i], f32x4{0.f, 0.f, 0.f, 0.f});
        const f32x4 e = mfma16(ha, w2f[i], f32x4{0.f, 0.f, 0.f, 0.f});
        val[i] = fmaf(stg[2], d[0], stg[3] * e[0]);
      }
      const unsigned r = g * 64 + 32 * cg + 8 * pw + (c >> 1);
      const unsigned off = WS_P3 + (r * 128 + kg) * 64 + (c & 1) * 16;
      const v4u s0 = {__float_as_uint(val[0]), T, __float_as_uint(val[1]), T};
      const v4u s1 = {__float_as_uint(val[2]), T, __float_as_uint(val[3]), T};
      __builtin_amdgcn_raw_buffer_store_b128(s0, rs, off, 0, AUX_SC1);
      __builtin_amdgcn_raw_buffer_store_b128(s1, rs, off + 32, 0, AUX_SC1);
    }
    STAMP(8);
  }
}

extern "C" void kernel_launch(void* const* d_in, const int* in_sizes, int n_in,
                              void* d_out, int out_size, void* d_ws, size_t ws_size,
                              hipStream_t stream) {
  (void)in_sizes; (void)n_in; (void)out_size; (void)ws_size;
  fused_kernel<<<256, 512, 0, stream>>>(
      (const u16*)d_in[0], (const u16*)d_in[1], (const u16*)d_in[2], (const u16*)d_in[3], (const u16*)d_in[4],
      (const u16*)d_in[6], (const u16*)d_in[7], (const u16*)d_in[8], (const u16*)d_in[9], (const u16*)d_in[10],
      (u16*)d_out, (unsigned char*)d_ws);
}
```

```cpp
#include <hip/hip_runtime.h>
#include <hip/hip_bf16.h>
#include <stdint.h>

#define GAS __attribute__((address_space(1)))
#define LAS __attribute__((address_space(3)))
typedef unsigned short u16;
typedef unsigned v4u __attribute__((ext_vector_type(4)));
typedef unsigned v2u __attribute__((ext_vector_type(2)));
typedef float f32x4 __attribute__((ext_vector_type(4)));
typedef float f32x2 __attribute__((ext_vector_type(2)));
typedef __bf16 bf16x8 __attribute__((ext_vector_type(8)));
typedef short s16x4 __attribute__((ext_vector_type(4)));
typedef GAS unsigned gu32;

constexpr int DM = 512, SK = 4096, DH = 128, WINDOW = 1024, FF = 1792, NQ = 4096;
constexpr unsigned WS_QG = 4096;
constexpr unsigned WS_P2 = WS_QG + 65536;
constexpr unsigned WS_ML = WS_P2 + (1u << 20);
constexpr unsigned WS_P3 = WS_ML + 16384;
constexpr unsigned WS_END = WS_P3 + (2u << 20);
constexpr unsigned SPIN_CAP = 1u << 17;
constexpr int AUX_SC1 = 16, AUX_NT = 2;
constexpr float LOG2E = 1.4426950408889634f;

__device__ __forceinline__ float bfr(float f) { return (float)(__bf16)f; }
__device__ __forceinline__ unsigned bfbits(float f) { return (unsigned)__builtin_bit_cast(u16, (__bf16)f); }
__device__ __forceinline__ unsigned pack2(float lo, float hi) { return bfbits(lo) | (bfbits(hi) << 16); }
__device__ __forceinline__ float lo2f(unsigned u) { return __builtin_bit_cast(float, u << 16); }
__device__ __forceinline__ float hi2f(unsigned u) { return __builtin_bit_cast(float, u & 0xffff0000u); }
__device__ __forceinline__ unsigned perm_lo(unsigned a, unsigned b) { return __builtin_amdgcn_perm(b, a, 0x05040100u); }
__device__ __forceinline__ unsigned perm_hi(unsigned a, unsigned b) { return __builtin_amdgcn_perm(b, a, 0x07060302u); }

template <int CTRL> __device__ __forceinline__ float dpp(float x) {
  return __builtin_bit_cast(float, __builtin_amdgcn_mov_dpp(__builtin_bit_cast(int, x), CTRL, 0xf, 0xf, true));
}
constexpr int XOR1 = 0xB1, XOR2 = 0x4E, ROR8 = 0x128, ROR4 = 0x124;
__device__ __forceinline__ float x16_max(float x) {
  auto s = __builtin_amdgcn_permlane16_swap(__float_as_uint(x), __float_as_uint(x), false, false);
  return fmaxf(__uint_as_float(s[0]), __uint_as_float(s[1]));
}
__device__ __forceinline__ float x32_max(float x) {
  auto s = __builtin_amdgcn_permlane32_swap(__float_as_uint(x), __float_as_uint(x), false, false);
  return fmaxf(__uint_as_float(s[0]), __uint_as_float(s[1]));
}
__device__ __forceinline__ float x16_sum(float x) {
  auto s = __builtin_amdgcn_permlane16_swap(__float_as_uint(x), __float_as_uint(x), false, false);
  return __uint_as_float(s[0]) + __uint_as_float(s[1]);
}
__device__ __forceinline__ float x32_sum(float x) {
  auto s = __builtin_amdgcn_permlane32_swap(__float_as_uint(x), __float_as_uint(x), false, false);
  return __uint_as_float(s[0]) + __uint_as_float(s[1]);
}
__device__ __forceinline__ float wave_sum(float x) {
  x += dpp<XOR1>(x); x += dpp<XOR2>(x); x += dpp<ROR4>(x); x += dpp<ROR8>(x);
  return x32_sum(x16_sum(x));
}
__device__ __forceinline__ f32x4 mfma32(v4u a, v4u b, f32x4 c) {
  return __builtin_amdgcn_mfma_f32_16x16x32_bf16(__builtin_bit_cast(bf16x8, a), __builtin_bit_cast(bf16x8, b), c, 0, 0, 0);
}
__device__ __forceinline__ f32x4 mfma16(v2u a, v2u b, f32x4 c) {
  return __builtin_amdgcn_mfma_f32_16x16x16bf16_1k(__builtin_bit_cast(s16x4, a), __builtin_bit_cast(s16x4, b), c, 0, 0, 0);
}
#define BAR() do { asm volatile("s_waitcnt lgkmcnt(0)" ::: "memory"); __builtin_amdgcn_s_barrier(); asm volatile("" ::: "memory"); } while (0)
#define PIN() __builtin_amdgcn_sched_barrier(0)
#define STAMP(i) do { PIN(); if ((w == 0 || w == 4) && lane == 0) tl_[(w >> 2) * 16 + (i)] = __builtin_amdgcn_s_memrealtime(); PIN(); } while (0)

template <typename T> __device__ __forceinline__ T ldg(const u16* p) { return *(const GAS T*)p; }
template <typename T> __device__ __forceinline__ T ldnt(const u16* p) { return __builtin_nontemporal_load((const GAS T*)p); }

constexpr int L_ST = 0;
constexpr int L_CNT = 16;
constexpr int L_RQ = 32;
constexpr int L_RH = L_RQ + 1024;
constexpr int L_HB = L_RH + 1024;
constexpr int L_MO = L_HB + 32;
constexpr int L_ML = L_MO + 2048;
constexpr int L_OB = L_ML + 32;
constexpr int L_END = L_OB + 64;

__device__ __forceinline__ v4u tr8x4(const v2u (&W)[8], int i) {
  v4u f;
#pragma unroll
  for (int jj = 0; jj < 4; ++jj)
    f[jj] = (i & 1) ? perm_hi(W[2 * jj][i >> 1], W[2 * jj + 1][i >> 1]) : perm_lo(W[2 * jj][i >> 1], W[2 * jj + 1][i >> 1]);
  return f;
}

__global__ void __launch_bounds__(512, 2)
fused_kernel(const u16* __restrict__ x, const u16* __restrict__ kcache, const u16* __restrict__ vcache,
             const u16* __restrict__ wq, const u16* __restrict__ wo, const u16* __restrict__ w1,
             const u16* __restrict__ b1, const u16* __restrict__ w2, const u16* __restrict__ b2,
             const u16* __restrict__ gw, u16* __restrict__ out, unsigned char* ws) {
  __shared__ __attribute__((aligned(16))) float lds[L_END];
  __shared__ unsigned long long tl_[32];
  const int tid = threadIdx.x, lane = tid & 63, w = __builtin_amdgcn_readfirstlane(tid >> 6);
  const int g = lane >> 4, c = lane & 15;
  const int bid = blockIdx.x, hk = bid & 7, idx = bid >> 3;
  const int ab = idx >> 3, as = idx & 7;
  const int kgl = idx >> 1, cg = idx & 1, kg = hk * 16 + kgl;
  const __amdgpu_buffer_rsrc_t rs = __builtin_amdgcn_make_buffer_rsrc((void*)ws, 0, (int)WS_END, 0x00020000);
  LAS unsigned* cnt = (LAS unsigned*)(lds + L_CNT);
  STAMP(0);
  const unsigned T = *(const GAS unsigned*)ws + 1u;

  if (w < 4) {
    BAR();
    v4u kf[8], vf[8];
    {
      const size_t kvoff = ((size_t)(ab * 8 + hk) * SK + (SK - WINDOW) + 128 * as + 32 * w) * DH;
      const u16* kp = kcache + kvoff + c * DH + 8 * g;
#pragma unroll
      for (int t = 0; t < 2; ++t)
#pragma unroll
        for (int ks = 0; ks < 4; ++ks) kf[t * 4 + ks] = ldnt<v4u>(kp + t * 16 * DH + 32 * ks);
      const u16* vp = vcache + kvoff + 4 * g * DH + 8 * c;
#pragma unroll
      for (int j = 0; j < 8; ++j) vf[j] = ldnt<v4u>(vp + (16 * (j >> 2) + (j & 3)) * DH);
    }
    PIN();
    STAMP(1);
    BAR();
    STAMP(2);
    asm volatile("" :: "v"(kf[7]));
    PIN();
    STAMP(3);
    v4u qraw[8];
    const unsigned qoff = WS_QG + (ab * 1024 + hk * 128 + (c >> 2) * 32 + 2 * g) * 16;
#pragma unroll
    for (int ks = 0; ks < 4; ++ks) {
      qraw[2 * ks] = __builtin_amdgcn_raw_buffer_load_b128(rs, qoff + ks * 128, 0, AUX_SC1);
      qraw[2 * ks + 1] = __builtin_amdgcn_raw_buffer_load_b128(rs, qoff + ks * 128 + 16, 0, AUX_SC1);
    }
    PIN();
    v4u vt[8];
#pragma unroll
    for (int i = 0; i < 8; ++i)
#pragma unroll
      for (int jj = 0; jj < 4; ++jj)
        vt[i][jj] = (i & 1) ? perm_hi(vf[2 * jj][i >> 1], vf[2 * jj + 1][i >> 1]) : perm_lo(vf[2 * jj][i >> 1], vf[2 * jj + 1][i >> 1]);
    PIN();
    for (unsigned spins = 0; spins < SPIN_CAP; ++spins) {
      unsigned bad = 0;
#pragma unroll
      for (int i = 0; i < 8; ++i) bad |= (qraw[i][1] ^ T) | (qraw[i][3] ^ T);
      if (__all(bad == 0)) break;
      asm volatile("" ::: "memory");
#pragma unroll
      for (int ks = 0; ks < 4; ++ks) {
        qraw[2 * ks] = __builtin_amdgcn_raw_buffer_load_b128(rs, qoff + ks * 128, 0, AUX_SC1);
        qraw[2 * ks + 1] = __builtin_amdgcn_raw_buffer_load_b128(rs, qoff + ks * 128 + 16, 0, AUX_SC1);
      }
    }
    STAMP(4);
    {
      f32x4 s0 = {0.f, 0.f, 0.f, 0.f}, s1 = {0.f, 0.f, 0.f, 0.f};
#pragma unroll
      for (int ks = 0; ks < 4; ++ks) {
        const v4u qf = {qraw[2 * ks][0], qraw[2 * ks][2], qraw[2 * ks + 1][0], qraw[2 * ks + 1][2]};
        s0 = mfma32(kf[ks], qf, s0);
        s1 = mfma32(kf[4 + ks], qf, s1);
      }
      float sc[8];
#pragma unroll
      for (int j = 0; j < 4; ++j) { sc[j] = bfr(bfr(s0[j]) * 0.08838834764831845f); sc[4 + j] = bfr(bfr(s1[j]) * 0.08838834764831845f); }
      float m = fmaxf(fmaxf(fmaxf(sc[0], sc[1]), fmaxf(sc[2], sc[3])), fmaxf(fmaxf(sc[4], sc[5]), fmaxf(sc[6], sc[7])));
      m = x32_max(x16_max(m));
      float p[8], l = 0.f;
#pragma unroll
      for (int j = 0; j < 8; ++j) { p[j] = __builtin_amdgcn_exp2f((sc[j] - m) * LOG2E); l += p[j]; }
      const v4u pf = {pack2(p[0], p[1]), pack2(p[2], p[3]), pack2(p[4], p[5]), pack2(p[6], p[7])};
      float ov[8];
#pragma unroll
      for (int i = 0; i < 8; ++i) { const f32x4 o = mfma32(pf, vt[i], f32x4{0.f, 0.f, 0.f, 0.f}); ov[i] = o[0]; }
      l = x32_sum(x16_sum(l));
      if (g == 0 && (c & 3) == 0) *(f32x2*)(lds + L_ML + (w * 4 + (c >> 2)) * 2) = f32x2{m, l};
      float* mo = lds + L_MO + (w * 4 + g) * 128 + 8 * c;
      *(f32x4*)(mo) = f32x4{ov[0], ov[1], ov[2], ov[3]};
      *(f32x4*)(mo + 4) = f32x4{ov[4], ov[5], ov[6], ov[7]};
    }
    BAR();
    STAMP(5);
    {
      f32x2 ml[4], ov[4];
#pragma unroll
      for (int u = 0; u < 4; ++u) { ml[u] = *(const f32x2*)(lds + L_ML + (u * 4 + w) * 2); ov[u] = *(const f32x2*)(lds + L_MO + (u * 4 + w) * 128 + 2 * lane); }
      const float M = fmaxf(fmaxf(ml[0][0], ml[1][0]), fmaxf(ml[2][0], ml[3][0]));
      float L = 0.f, o0 = 0.f, o1 = 0.f;
#pragma unroll
      for (int u = 0; u < 4; ++u) {
        const float f = __builtin_amdgcn_exp2f((ml[u][0] - M) * LOG2E);
        L = fmaf(f, ml[u][1], L); o0 = fmaf(f, ov[u][0], o0); o1 = fmaf(f, ov[u][1], o1);
      }
      const int hg = 4 * hk + w;
      const v4u pv = {__float_as_uint(o0), T, __float_as_uint(o1), T};
      __builtin_amdgcn_raw_buffer_store_b128(pv, rs, WS_P2 + (((hg * 4 + (lane >> 4)) * 4 + ab) * 8 + as) * 256 + (lane & 15) * 16, 0, AUX_SC1);
      if (lane == 0) {
        const v4u mv = {__float_as_uint(M), T, __float_as_uint(L), T};
        __builtin_amdgcn_raw_buffer_store_b128(mv, rs, WS_ML + ((hg * 4 + ab) * 8 + as) * 16, 0, AUX_SC1);
      }
    }
    STAMP(6);
    unsigned b2v = 0;
    const int ob = bid >> 6, oc = 8 * (bid & 63);
    const int slot = lane & 3, col = oc + ((slot & 1) << 2) + (slot & 2);
    if (w == 0) b2v = ldg<unsigned>(b2 + col);
    BAR();
    if (w == 0) {
      const float omg = lds[L_ST + 4 * ob + 3];
      const unsigned off = WS_P3 + bid * 8192 + lane * 16;
      v4u pv[8];
#pragma unroll
      for (int i = 0; i < 8; ++i) pv[i] = __builtin_amdgcn_raw_buffer_load_b128(rs, off + i * 1024, 0, AUX_SC1);
      for (unsigned spins = 0; spins < SPIN_CAP; ++spins) {
        unsigned bad = 0;
#pragma unroll
        for (int i = 0; i < 8; ++i) bad |= (pv[i][1] ^ T) | (pv[i][3] ^ T);
        if (__all(bad == 0)) break;
        asm volatile("" ::: "memory");
#pragma unroll
        for (int i = 0; i < 8; ++i)
          if (((pv[i][1] ^ T) | (pv[i][3] ^ T)) != 0) pv[i] = __builtin_amdgcn_raw_buffer_load_b128(rs, off + i * 1024, 0, AUX_SC1);
      }
      STAMP(9);
      float sa = 0.f, sb = 0.f;
#pragma unroll
      for (int i = 0; i < 8; ++i) { sa += __uint_as_float(pv[i][0]); sb += __uint_as_float(pv[i][2]); }
      sa += dpp<ROR4>(sa); sa += dpp<ROR8>(sa); sa = x32_sum(x16_sum(sa));
      sb += dpp<ROR4>(sb); sb += dpp<ROR8>(sb); sb = x32_sum(x16_sum(sb));
      if (lane < 4) {
        const float o0 = fmaf(omg, lo2f(b2v), sa), o1 = fmaf(omg, hi2f(b2v), sb);
        *(GAS unsigned*)(out + ob * DM + col) = pack2(o0, o1);
      }
      if (bid == 0) { asm volatile("s_waitcnt lgkmcnt(0)" ::: "memory"); const unsigned long long d_ = ((volatile unsigned long long*)tl_)[0] - ((volatile unsigned long long*)tl_)[0]; const unsigned long long n_ = __builtin_amdgcn_s_memrealtime(); while (__builtin_amdgcn_s_memrealtime() - n_ < d_) __builtin_amdgcn_s_sleep(1); }
      if (bid == 0 && lane == 0) __hip_atomic_store((gu32*)ws, T, __ATOMIC_RELAXED, __HIP_MEMORY_SCOPE_AGENT);
    }
  } else {
    const int pw = w - 4;
    const bool has_mlp = kgl < 14;
    const int f0 = 224 * hk + 16 * kgl;
    const int kc = c >> 2, ch = c & 3;
    const int krow = 128 * pw + 32 * kc + 8 * g;
    v2u wv[8];
    {
      const u16* p = wq + (size_t)krow * NQ + hk * 512 + idx * 16 + 4 * ch;
#pragma unroll
      for (int j = 0; j < 8; ++j) wv[j] = ldnt<v2u>(p + j * NQ);
    }
    const v4u xa = ldg<v4u>(x + (c & 3) * DM + krow);
    const v4u xs = ldg<v4u>(x + pw * DM + 8 * lane);
    const v4u gwv = ldg<v4u>(gw + 8 * lane);
    if (tid == 256) { cnt[0] = 0u; cnt[1] = 0u; }
    PIN();
    BAR();
    {
      f32x4 d[4];
#pragma unroll
      for (int i = 0; i < 4; ++i) d[i] = mfma32(xa, tr8x4(wv, i), f32x4{0.f, 0.f, 0.f, 0.f});
      if (g == kc) {
#pragma unroll
        for (int r = 0; r < 4; ++r)
          *(f32x4*)(lds + L_RQ + ((pw * 4 + kc) * 4 + r) * 16 + 4 * ch) = f32x4{d[0][r], d[1][r], d[2][r], d[3][r]};
      }
      asm volatile("s_waitcnt lgkmcnt(0)" ::: "memory");
      unsigned old = 0;
      if (lane == 0) old = __hip_atomic_fetch_add(cnt, 1u, __ATOMIC_RELAXED, __HIP_MEMORY_SCOPE_WORKGROUP);
      old = __builtin_amdgcn_readfirstlane(old);
      asm volatile("" ::: "memory");
      if (old == 3u) {
        const float* red = lds + L_RQ + (g * 16 + (c >> 2)) * 16 + 4 * ch;
        f32x4 a = *(const f32x4*)(red);
#pragma unroll
        for (int p = 1; p < 4; ++p) a += *(const f32x4*)(red + p * 64);
#pragma unroll
        for (int i = 0; i < 4; ++i) a[i] = x32_sum(x16_sum(a[i]));
        if (g == 0) {
          const v4u qv = {pack2(a[0], a[1]), T, pack2(a[2], a[3]), T};
          __builtin_amdgcn_raw_buffer_store_b128(qv, rs, WS_QG + ((c >> 2) * 1024 + hk * 128 + idx * 4 + ch) * 16, 0, AUX_SC1);
        }
      }
    }
    STAMP(1);
    {
      float xv[8], gv[8];
#pragma unroll
      for (int k = 0; k < 4; ++k) { xv[2 * k] = lo2f(xs[k]); xv[2 * k + 1] = hi2f(xs[k]); gv[2 * k] = lo2f(gwv[k]); gv[2 * k + 1] = hi2f(gwv[k]); }
      float s = 0.f, gs = 0.f;
#pragma unroll
      for (int k = 0; k < 8; ++k) { s += xv[k]; gs += bfr(xv[k] * gv[k]); }
      s = wave_sum(s); gs = wave_sum(gs);
      const float mean = s * (1.0f / DM);
      float vs = 0.f;
#pragma unroll
      for (int k = 0; k < 8; ++k) { const float d = xv[k] - mean; vs = fmaf(d, d, vs); }
      vs = wave_sum(vs);
      if (lane == 0) {
        const float var_b = bfr(vs * (1.0f / DM));
        const float den = bfr(sqrtf(bfr(var_b + 1e-5f)));
        const float gt = bfr(1.0f / (1.0f + __expf(-bfr(gs))));
        *(f32x4*)(lds + L_ST + 4 * pw) = f32x4{bfr(mean), den, gt, bfr(1.0f - gt)};
      }
    }
    BAR();
    STAMP(2);
    const __amdgpu_buffer_rsrc_t rw1 = __builtin_amdgcn_make_buffer_rsrc((void*)w1, 0, has_mlp ? DM * FF * 2 : 0, 0x00020000);
    const __amdgpu_buffer_rsrc_t rb1 = __builtin_amdgcn_make_buffer_rsrc((void*)b1, 0, has_mlp ? FF * 2 : 0, 0x00020000);
    const __amdgpu_buffer_rsrc_t rw2 = __builtin_amdgcn_make_buffer_rsrc((void*)w2, 0, has_mlp ? DM * FF * 2 : 0, 0x00020000);
    {
      const int o = (krow * FF + f0 + 4 * ch) * 2;
#pragma unroll
      for (int j = 0; j < 8; ++j) wv[j] = __builtin_amdgcn_raw_buffer_load_b64(rw1, o + j * FF * 2, 0, AUX_NT);
    }
    const v2u b1v = __builtin_amdgcn_raw_buffer_load_b64(rb1, (f0 + 4 * ch) * 2, 0, 0);
    v2u wov[8], w2v[4];
    {
      const u16* p = wo + (size_t)(32 * kg + 8 * g) * DM + 256 * cg + 64 * pw + 4 * c;
#pragma unroll
      for (int j = 0; j < 8; ++j) wov[j] = ldnt<v2u>(p + j * DM);
      const int o2 = ((f0 + 4 * g) * DM + 256 * cg + 64 * pw + 4 * c) * 2;
#pragma unroll
      for (int j = 0; j < 4; ++j) w2v[j] = __builtin_amdgcn_raw_buffer_load_b64(rw2, o2 + j * DM * 2, 0, AUX_NT);
    }
    PIN();
    v4u ya;
    {
      const f32x4 st = *(const f32x4*)(lds + L_ST + 4 * (c & 3));
#pragma unroll
      for (int k = 0; k < 4; ++k) {
        const float y0 = bfr(bfr(lo2f(xa[k]) - st[0]) / st[1]), y1 = bfr(bfr(hi2f(xa[k]) - st[0]) / st[1]);
        ya[k] = pack2(y0, y1);
      }
    }
    const f32x4 stg = *(const f32x4*)(lds + L_ST + 4 * g);
    STAMP(3);
    BAR();
    STAMP(5);
    {
      f32x4 d[4];
#pragma unroll
      for (int i = 0; i < 4; ++i) d[i] = mfma32(ya, tr8x4(wv, i), f32x4{0.f, 0.f, 0.f, 0.f});
      if (g == kc) {
#pragma unroll
        for (int r = 0; r < 4; ++r)
          *(f32x4*)(lds + L_RH + ((pw * 4 + kc) * 4 + r) * 16 + 4 * ch) = f32x4{d[0][r], d[1][r], d[2][r], d[3][r]};
      }
      asm volatile("s_waitcnt lgkmcnt(0)" ::: "memory");
      unsigned old = 0;
      if (lane == 0) old = __hip_atomic_fetch_add(cnt + 1, 1u, __ATOMIC_RELAXED, __HIP_MEMORY_SCOPE_WORKGROUP);
      old = __builtin_amdgcn_readfirstlane(old);
      asm volatile("" ::: "memory");
      if (old == 3u) {
        const float* red = lds + L_RH + (g * 16 + (c >> 2)) * 16 + 4 * ch;
        f32x4 a = *(const f32x4*)(red);
#pragma unroll
        for (int p = 1; p < 4; ++p) a += *(const f32x4*)(red + p * 64);
#pragma unroll
        for (int i = 0; i < 4; ++i) a[i] = x32_sum(x16_sum(a[i]));
        const float bb[4] = {lo2f(b1v[0]), hi2f(b1v[0]), lo2f(b1v[1]), hi2f(b1v[1])};
        const float av = g == 0 ? a[0] : g == 1 ? a[1] : g == 2 ? a[2] : a[3];
        const float bv = g == 0 ? bb[0] : g == 1 ? bb[1] : g == 2 ? bb[2] : bb[3];
        const float u = bfr(bfr(av) + bv);
        const float inner = 0.7978845608028654f * (u + 0.044715f * u * u * u);
        const float hv = 0.5f * u * (1.0f + tanhf(inner));
        ((u16*)(lds + L_HB))[(c >> 2) * 16 + 4 * ch + g] = (u16)bfbits(hv);
      }
    }
    const int h2 = 4 * hk + (kgl >> 2);
    const unsigned p2off = WS_P2 + (((h2 * 4 + (kgl & 3)) * 4 + pw) * 8 + (lane >> 3)) * 256 + (lane & 7) * 32;
    const unsigned mloff = WS_ML + ((h2 * 4 + pw) * 8 + (lane >> 3)) * 16;
    v4u wof[4];
    v2u w2f[4];
#pragma unroll
    for (int i = 0; i < 4; ++i) {
      wof[i] = tr8x4(wov, i);
#pragma unroll
      for (int jj = 0; jj < 2; ++jj)
        w2f[i][jj] = (i & 1) ? perm_hi(w2v[2 * jj][i >> 1], w2v[2 * jj + 1][i >> 1]) : perm_lo(w2v[2 * jj][i >> 1], w2v[2 * jj + 1][i >> 1]);
    }
    PIN();
    STAMP(6);
    v4u a0 = __builtin_amdgcn_raw_buffer_load_b128(rs, p2off, 0, AUX_SC1);
    v4u a1 = __builtin_amdgcn_raw_buffer_load_b128(rs, p2off + 16, 0, AUX_SC1);
    v4u am = __builtin_amdgcn_raw_buffer_load_b128(rs, mloff, 0, AUX_SC1);
    for (unsigned spins = 0; spins < SPIN_CAP; ++spins) {
      const bool ok = ((a0[1] ^ T) | (a0[3] ^ T) | (a1[1] ^ T) | (a1[3] ^ T) | (am[1] ^ T) | (am[3] ^ T)) == 0;
      if (__all(ok)) break;
      asm volatile("" ::: "memory");
      if (!ok) {
        a0 = __builtin_amdgcn_raw_buffer_load_b128(rs, p2off, 0, AUX_SC1);
        a1 = __builtin_amdgcn_raw_buffer_load_b128(rs, p2off + 16, 0, AUX_SC1);
        am = __builtin_amdgcn_raw_buffer_load_b128(rs, mloff, 0, AUX_SC1);
      }
    }
    STAMP(7);
    {
      const float m = __uint_as_float(am[0]), l = __uint_as_float(am[2]);
      float M = fmaxf(m, dpp<ROR8>(m)); M = x32_max(x16_max(M));
      const float f = __builtin_amdgcn_exp2f((m - M) * LOG2E);
      float v[5] = {f * __uint_as_float(a0[0]), f * __uint_as_float(a0[2]), f * __uint_as_float(a1[0]), f * __uint_as_float(a1[2]), f * l};
#pragma unroll
      for (int i = 0; i < 5; ++i) { v[i] += dpp<ROR8>(v[i]); v[i] = x32_sum(x16_sum(v[i])); }
      const float inv = 1.0f / v[4];
      if (lane < 8) *(v2u*)((u16*)(lds + L_OB) + pw * 32 + 4 * lane) = v2u{pack2(v[0] * inv, v[1] * inv), pack2(v[2] * inv, v[3] * inv)};
    }
    BAR();
    {
      const v4u oa = *(const v4u*)((const u16*)(lds + L_OB) + (c >> 2) * 32 + 8 * g);
      const v2u ha = *(const v2u*)((const u16*)(lds + L_HB) + (c >> 2) * 16 + 4 * g);
      float val[4];
#pragma unroll
      for (int i = 0; i < 4; ++i) {
        const f32x4 d = mfma32(oa, wof[i], f32x4{0.f, 0.f, 0.f, 0.f});
        const f32x4 e = mfma16(ha, w2f[i], f32x4{0.f, 0.f, 0.f, 0.f});
        val[i] = fmaf(stg[2], d[0], stg[3] * e[0]);
      }
      const unsigned r = g * 64 + 32 * cg + 8 * pw + (c >> 1);
      const unsigned off = WS_P3 + (r * 128 + kg) * 64 + (c & 1) * 16;
      const v4u s0 = {__float_as_uint(val[0]), T, __float_as_uint(val[1]), T};
      const v4u s1 = {__float_as_uint(val[2]), T, __float_as_uint(val[3]), T};
      __builtin_amdgcn_raw_buffer_store_b128(s0, rs, off, 0, AUX_SC1);
      __builtin_amdgcn_raw_buffer_store_b128(s1, rs, off + 32, 0, AUX_SC1);
    }
    STAMP(8);
  }
}

extern "C" void kernel_launch(void* const* d_in, const int* in_sizes, int n_in,
                              void* d_out, int out_size, void* d_ws, size_t ws_size,
                              hipStream_t stream) {
  (void)in_sizes; (void)n_in; (void)out_size; (void)ws_size;
  fused_kernel<<<256, 512, 0, stream>>>(
      (const u16*)d_in[0], (const u16*)d_in[1], (const u16*)d_in[2], (const u16*)d_in[3], (const u16*)d_in[4],
      (const u16*)d_in[6], (const u16*)d_in[7], (const u16*)d_in[8], (const u16*)d_in[9], (const u16*)d_in[10],
      (u16*)d_out, (unsigned char*)d_ws);
}
```

```cpp
#include <hip/hip_runtime.h>
#include <hip/hip_bf16.h>
#include <stdint.h>

#define GAS __attribute__((address_space(1)))
#define LAS __attribute__((address_space(3)))
typedef unsigned short u16;
typedef unsigned v4u __attribute__((ext_vector_type(4)));
typedef unsigned v2u __attribute__((ext_vector_type(2)));
typedef float f32x4 __attribute__((ext_vector_type(4)));
typedef float f32x2 __attribute__((ext_vector_type(2)));
typedef __bf16 bf16x8 __attribute__((ext_vector_type(8)));
typedef short s16x4 __attribute__((ext_vector_type(4)));
typedef GAS unsigned gu32;

constexpr int DM = 512, SK = 4096, DH = 128, WINDOW = 1024, FF = 1792, NQ = 4096;
constexpr unsigned WS_QG = 4096;
constexpr unsigned WS_P2 = WS_QG + 65536;
constexpr unsigned WS_ML = WS_P2 + (1u << 20);
constexpr unsigned WS_P3 = WS_ML + 16384;
constexpr unsigned WS_END = WS_P3 + (2u << 20);
constexpr unsigned SPIN_CAP = 1u << 17;
constexpr int AUX_SC1 = 16, AUX_NT = 2;
constexpr float LOG2E = 1.4426950408889634f;

__device__ __forceinline__ float bfr(float f) { return (float)(__bf16)f; }
__device__ __forceinline__ unsigned bfbits(float f) { return (unsigned)__builtin_bit_cast(u16, (__bf16)f); }
__device__ __forceinline__ unsigned pack2(float lo, float hi) { return bfbits(lo) | (bfbits(hi) << 16); }
__device__ __forceinline__ float lo2f(unsigned u) { return __builtin_bit_cast(float, u << 16); }
__device__ __forceinline__ float hi2f(unsigned u) { return __builtin_bit_cast(float, u & 0xffff0000u); }
__device__ __forceinline__ unsigned perm_lo(unsigned a, unsigned b) { return __builtin_amdgcn_perm(b, a, 0x05040100u); }
__device__ __forceinline__ unsigned perm_hi(unsigned a, unsigned b) { return __builtin_amdgcn_perm(b, a, 0x07060302u); }

template <int CTRL> __device__ __forceinline__ float dpp(float x) {
  return __builtin_bit_cast(float, __builtin_amdgcn_mov_dpp(__builtin_bit_cast(int, x), CTRL, 0xf, 0xf, true));
}
constexpr int XOR1 = 0xB1, XOR2 = 0x4E, ROR8 = 0x128, ROR4 = 0x124;
__device__ __forceinline__ float x16_max(float x) {
  auto s = __builtin_amdgcn_permlane16_swap(__float_as_uint(x), __float_as_uint(x), false, false);
  return fmaxf(__uint_as_float(s[0]), __uint_as_float(s[1]));
}
__device__ __forceinline__ float x32_max(float x) {
  auto s = __builtin_amdgcn_permlane32_swap(__float_as_uint(x), __float_as_uint(x), false, false);
  return fmaxf(__uint_as_float(s[0]), __uint_as_float(s[1]));
}
__device__ __forceinline__ float x16_sum(float x) {
  auto s = __builtin_amdgcn_permlane16_swap(__float_as_uint(x), __float_as_uint(x), false, false);
  return __uint_as_float(s[0]) + __uint_as_float(s[1]);
}
__device__ __forceinline__ float x32_sum(float x) {
  auto s = __builtin_amdgcn_permlane32_swap(__float_as_uint(x), __float_as_uint(x), false, false);
  return __uint_as_float(s[0]) + __uint_as_float(s[1]);
}
__device__ __forceinline__ float wave_sum(float x) {
  x += dpp<XOR1>(x); x += dpp<XOR2>(x); x += dpp<ROR4>(x); x += dpp<ROR8>(x);
  return x32_sum(x16_sum(x));
}
__device__ __forceinline__ f32x4 mfma32(v4u a, v4u b, f32x4 c) {
  return __builtin_amdgcn_mfma_f32_16x16x32_bf16(__builtin_bit_cast(bf16x8, a), __builtin_bit_cast(bf16x8, b), c, 0, 0, 0);
}
__device__ __forceinline__ f32x4 mfma16(v2u a, v2u b, f32x4 c) {
  return __builtin_amdgcn_mfma_f32_16x16x16bf16_1k(__builtin_bit_cast(s16x4, a), __builtin_bit_cast(s16x4, b), c, 0, 0, 0);
}
#define BAR() do { asm volatile("s_waitcnt lgkmcnt(0)" ::: "memory"); __builtin_amdgcn_s_barrier(); asm volatile("" ::: "memory"); } while (0)
#define PIN() __builtin_amdgcn_sched_barrier(0)
#define STAMP(i)

template <typename T> __device__ __forceinline__ T ldg(const u16* p) { return *(const GAS T*)p; }
template <typename T> __device__ __forceinline__ T ldnt(const u16* p) { return __builtin_nontemporal_load((const GAS T*)p); }

constexpr int L_ST = 0;
constexpr int L_CNT = 16;
constexpr int L_RQ = 32;
constexpr int L_RH = L_RQ + 1024;
constexpr int L_HB = L_RH + 1024;
constexpr int L_MO = L_HB + 32;
constexpr int L_ML = L_MO + 2048;
constexpr int L_OB = L_ML + 32;
constexpr int L_END = L_OB + 64;

__device__ __forceinline__ v4u tr8x4(const v2u (&W)[8], int i) {
  v4u f;
#pragma unroll
  for (int jj = 0; jj < 4; ++jj)
    f[jj] = (i & 1) ? perm_hi(W[2 * jj][i >> 1], W[2 * jj + 1][i >> 1]) : perm_lo(W[2 * jj][i >> 1], W[2 * jj + 1][i >> 1]);
  return f;
}

__global__ void __launch_bounds__(512, 2)
fused_kernel(const u16* __restrict__ x, const u16* __restrict__ kcache, const u16* __restrict__ vcache,
             const u16* __restrict__ wq, const u16* __restrict__ wo, const u16* __restrict__ w1,
             const u16* __restrict__ b1, const u16* __restrict__ w2, const u16* __restrict__ b2,
             const u16* __restrict__ gw, u16* __restrict__ out, unsigned char* ws) {
  __shared__ __attribute__((aligned(16))) float lds[L_END];
  const int tid = threadIdx.x, lane = tid & 63, w = __builtin_amdgcn_readfirstlane(tid >> 6);
  const int g = lane >> 4, c = lane & 15;
  const int bid = blockIdx.x, hk = bid & 7, idx = bid >> 3;
  const int ab = idx >> 3, as = idx & 7;
  const int kgl = idx >> 1, cg = idx & 1, kg = hk * 16 + kgl;
  const __amdgpu_buffer_rsrc_t rs = __builtin_amdgcn_make_buffer_rsrc((void*)ws, 0, (int)WS_END, 0x00020000);
  LAS unsigned* cnt = (LAS unsigned*)(lds + L_CNT);
  STAMP(0);
  const unsigned T = *(const GAS unsigned*)ws + 1u;

  if (w >= 4) {
    const int aw = w - 4;
    BAR();
    v4u kf[8], vf[8];
    {
      const size_t kvoff = ((size_t)(ab * 8 + hk) * SK + (SK - WINDOW) + 128 * as + 32 * aw) * DH;
      const u16* kp = kcache + kvoff + c * DH + 8 * g;
#pragma unroll
      for (int t = 0; t < 2; ++t)
#pragma unroll
        for (int ks = 0; ks < 4; ++ks) kf[t * 4 + ks] = ldnt<v4u>(kp + t * 16 * DH + 32 * ks);
      const u16* vp = vcache + kvoff + 4 * g * DH + 8 * c;
#pragma unroll
      for (int j = 0; j < 8; ++j) vf[j] = ldnt<v4u>(vp + (16 * (j >> 2) + (j & 3)) * DH);
    }
    PIN();
    STAMP(1);
    BAR();
    STAMP(2);
    asm volatile("" :: "v"(kf[7]));
    PIN();
    STAMP(3);
    v4u qraw[8];
    const unsigned qoff = WS_QG + ((hk * 32 + (c >> 2) * 8 + (g >> 1)) * 4 + ab) * 64 + (g & 1) * 32;
#pragma unroll
    for (int ks = 0; ks < 4; ++ks) {
      qraw[2 * ks] = __builtin_amdgcn_raw_buffer_load_b128(rs, qoff + ks * 512, 0, AUX_SC1);
      qraw[2 * ks + 1] = __builtin_amdgcn_raw_buffer_load_b128(rs, qoff + ks * 512 + 16, 0, AUX_SC1);
    }
    PIN();
    v4u vt[8];
#pragma unroll
    for (int i = 0; i < 8; ++i)
#pragma unroll
      for (int jj = 0; jj < 4; ++jj)
        vt[i][jj] = (i & 1) ? perm_hi(vf[2 * jj][i >> 1], vf[2 * jj + 1][i >> 1]) : perm_lo(vf[2 * jj][i >> 1], vf[2 * jj + 1][i >> 1]);
    PIN();
    for (unsigned spins = 0; spins < SPIN_CAP; ++spins) {
      unsigned bad = 0;
#pragma unroll
      for (int i = 0; i < 8; ++i) bad |= (qraw[i][1] ^ T) | (qraw[i][3] ^ T);
      if (__all(bad == 0)) break;
      asm volatile("" ::: "memory");
#pragma unroll
      for (int ks = 0; ks < 4; ++ks) {
        qraw[2 * ks] = __builtin_amdgcn_raw_buffer_load_b128(rs, qoff + ks * 512, 0, AUX_SC1);
        qraw[2 * ks + 1] = __builtin_amdgcn_raw_buffer_load_b128(rs, qoff + ks * 512 + 16, 0, AUX_SC1);
      }
    }
    STAMP(4);
    {
      f32x4 s0 = {0.f, 0.f, 0.f, 0.f}, s1 = {0.f, 0.f, 0.f, 0.f};
#pragma unroll
      for (int ks = 0; ks < 4; ++ks) {
        const v4u qf = {qraw[2 * ks][0], qraw[2 * ks][2], qraw[2 * ks + 1][0], qraw[2 * ks + 1][2]};
        s0 = mfma32(kf[ks], qf, s0);
        s1 = mfma32(kf[4 + ks], qf, s1);
      }
      float sc[8];
#pragma unroll
      for (int j = 0; j < 4; ++j) { sc[j] = bfr(bfr(s0[j]) * 0.08838834764831845f); sc[4 + j] = bfr(bfr(s1[j]) * 0.08838834764831845f); }
      float m = fmaxf(fmaxf(fmaxf(sc[0], sc[1]), fmaxf(sc[2], sc[3])), fmaxf(fmaxf(sc[4], sc[5]), fmaxf(sc[6], sc[7])));
      m = x32_max(x16_max(m));
      float p[8], l = 0.f;
#pragma unroll
      for (int j = 0; j < 8; ++j) { p[j] = __builtin_amdgcn_exp2f((sc[j] - m) * LOG2E); l += p[j]; }
      const v4u pf = {pack2(p[0], p[1]), pack2(p[2], p[3]), pack2(p[4], p[5]), pack2(p[6], p[7])};
      float ov[8];
#pragma unroll
      for (int i = 0; i < 8; ++i) { const f32x4 o = mfma32(pf, vt[i], f32x4{0.f, 0.f, 0.f, 0.f}); ov[i] = o[0]; }
      l = x32_sum(x16_sum(l));
      if (g == 0 && (c & 3) == 0) *(f32x2*)(lds + L_ML + (aw * 4 + (c >> 2)) * 2) = f32x2{m, l};
      float* mo = lds + L_MO + (aw * 4 + g) * 128 + 8 * c;
      *(f32x4*)(mo) = f32x4{ov[0], ov[1], ov[2], ov[3]};
      *(f32x4*)(mo + 4) = f32x4{ov[4], ov[5], ov[6], ov[7]};
    }
    BAR();
    STAMP(5);
    {
      f32x2 ml[4], ov[4];
#pragma unroll
      for (int u = 0; u < 4; ++u) { ml[u] = *(const f32x2*)(lds + L_ML + (u * 4 + aw) * 2); ov[u] = *(const f32x2*)(lds + L_MO + (u * 4 + aw) * 128 + 2 * lane); }
      const float M = fmaxf(fmaxf(ml[0][0], ml[1][0]), fmaxf(ml[2][0], ml[3][0]));
      float L = 0.f, o0 = 0.f, o1 = 0.f;
#pragma unroll
      for (int u = 0; u < 4; ++u) {
        const float f = __builtin_amdgcn_exp2f((ml[u][0] - M) * LOG2E);
        L = fmaf(f, ml[u][1], L); o0 = fmaf(f, ov[u][0], o0); o1 = fmaf(f, ov[u][1], o1);
      }
      const int hg = 4 * hk + aw;
      const v4u pv = {__float_as_uint(o0), T, __float_as_uint(o1), T};
      __builtin_amdgcn_raw_buffer_store_b128(pv, rs, WS_P2 + (((hg * 4 + (lane >> 4)) * 4 + ab) * 8 + as) * 256 + (lane & 15) * 16, 0, AUX_SC1);
      if (lane == 0) {
        const v4u mv = {__float_as_uint(M), T, __float_as_uint(L), T};
        __builtin_amdgcn_raw_buffer_store_b128(mv, rs, WS_ML + ((hg * 4 + ab) * 8 + as) * 16, 0, AUX_SC1);
      }
    }
    STAMP(6);
    unsigned b2v = 0;
    const int ob = bid >> 6, ocw = (bid >> 3) & 7, om = (bid >> 1) & 3, oh = bid & 1;
    const int col = 64 * ocw + 4 * (4 * om + (lane & 3)) + 2 * oh;
    if (aw == 0) b2v = ldg<unsigned>(b2 + col);
    BAR();
    if (aw == 0) {
      const float omg = lds[L_ST + 4 * ob + 3];
      const unsigned off = WS_P3 + ((lane >> 2) * 8 + ocw) * 2048 + oh * 1024 + (ob * 16 + 4 * om + (lane & 3)) * 16;
      v4u pv[8];
#pragma unroll
      for (int i = 0; i < 8; ++i) pv[i] = __builtin_amdgcn_raw_buffer_load_b128(rs, off + i * (16 * 8 * 2048), 0, AUX_SC1);
      for (unsigned spins = 0; spins < SPIN_CAP; ++spins) {
        unsigned bad = 0;
#pragma unroll
        for (int i = 0; i < 8; ++i) bad |= (pv[i][1] ^ T) | (pv[i][3] ^ T);
        if (__all(bad == 0)) break;
        asm volatile("" ::: "memory");
#pragma unroll
        for (int i = 0; i < 8; ++i)
          if (((pv[i][1] ^ T) | (pv[i][3] ^ T)) != 0) pv[i] = __builtin_amdgcn_raw_buffer_load_b128(rs, off + i * (16 * 8 * 2048), 0, AUX_SC1);
      }
      STAMP(9);
      float sa = 0.f, sb = 0.f;
#pragma unroll
      for (int i = 0; i < 8; ++i) { sa += __uint_as_float(pv[i][0]); sb += __uint_as_float(pv[i][2]); }
      sa += dpp<ROR4>(sa); sa += dpp<ROR8>(sa); sa = x32_sum(x16_sum(sa));
      sb += dpp<ROR4>(sb); sb += dpp<ROR8>(sb); sb = x32_sum(x16_sum(sb));
      if (lane < 4) {
        const float o0 = fmaf(omg, lo2f(b2v), sa), o1 = fmaf(omg, hi2f(b2v), sb);
        *(GAS unsigned*)(out + ob * DM + col) = pack2(o0, o1);
      }
      if (bid == 0 && lane == 0) __hip_atomic_store((gu32*)ws, T, __ATOMIC_RELAXED, __HIP_MEMORY_SCOPE_AGENT);
    }
  } else {
    const int pw = w;
    __builtin_amdgcn_s_setprio(3);
    const bool has_mlp = kgl < 14;
    const int f0 = 224 * hk + 16 * kgl;
    const int kc = c >> 2, ch = c & 3;
    const int krow = 128 * pw + 32 * kc + 8 * g;
    v2u wv[8];
    {
      const u16* p = wq + (size_t)krow * NQ + hk * 512 + idx * 16 + 4 * ch;
#pragma unroll
      for (int j = 0; j < 8; ++j) wv[j] = ldnt<v2u>(p + j * NQ);
    }
    const v4u xa = ldg<v4u>(x + (c & 3) * DM + krow);
    const v4u xs = ldg<v4u>(x + pw * DM + 8 * lane);
    const v4u gwv = ldg<v4u>(gw + 8 * lane);
    if (tid == 0) { cnt[0] = 0u; cnt[1] = 0u; }
    PIN();
    BAR();
    {
      f32x4 d[4];
#pragma unroll
      for (int i = 0; i < 4; ++i) d[i] = mfma32(xa, tr8x4(wv, i), f32x4{0.f, 0.f, 0.f, 0.f});
      if (g == kc) {
#pragma unroll
        for (int r = 0; r < 4; ++r)
          *(f32x4*)(lds + L_RQ + ((pw * 4 + kc) * 4 + r) * 16 + 4 * ch) = f32x4{d[0][r], d[1][r], d[2][r], d[3][r]};
      }
      asm volatile("s_waitcnt lgkmcnt(0)" ::: "memory");
      unsigned old = 0;
      if (lane == 0) old = __hip_atomic_fetch_add(cnt, 1u, __ATOMIC_RELAXED, __HIP_MEMORY_SCOPE_WORKGROUP);
      old = __builtin_amdgcn_readfirstlane(old);
      asm volatile("" ::: "memory");
      if (old == 3u) {
        const float* red = lds + L_RQ + (g * 16 + (c >> 2)) * 16 + 4 * ch;
        f32x4 a = *(const f32x4*)(red);
#pragma unroll
        for (int p = 1; p < 4; ++p) a += *(const f32x4*)(red + p * 64);
#pragma unroll
        for (int i = 0; i < 4; ++i) a[i] = x32_sum(x16_sum(a[i]));
        if (g == 0) {
          const v4u qv = {pack2(a[0], a[1]), T, pack2(a[2], a[3]), T};
          __builtin_amdgcn_raw_buffer_store_b128(qv, rs, WS_QG + (hk * 32 + idx) * 256 + c * 16, 0, AUX_SC1);
        }
      }
    }
    STAMP(1);
    {
      float xv[8], gv[8];
#pragma unroll
      for (int k = 0; k < 4; ++k) { xv[2 * k] = lo2f(xs[k]); xv[2 * k + 1] = hi2f(xs[k]); gv[2 * k] = lo2f(gwv[k]); gv[2 * k + 1] = hi2f(gwv[k]); }
      float s = 0.f, gs = 0.f;
#pragma unroll
      for (int k = 0; k < 8; ++k) { s += xv[k]; gs += bfr(xv[k] * gv[k]); }
      s = wave_sum(s); gs = wave_sum(gs);
      const float mean = s * (1.0f / DM);
      float vs = 0.f;
#pragma unroll
      for (int k = 0; k < 8; ++k) { const float d = xv[k] - mean; vs = fmaf(d, d, vs); }
      vs = wave_sum(vs);
      if (lane == 0) {
        const float var_b = bfr(vs * (1.0f / DM));
        const float den = bfr(sqrtf(bfr(var_b + 1e-5f)));
        const float gt = bfr(1.0f / (1.0f + __expf(-bfr(gs))));
        *(f32x4*)(lds + L_ST + 4 * pw) = f32x4{bfr(mean), den, gt, bfr(1.0f - gt)};
      }
    }
    BAR();
    STAMP(2);
    const __amdgpu_buffer_rsrc_t rw1 = __builtin_amdgcn_make_buffer_rsrc((void*)w1, 0, has_mlp ? DM * FF * 2 : 0, 0x00020000);
    const __amdgpu_buffer_rsrc_t rb1 = __builtin_amdgcn_make_buffer_rsrc((void*)b1, 0, has_mlp ? FF * 2 : 0, 0x00020000);
    const __amdgpu_buffer_rsrc_t rw2 = __builtin_amdgcn_make_buffer_rsrc((void*)w2, 0, has_mlp ? DM * FF * 2 : 0, 0x00020000);
    {
      const int o = (krow * FF + f0 + 4 * ch) * 2;
#pragma unroll
      for (int j = 0; j < 8; ++j) wv[j] = __builtin_amdgcn_raw_buffer_load_b64(rw1, o + j * FF * 2, 0, AUX_NT);
    }
    const v2u b1v = __builtin_amdgcn_raw_buffer_load_b64(rb1, (f0 + 4 * ch) * 2, 0, 0);
    v2u wov[8], w2v[4];
    {
      const u16* p = wo + (size_t)(32 * kg + 8 * g) * DM + 256 * cg + 64 * pw + 4 * c;
#pragma unroll
      for (int j = 0; j < 8; ++j) wov[j] = ldnt<v2u>(p + j * DM);
      const int o2 = ((f0 + 4 * g) * DM + 256 * cg + 64 * pw + 4 * c) * 2;
#pragma unroll
      for (int j = 0; j < 4; ++j) w2v[j] = __builtin_amdgcn_raw_buffer_load_b64(rw2, o2 + j * DM * 2, 0, AUX_NT);
    }
    PIN();
    v4u ya;
    {
      const f32x4 st = *(const f32x4*)(lds + L_ST + 4 * (c & 3));
#pragma unroll
      for (int k = 0; k < 4; ++k) {
        const float y0 = bfr(bfr(lo2f(xa[k]) - st[0]) / st[1]), y1 = bfr(bfr(hi2f(xa[k]) - st[0]) / st[1]);
        ya[k] = pack2(y0, y1);
      }
    }
    const f32x4 stg = *(const f32x4*)(lds + L_ST + 4 * g);
    STAMP(3);
    BAR();
    STAMP(5);
    {
      f32x4 d[4];
#pragma unroll
      for (int i = 0; i < 4; ++i) d[i] = mfma32(ya, tr8x4(wv, i), f32x4{0.f, 0.f, 0.f, 0.f});
      if (g == kc) {
#pragma unroll
        for (int r = 0; r < 4; ++r)
          *(f32x4*)(lds + L_RH + ((pw * 4 + kc) * 4 + r) * 16 + 4 * ch) = f32x4{d[0][r], d[1][r], d[2][r], d[3][r]};
      }
      asm volatile("s_waitcnt lgkmcnt(0)" ::: "memory");
      unsigned old = 0;
      if (lane == 0) old = __hip_atomic_fetch_add(cnt + 1, 1u, __ATOMIC_RELAXED, __HIP_MEMORY_SCOPE_WORKGROUP);
      old = __builtin_amdgcn_readfirstlane(old);
      asm volatile("" ::: "memory");
      if (old == 3u) {
        const float* red = lds + L_RH + (g * 16 + (c >> 2)) * 16 + 4 * ch;
        f32x4 a = *(const f32x4*)(red);
#pragma unroll
        for (int p = 1; p < 4; ++p) a += *(const f32x4*)(red + p * 64);
#pragma unroll
        for (int i = 0; i < 4; ++i) a[i] = x32_sum(x16_sum(a[i]));
        const float bb[4] = {lo2f(b1v[0]), hi2f(b1v[0]), lo2f(b1v[1]), hi2f(b1v[1])};
        const float av = g == 0 ? a[0] : g == 1 ? a[1] : g == 2 ? a[2] : a[3];
        const float bv = g == 0 ? bb[0] : g == 1 ? bb[1] : g == 2 ? bb[2] : bb[3];
        const float u = bfr(bfr(av) + bv);
        const float inner = 0.7978845608028654f * (u + 0.044715f * u * u * u);
        const float hv = 0.5f * u * (1.0f + tanhf(inner));
        ((u16*)(lds + L_HB))[(c >> 2) * 16 + 4 * ch + g] = (u16)bfbits(hv);
      }
    }
    const int h2 = 4 * hk + (kgl >> 2);
    const unsigned p2off = WS_P2 + (((h2 * 4 + (kgl & 3)) * 4 + pw) * 8 + (lane >> 3)) * 256 + (lane & 7) * 32;
    const unsigned mloff = WS_ML + ((h2 * 4 + pw) * 8 + (lane >> 3)) * 16;
    v4u wof[4];
    v2u w2f[4];
#pragma unroll
    for (int i = 0; i < 4; ++i) {
      wof[i] = tr8x4(wov, i);
#pragma unroll
      for (int jj = 0; jj < 2; ++jj)
        w2f[i][jj] = (i & 1) ? perm_hi(w2v[2 * jj][i >> 1], w2v[2 * jj + 1][i >> 1]) : perm_lo(w2v[2 * jj][i >> 1], w2v[2 * jj + 1][i >> 1]);
    }
    PIN();
    STAMP(6);
    v4u a0 = __builtin_amdgcn_raw_buffer_load_b128(rs, p2off, 0, AUX_SC1);
    v4u a1 = __builtin_amdgcn_raw_buffer_load_b128(rs, p2off + 16, 0, AUX_SC1);
    v4u am = __builtin_amdgcn_raw_buffer_load_b128(rs, mloff, 0, AUX_SC1);
    for (unsigned spins = 0; spins < SPIN_CAP; ++spins) {
      const bool ok = ((a0[1] ^ T) | (a0[3] ^ T) | (a1[1] ^ T) | (a1[3] ^ T) | (am[1] ^ T) | (am[3] ^ T)) == 0;
      if (__all(ok)) break;
      asm volatile("" ::: "memory");
      if (!ok) {
        a0 = __builtin_amdgcn_raw_buffer_load_b128(rs, p2off, 0, AUX_SC1);
        a1 = __builtin_amdgcn_raw_buffer_load_b128(rs, p2off + 16, 0, AUX_SC1);
        am = __builtin_amdgcn_raw_buffer_load_b128(rs, mloff, 0, AUX_SC1);
      }
    }
    STAMP(7);
    {
      const float m = __uint_as_float(am[0]), l = __uint_as_float(am[2]);
      float M = fmaxf(m, dpp<ROR8>(m)); M = x32_max(x16_max(M));
      const float f = __builtin_amdgcn_exp2f((m - M) * LOG2E);
      float v[5] = {f * __uint_as_float(a0[0]), f * __uint_as_float(a0[2]), f * __uint_as_float(a1[0]), f * __uint_as_float(a1[2]), f * l};
#pragma unroll
      for (int i = 0; i < 5; ++i) { v[i] += dpp<ROR8>(v[i]); v[i] = x32_sum(x16_sum(v[i])); }
      const float inv = 1.0f / v[4];
      if (lane < 8) *(v2u*)((u16*)(lds + L_OB) + pw * 32 + 4 * lane) = v2u{pack2(v[0] * inv, v[1] * inv), pack2(v[2] * inv, v[3] * inv)};
    }
    BAR();
    {
      const v4u oa = *(const v4u*)((const u16*)(lds + L_OB) + (c >> 2) * 32 + 8 * g);
      const v2u ha = *(const v2u*)((const u16*)(lds + L_HB) + (c >> 2) * 16 + 4 * g);
      float val[4];
#pragma unroll
      for (int i = 0; i < 4; ++i) {
        const f32x4 d = mfma32(oa, wof[i], f32x4{0.f, 0.f, 0.f, 0.f});
        const f32x4 e = mfma16(ha, w2f[i], f32x4{0.f, 0.f, 0.f, 0.f});
        val[i] = fmaf(stg[2], d[0], stg[3] * e[0]);
      }
      const unsigned off = WS_P3 + ((kg * 2 + cg) * 4 + pw) * 2048 + lane * 16;
      const v4u s0 = {__float_as_uint(val[0]), T, __float_as_uint(val[1]), T};
      const v4u s1 = {__float_as_uint(val[2]), T, __float_as_uint(val[3]), T};
      __builtin_amdgcn_raw_buffer_store_b128(s0, rs, off, 0, AUX_SC1);
      __builtin_amdgcn_raw_buffer_store_b128(s1, rs, off + 1024, 0, AUX_SC1);
    }
    STAMP(8);
  }
}

extern "C" void kernel_launch(void* const* d_in, const int* in_sizes, int n_in,
                              void* d_out, int out_size, void* d_ws, size_t ws_size,
                              hipStream_t stream) {
  (void)in_sizes; (void)n_in; (void)out_size; (void)ws_size;
  fused_kernel<<<256, 512, 0, stream>>>(
      (const u16*)d_in[0], (const u16*)d_in[1], (const u16*)d_in[2], (const u16*)d_in[3], (const u16*)d_in[4],
      (const u16*)d_in[6], (const u16*)d_in[7], (const u16*)d_in[8], (const u16*)d_in[9], (const u16*)d_in[10],
      (u16*)d_out, (unsigned char*)d_ws);
}
```

```cpp
#include <hip/hip_runtime.h>
#include <hip/hip_bf16.h>
#include <stdint.h>

#define GAS __attribute__((address_space(1)))
#define LAS __attribute__((address_space(3)))
typedef unsigned short u16;
typedef unsigned v4u __attribute__((ext_vector_type(4)));
typedef unsigned v2u __attribute__((ext_vector_type(2)));
typedef float f32x4 __attribute__((ext_vector_type(4)));
typedef float f32x2 __attribute__((ext_vector_type(2)));
typedef __bf16 bf16x8 __attribute__((ext_vector_type(8)));
typedef short s16x4 __attribute__((ext_vector_type(4)));
typedef GAS unsigned gu32;

constexpr int DM = 512, SK = 4096, DH = 128, WINDOW = 1024, FF = 1792, NQ = 4096;
constexpr unsigned WS_QG = 4096;
constexpr unsigned WS_P2 = WS_QG + 65536;
constexpr unsigned WS_ML = WS_P2 + (1u << 20);
constexpr unsigned WS_P3 = WS_ML + 16384;
constexpr unsigned WS_END = WS_P3 + (2u << 20);
constexpr unsigned SPIN_CAP = 1u << 17;
constexpr unsigned LSPIN_CAP = 1u << 22;
constexpr int AUX_SC1 = 16, AUX_NT = 2;
constexpr float LOG2E = 1.4426950408889634f;

#define AWIN 6
#define PWIN 3
#define DLY2 8
#define DLY3 10

__device__ __forceinline__ float bfr(float f) { return (float)(__bf16)f; }
__device__ __forceinline__ unsigned bfbits(float f) { return (unsigned)__builtin_bit_cast(u16, (__bf16)f); }
__device__ __forceinline__ unsigned pack2(float lo, float hi) { return bfbits(lo) | (bfbits(hi) << 16); }
__device__ __forceinline__ float lo2f(unsigned u) { return __builtin_bit_cast(float, u << 16); }
__device__ __forceinline__ float hi2f(unsigned u) { return __builtin_bit_cast(float, u & 0xffff0000u); }
__device__ __forceinline__ unsigned perm_lo(unsigned a, unsigned b) { return __builtin_amdgcn_perm(b, a, 0x05040100u); }
__device__ __forceinline__ unsigned perm_hi(unsigned a, unsigned b) { return __builtin_amdgcn_perm(b, a, 0x07060302u); }

template <int CTRL> __device__ __forceinline__ float dpp(float x) {
  return __builtin_bit_cast(float, __builtin_amdgcn_mov_dpp(__builtin_bit_cast(int, x), CTRL, 0xf, 0xf, true));
}
constexpr int XOR1 = 0xB1, XOR2 = 0x4E, ROR8 = 0x128, ROR4 = 0x124;
__device__ __forceinline__ float x16_max(float x) {
  auto s = __builtin_amdgcn_permlane16_swap(__float_as_uint(x), __float_as_uint(x), false, false);
  return fmaxf(__uint_as_float(s[0]), __uint_as_float(s[1]));
}
__device__ __forceinline__ float x32_max(float x) {
  auto s = __builtin_amdgcn_permlane32_swap(__float_as_uint(x), __float_as_uint(x), false, false);
  return fmaxf(__uint_as_float(s[0]), __uint_as_float(s[1]));
}
__device__ __forceinline__ float x16_sum(float x) {
  auto s = __builtin_amdgcn_permlane16_swap(__float_as_uint(x), __float_as_uint(x), false, false);
  return __uint_as_float(s[0]) + __uint_as_float(s[1]);
}
__device__ __forceinline__ float x32_sum(float x) {
  auto s = __builtin_amdgcn_permlane32_swap(__float_as_uint(x), __float_as_uint(x), false, false);
  return __uint_as_float(s[0]) + __uint_as_float(s[1]);
}
__device__ __forceinline__ float wave_sum(float x) {
  x += dpp<XOR1>(x); x += dpp<XOR2>(x); x += dpp<ROR4>(x); x += dpp<ROR8>(x);
  return x32_sum(x16_sum(x));
}
__device__ __forceinline__ f32x4 mfma32(v4u a, v4u b, f32x4 c) {
  return __builtin_amdgcn_mfma_f32_16x16x32_bf16(__builtin_bit_cast(bf16x8, a), __builtin_bit_cast(bf16x8, b), c, 0, 0, 0);
}
__device__ __forceinline__ f32x4 mfma16(v2u a, v2u b, f32x4 c) {
  return __builtin_amdgcn_mfma_f32_16x16x16bf16_1k(__builtin_bit_cast(s16x4, a), __builtin_bit_cast(s16x4, b), c, 0, 0, 0);
}
#define PIN() __builtin_amdgcn_sched_barrier(0)
#define BAR() do { PIN(); asm volatile("s_waitcnt lgkmcnt(0)" ::: "memory"); __builtin_amdgcn_s_barrier(); asm volatile("" ::: "memory"); PIN(); } while (0)
#define KEEP(x) asm volatile("" : "+v"(x))
#define STAMP(i)
#define STAMP_INIT()
#define STAMP_DUMP()

template <typename T> __device__ __forceinline__ T ldg(const u16* p) { return *(const GAS T*)p; }
template <typename T> __device__ __forceinline__ T ldnt(const u16* p) { return __builtin_nontemporal_load((const GAS T*)p); }

constexpr int L_ST = 0;
constexpr int L_CNT = 16;
constexpr int L_RQ = 32;
constexpr int L_RH = L_RQ + 1024;
constexpr int L_HB = L_RH + 1024;
constexpr int L_MO = L_HB + 32;
constexpr int L_ML = L_MO + 2048;
constexpr int L_OB = L_ML + 32;
constexpr int L_QB = L_OB + 64;
constexpr int L_FR = L_QB + 256;
constexpr int L_KS = L_FR + 32;
constexpr int L_END = L_KS + 8192;
enum { C_Q = 0, C_QRDY, C_ISS, C_ATT, C_PUB2, C_H, C_HRDY, C_O, C_PUB3, C_FIN, C_ST };

__device__ __forceinline__ unsigned arrive(LAS unsigned* c, int lane) {
  asm volatile("s_waitcnt lgkmcnt(0)" ::: "memory");
  unsigned old = 0;
  if (lane == 0) old = __hip_atomic_fetch_add(c, 1u, __ATOMIC_RELAXED, __HIP_MEMORY_SCOPE_WORKGROUP);
  old = __builtin_amdgcn_readfirstlane(old);
  asm volatile("" ::: "memory");
  return old;
}
template <int SLEEP> __device__ __forceinline__ void await(LAS unsigned* c, unsigned n) {
  for (unsigned s = 0; s < LSPIN_CAP; ++s) {
    if (__builtin_amdgcn_readfirstlane(__hip_atomic_load(c, __ATOMIC_RELAXED, __HIP_MEMORY_SCOPE_WORKGROUP)) >= n) break;
    if (SLEEP) __builtin_amdgcn_s_sleep(SLEEP);
  }
  asm volatile("" ::: "memory");
}

__device__ __forceinline__ v4u tr8x4(const v2u (&W)[8], int i) {
  v4u f;
#pragma unroll
  for (int jj = 0; jj < 4; ++jj)
    f[jj] = (i & 1) ? perm_hi(W[2 * jj][i >> 1], W[2 * jj + 1][i >> 1]) : perm_lo(W[2 * jj][i >> 1], W[2 * jj + 1][i >> 1]);
  return f;
}

__global__ void __launch_bounds__(512, 2)
fused_kernel(const u16* __restrict__ x, const u16* __restrict__ kcache, const u16* __restrict__ vcache,
             const u16* __restrict__ wq, const u16* __restrict__ wo, const u16* __restrict__ w1,
             const u16* __restrict__ b1, const u16* __restrict__ w2, const u16* __restrict__ b2,
             const u16* __restrict__ gw, u16* __restrict__ out, unsigned char* ws) {
  __shared__ __attribute__((aligned(16))) float lds[L_END];
  const int tid = threadIdx.x, lane = tid & 63, w = __builtin_amdgcn_readfirstlane(tid >> 6);
  const int g = lane >> 4, c = lane & 15;
  const int bid = blockIdx.x, hk = bid & 7, idx = bid >> 3;
  const int ab = idx >> 3, as = idx & 7;
  const int kgl = idx >> 1, cg = idx & 1, kg = hk * 16 + kgl;
  const __amdgpu_buffer_rsrc_t rs = __builtin_amdgcn_make_buffer_rsrc((void*)ws, 0, (int)WS_END, 0x00020000);
  LAS unsigned* cnt = (LAS unsigned*)(lds + L_CNT);
  STAMP_INIT();
  STAMP(0);
  const unsigned T = *(const GAS unsigned*)ws + 1u;

  if (w >= 4) {
    const int aw = w - 4;
    BAR();
    v4u kr[8], vf[8];
    {
      const size_t kvoff = ((size_t)(ab * 8 + hk) * SK + (SK - WINDOW) + 128 * as + 32 * aw) * DH;
      const u16* kp = kcache + kvoff + 8 * lane;
      const u16* vp = vcache + kvoff + 4 * g * DH + 8 * c;
#pragma unroll
      for (int i = 0; i < 16; ++i) {
        if (i >= AWIN) {
          PIN();
          asm volatile("s_waitcnt vmcnt(%0)" :: "n"(AWIN - 1) : "memory");
          PIN();
        }
        if (i < 8) kr[i] = ldnt<v4u>(kp + i * 512);
        else { const int j = i - 8; vf[j] = ldnt<v4u>(vp + (16 * (j >> 2) + (j & 3)) * DH); }
      }
    }
    PIN();
    (void)arrive(cnt + C_ISS, lane);
    STAMP(1);
    v4u kf[8];
    {
      unsigned char* ks_ = (unsigned char*)(lds + L_KS) + aw * 8192;
#pragma unroll
      for (int i = 0; i < 8; ++i) { const int row = 4 * i + g; *(v4u*)(ks_ + row * 256 + ((c ^ (row & 15)) << 4)) = kr[i]; }
      asm volatile("s_waitcnt lgkmcnt(0)" ::: "memory");
#pragma unroll
      for (int t = 0; t < 2; ++t)
#pragma unroll
        for (int ks = 0; ks < 4; ++ks) kf[t * 4 + ks] = *(const v4u*)(ks_ + (16 * t + c) * 256 + (((4 * ks + g) ^ c) << 4));
#pragma unroll
      for (int i = 0; i < 8; ++i) KEEP(kf[i]);
    }
    PIN();
    STAMP(2);
    await<0>(cnt + C_QRDY, 1u);
    STAMP(3);
    float m, l;
    v4u pf;
    {
      f32x4 s0 = {0.f, 0.f, 0.f, 0.f}, s1 = {0.f, 0.f, 0.f, 0.f};
      const u16* qb = (const u16*)(lds + L_QB) + (c >> 2) * 128 + 8 * g;
#pragma unroll
      for (int ks = 0; ks < 4; ++ks) {
        const v4u qf = *(const v4u*)(qb + 32 * ks);
        s0 = mfma32(kf[ks], qf, s0);
        s1 = mfma32(kf[4 + ks], qf, s1);
      }
      float sc[8];
#pragma unroll
      for (int j = 0; j < 4; ++j) { sc[j] = bfr(bfr(s0[j]) * 0.08838834764831845f); sc[4 + j] = bfr(bfr(s1[j]) * 0.08838834764831845f); }
      m = fmaxf(fmaxf(fmaxf(sc[0], sc[1]), fmaxf(sc[2], sc[3])), fmaxf(fmaxf(sc[4], sc[5]), fmaxf(sc[6], sc[7])));
      m = x32_max(x16_max(m));
      float p[8];
      l = 0.f;
#pragma unroll
      for (int j = 0; j < 8; ++j) { p[j] = __builtin_amdgcn_exp2f((sc[j] - m) * LOG2E); l += p[j]; }
      pf = v4u{pack2(p[0], p[1]), pack2(p[2], p[3]), pack2(p[4], p[5]), pack2(p[6], p[7])};
      l = x32_sum(x16_sum(l));
      if (g == 0 && (c & 3) == 0) *(f32x2*)(lds + L_ML + (aw * 4 + (c >> 2)) * 2) = f32x2{m, l};
      KEEP(pf);
    }
    PIN();
    STAMP(4);
    {
      float ov[8];
#pragma unroll
      for (int i = 0; i < 8; ++i) {
        v4u vt;
#pragma unroll
        for (int jj = 0; jj < 4; ++jj)
          vt[jj] = (i & 1) ? perm_hi(vf[2 * jj][i >> 1], vf[2 * jj + 1][i >> 1]) : perm_lo(vf[2 * jj][i >> 1], vf[2 * jj + 1][i >> 1]);
        const f32x4 o = mfma32(pf, vt, f32x4{0.f, 0.f, 0.f, 0.f});
        ov[i] = o[0];
      }
      float* mo = lds + L_MO + (aw * 4 + g) * 128 + 8 * c;
      *(f32x4*)(mo) = f32x4{ov[0], ov[1], ov[2], ov[3]};
      *(f32x4*)(mo + 4) = f32x4{ov[4], ov[5], ov[6], ov[7]};
    }
    if (arrive(cnt + C_ATT, lane) != 3u) await<0>(cnt + C_ATT, 4u);
    STAMP(5);
    {
      f32x2 ml[4], ov[4];
#pragma unroll
      for (int u = 0; u < 4; ++u) { ml[u] = *(const f32x2*)(lds + L_ML + (u * 4 + aw) * 2); ov[u] = *(const f32x2*)(lds + L_MO + (u * 4 + aw) * 128 + 2 * lane); }
      const float M = fmaxf(fmaxf(ml[0][0], ml[1][0]), fmaxf(ml[2][0], ml[3][0]));
      float L = 0.f, o0 = 0.f, o1 = 0.f;
#pragma unroll
      for (int u = 0; u < 4; ++u) {
        const float f = __builtin_amdgcn_exp2f((ml[u][0] - M) * LOG2E);
        L = fmaf(f, ml[u][1], L); o0 = fmaf(f, ov[u][0], o0); o1 = fmaf(f, ov[u][1], o1);
      }
      const int hg = 4 * hk + aw;
      const v4u pv = {__float_as_uint(o0), T, __float_as_uint(o1), T};
      __builtin_amdgcn_raw_buffer_store_b128(pv, rs, WS_P2 + (((hg * 4 + (lane >> 4)) * 4 + ab) * 8 + as) * 256 + (lane & 15) * 16, 0, AUX_SC1);
      if (lane == 0) {
        const v4u mv = {__float_as_uint(M), T, __float_as_uint(L), T};
        __builtin_amdgcn_raw_buffer_store_b128(mv, rs, WS_ML + ((hg * 4 + ab) * 8 + as) * 16, 0, AUX_SC1);
      }
    }
    PIN();
    (void)arrive(cnt + C_PUB2, lane);
    STAMP(6);
    const int ob = bid >> 6, ocw = (bid >> 3) & 7, om = (bid >> 1) & 3, oh = bid & 1;
    const int col = 64 * ocw + 4 * (4 * om + (lane & 3)) + 2 * oh;
    const unsigned b2v = ldg<unsigned>(b2 + col);
    const unsigned off = WS_P3 + ((aw * 32 + (lane >> 2)) * 8 + ocw) * 2048 + oh * 1024 + (ob * 16 + 4 * om + (lane & 3)) * 16;
    await<2>(cnt + C_PUB3, 4u);
    __builtin_amdgcn_s_sleep(DLY3);
    STAMP(8);
    v4u pv[2];
#pragma unroll
    for (int i = 0; i < 2; ++i) pv[i] = __builtin_amdgcn_raw_buffer_load_b128(rs, off + i * (16 * 8 * 2048), 0, AUX_SC1);
    for (unsigned spins = 0; spins < SPIN_CAP; ++spins) {
      const unsigned bad0 = (pv[0][1] ^ T) | (pv[0][3] ^ T), bad1 = (pv[1][1] ^ T) | (pv[1][3] ^ T);
      if (__all((bad0 | bad1) == 0)) break;
      asm volatile("" ::: "memory");
      if (bad0) pv[0] = __builtin_amdgcn_raw_buffer_load_b128(rs, off, 0, AUX_SC1);
      if (bad1) pv[1] = __builtin_amdgcn_raw_buffer_load_b128(rs, off + 16 * 8 * 2048, 0, AUX_SC1);
    }
    STAMP(9);
    float sa = __uint_as_float(pv[0][0]) + __uint_as_float(pv[1][0]), sb = __uint_as_float(pv[0][2]) + __uint_as_float(pv[1][2]);
    sa += dpp<ROR4>(sa); sa += dpp<ROR8>(sa); sa = x32_sum(x16_sum(sa));
    sb += dpp<ROR4>(sb); sb += dpp<ROR8>(sb); sb = x32_sum(x16_sum(sb));
    if (lane < 4) *(f32x2*)(lds + L_FR + aw * 8 + 2 * lane) = f32x2{sa, sb};
    if (arrive(cnt + C_FIN, lane) == 3u) {
      if (lane < 4) {
        const float omg = lds[L_ST + 4 * ob + 3];
        f32x2 t = *(const f32x2*)(lds + L_FR + 2 * lane);
#pragma unroll
        for (int u = 1; u < 4; ++u) t += *(const f32x2*)(lds + L_FR + u * 8 + 2 * lane);
        *(GAS unsigned*)(out + ob * DM + col) = pack2(fmaf(omg, lo2f(b2v), t[0]), fmaf(omg, hi2f(b2v), t[1]));
      }
      if (bid == 0 && lane == 0) __hip_atomic_store((gu32*)ws, T, __ATOMIC_RELAXED, __HIP_MEMORY_SCOPE_AGENT);
    }
  } else {
    const int pw = w;
    const bool has_mlp = kgl < 14;
    const int f0 = 224 * hk + 16 * kgl;
    const int kc = c >> 2, ch = c & 3;
    const int krow = 128 * pw + 32 * kc + 8 * g;
    v2u wv[8];
    {
      const u16* p = wq + (size_t)krow * NQ + hk * 512 + idx * 16 + 4 * ch;
#pragma unroll
      for (int j = 0; j < 8; ++j) wv[j] = ldnt<v2u>(p + j * NQ);
    }
    const v4u xa = ldg<v4u>(x + (c & 3) * DM + krow);
    const v4u xs = ldg<v4u>(x + pw * DM + 8 * lane);
    const v4u gwv = ldg<v4u>(gw + 8 * lane);
    if (tid < 16) cnt[tid] = 0u;
    BAR();
    {
      f32x4 d[4];
#pragma unroll
      for (int i = 0; i < 4; ++i) d[i] = mfma32(xa, tr8x4(wv, i), f32x4{0.f, 0.f, 0.f, 0.f});
      if (g == kc) {
#pragma unroll
        for (int r = 0; r < 4; ++r)
          *(f32x4*)(lds + L_RQ + ((pw * 4 + kc) * 4 + r) * 16 + 4 * ch) = f32x4{d[0][r], d[1][r], d[2][r], d[3][r]};
      }
      if (arrive(cnt + C_Q, lane) == 3u) {
        const float* red = lds + L_RQ + (g * 16 + (c >> 2)) * 16 + 4 * ch;
        f32x4 a = *(const f32x4*)(red);
#pragma unroll
        for (int p = 1; p < 4; ++p) a += *(const f32x4*)(red + p * 64);
#pragma unroll
        for (int i = 0; i < 4; ++i) a[i] = x32_sum(x16_sum(a[i]));
        if (g == 0) {
          const v4u qv = {pack2(a[0], a[1]), T, pack2(a[2], a[3]), T};
          __builtin_amdgcn_raw_buffer_store_b128(qv, rs, WS_QG + (hk * 32 + idx) * 256 + c * 16, 0, AUX_SC1);
        }
      }
    }
    STAMP(1);
    {
      float xv[8], gv[8];
#pragma unroll
      for (int k = 0; k < 4; ++k) { xv[2 * k] = lo2f(xs[k]); xv[2 * k + 1] = hi2f(xs[k]); gv[2 * k] = lo2f(gwv[k]); gv[2 * k + 1] = hi2f(gwv[k]); }
      float s = 0.f, gs = 0.f;
#pragma unroll
      for (int k = 0; k < 8; ++k) { s += xv[k]; gs += bfr(xv[k] * gv[k]); }
      s = wave_sum(s); gs = wave_sum(gs);
      const float mean = s * (1.0f / DM);
      float vs = 0.f;
#pragma unroll
      for (int k = 0; k < 8; ++k) { const float d = xv[k] - mean; vs = fmaf(d, d, vs); }
      vs = wave_sum(vs);
      if (lane == 0) {
        const float var_b = bfr(vs * (1.0f / DM));
        const float den = bfr(sqrtf(bfr(var_b + 1e-5f)));
        const float gt = bfr(1.0f / (1.0f + __expf(-bfr(gs))));
        *(f32x4*)(lds + L_ST + 4 * pw) = f32x4{bfr(mean), den, gt, bfr(1.0f - gt)};
      }
      (void)arrive(cnt + C_ST, lane);
    }
    STAMP(2);
    if (pw == 0) {
      const unsigned qoff = WS_QG + ((hk * 32 + (lane >> 1)) * 4 + ab) * 64 + (lane & 1) * 32;
      v4u q0 = __builtin_amdgcn_raw_buffer_load_b128(rs, qoff, 0, AUX_SC1);
      v4u q1 = __builtin_amdgcn_raw_buffer_load_b128(rs, qoff + 16, 0, AUX_SC1);
      for (unsigned spins = 0; spins < SPIN_CAP; ++spins) {
        const bool ok = ((q0[1] ^ T) | (q0[3] ^ T) | (q1[1] ^ T) | (q1[3] ^ T)) == 0;
        if (__all(ok)) break;
        asm volatile("" ::: "memory");
        if (!ok) {
          q0 = __builtin_amdgcn_raw_buffer_load_b128(rs, qoff, 0, AUX_SC1);
          q1 = __builtin_amdgcn_raw_buffer_load_b128(rs, qoff + 16, 0, AUX_SC1);
        }
      }
      *(v4u*)((u16*)(lds + L_QB) + 8 * lane) = v4u{q0[0], q0[2], q1[0], q1[2]};
      (void)arrive(cnt + C_QRDY, lane);
    }
    STAMP(3);
    await<4>(cnt + C_ISS, 4u);
    const __amdgpu_buffer_rsrc_t rw1 = __builtin_amdgcn_make_buffer_rsrc((void*)w1, 0, has_mlp ? DM * FF * 2 : 0, 0x00020000);
    const __amdgpu_buffer_rsrc_t rb1 = __builtin_amdgcn_make_buffer_rsrc((void*)b1, 0, has_mlp ? FF * 2 : 0, 0x00020000);
    const __amdgpu_buffer_rsrc_t rw2 = __builtin_amdgcn_make_buffer_rsrc((void*)w2, 0, has_mlp ? DM * FF * 2 : 0, 0x00020000);
    v2u wov[8], w2v[4], b1v;
    {
      const int o = (krow * FF + f0 + 4 * ch) * 2;
      const u16* p = wo + (size_t)(32 * kg + 8 * g) * DM + 256 * cg + 64 * pw + 4 * c;
      const int o2 = ((f0 + 4 * g) * DM + 256 * cg + 64 * pw + 4 * c) * 2;
#pragma unroll
      for (int j = 0; j < 8; ++j) {
        if (j >= PWIN) { PIN(); asm volatile("s_waitcnt vmcnt(%0)" :: "n"(PWIN - 1) : "memory"); PIN(); }
        wv[j] = __builtin_amdgcn_raw_buffer_load_b64(rw1, o + j * FF * 2, 0, AUX_NT);
      }
      PIN();
      b1v = __builtin_amdgcn_raw_buffer_load_b64(rb1, (f0 + 4 * ch) * 2, 0, 0);
#pragma unroll
      for (int j = 0; j < 8; ++j) wov[j] = ldnt<v2u>(p + j * DM);
#pragma unroll
      for (int j = 0; j < 4; ++j) w2v[j] = __builtin_amdgcn_raw_buffer_load_b64(rw2, o2 + j * DM * 2, 0, AUX_NT);
    }
    PIN();
    await<1>(cnt + C_ST, 4u);
    v4u ya;
    {
      const f32x4 st = *(const f32x4*)(lds + L_ST + 4 * (c & 3));
#pragma unroll
      for (int k = 0; k < 4; ++k) {
        const float y0 = bfr(bfr(lo2f(xa[k]) - st[0]) / st[1]), y1 = bfr(bfr(hi2f(xa[k]) - st[0]) / st[1]);
        ya[k] = pack2(y0, y1);
      }
    }
    f32x4 stg = *(const f32x4*)(lds + L_ST + 4 * g);
    KEEP(stg);
    STAMP(4);
    {
      f32x4 d[4];
#pragma unroll
      for (int i = 0; i < 4; ++i) d[i] = mfma32(ya, tr8x4(wv, i), f32x4{0.f, 0.f, 0.f, 0.f});
      if (g == kc) {
#pragma unroll
        for (int r = 0; r < 4; ++r)
          *(f32x4*)(lds + L_RH + ((pw * 4 + kc) * 4 + r) * 16 + 4 * ch) = f32x4{d[0][r], d[1][r], d[2][r], d[3][r]};
      }
      if (arrive(cnt + C_H, lane) == 3u) {
        const float* red = lds + L_RH + (g * 16 + (c >> 2)) * 16 + 4 * ch;
        f32x4 a = *(const f32x4*)(red);
#pragma unroll
        for (int p = 1; p < 4; ++p) a += *(const f32x4*)(red + p * 64);
#pragma unroll
        for (int i = 0; i < 4; ++i) a[i] = x32_sum(x16_sum(a[i]));
        const float bb[4] = {lo2f(b1v[0]), hi2f(b1v[0]), lo2f(b1v[1]), hi2f(b1v[1])};
        const float av = g == 0 ? a[0] : g == 1 ? a[1] : g == 2 ? a[2] : a[3];
        const float bv = g == 0 ? bb[0] : g == 1 ? bb[1] : g == 2 ? bb[2] : bb[3];
        const float u = bfr(bfr(av) + bv);
        const float inner = 0.7978845608028654f * (u + 0.044715f * u * u * u);
        const float hv = 0.5f * u * (1.0f + tanhf(inner));
        ((u16*)(lds + L_HB))[(c >> 2) * 16 + 4 * ch + g] = (u16)bfbits(hv);
        (void)arrive(cnt + C_HRDY, lane);
      }
    }
    v4u wof[4];
    float accm[4];
    {
      v2u w2f[4];
#pragma unroll
      for (int i = 0; i < 4; ++i) {
        wof[i] = tr8x4(wov, i);
#pragma unroll
        for (int jj = 0; jj < 2; ++jj)
          w2f[i][jj] = (i & 1) ? perm_hi(w2v[2 * jj][i >> 1], w2v[2 * jj + 1][i >> 1]) : perm_lo(w2v[2 * jj][i >> 1], w2v[2 * jj + 1][i >> 1]);
        KEEP(wof[i]);
      }
      await<1>(cnt + C_HRDY, 1u);
      const v2u ha = *(const v2u*)((const u16*)(lds + L_HB) + (c >> 2) * 16 + 4 * g);
#pragma unroll
      for (int i = 0; i < 4; ++i) {
        const f32x4 e = mfma16(ha, w2f[i], f32x4{0.f, 0.f, 0.f, 0.f});
        accm[i] = stg[3] * e[0];
        KEEP(accm[i]);
      }
    }
    PIN();
    STAMP(5);
    const int h2 = 4 * hk + (kgl >> 2);
    const unsigned p2off = WS_P2 + (((h2 * 4 + (kgl & 3)) * 4 + pw) * 8 + (lane >> 3)) * 256 + (lane & 7) * 32;
    const unsigned mloff = WS_ML + ((h2 * 4 + pw) * 8 + (lane >> 3)) * 16;
    await<1>(cnt + C_PUB2, 4u);
    __builtin_amdgcn_s_sleep(DLY2);
    STAMP(6);
    v4u a0 = __builtin_amdgcn_raw_buffer_load_b128(rs, p2off, 0, AUX_SC1);
    v4u a1 = __builtin_amdgcn_raw_buffer_load_b128(rs, p2off + 16, 0, AUX_SC1);
    v4u am = __builtin_amdgcn_raw_buffer_load_b128(rs, mloff, 0, AUX_SC1);
    for (unsigned spins = 0; spins < SPIN_CAP; ++spins) {
      const bool ok = ((a0[1] ^ T) | (a0[3] ^ T) | (a1[1] ^ T) | (a1[3] ^ T) | (am[1] ^ T) | (am[3] ^ T)) == 0;
      if (__all(ok)) break;
      asm volatile("" ::: "memory");
      if (!ok) {
        a0 = __builtin_amdgcn_raw_buffer_load_b128(rs, p2off, 0, AUX_SC1);
        a1 = __builtin_amdgcn_raw_buffer_load_b128(rs, p2off + 16, 0, AUX_SC1);
        am = __builtin_amdgcn_raw_buffer_load_b128(rs, mloff, 0, AUX_SC1);
      }
    }
    STAMP(7);
    {
      const float m = __uint_as_float(am[0]), l = __uint_as_float(am[2]);
      float M = fmaxf(m, dpp<ROR8>(m)); M = x32_max(x16_max(M));
      const float f = __builtin_amdgcn_exp2f((m - M) * LOG2E);
      float v[5] = {f * __uint_as_float(a0[0]), f * __uint_as_float(a0[2]), f * __uint_as_float(a1[0]), f * __uint_as_float(a1[2]), f * l};
#pragma unroll
      for (int i = 0; i < 5; ++i) { v[i] += dpp<ROR8>(v[i]); v[i] = x32_sum(x16_sum(v[i])); }
      const float inv = __builtin_amdgcn_rcpf(v[4]);
      if (lane < 8) *(v2u*)((u16*)(lds + L_OB) + pw * 32 + 4 * lane) = v2u{pack2(v[0] * inv, v[1] * inv), pack2(v[2] * inv, v[3] * inv)};
    }
    if (arrive(cnt + C_O, lane) != 3u) await<0>(cnt + C_O, 4u);
    {
      const v4u oa = *(const v4u*)((const u16*)(lds + L_OB) + (c >> 2) * 32 + 8 * g);
      float val[4];
#pragma unroll
      for (int i = 0; i < 4; ++i) {
        const f32x4 d = mfma32(oa, wof[i], f32x4{0.f, 0.f, 0.f, 0.f});
        val[i] = fmaf(stg[2], d[0], accm[i]);
      }
      const unsigned off = WS_P3 + ((kg * 2 + cg) * 4 + pw) * 2048 + lane * 16;
      const v4u s0 = {__float_as_uint(val[0]), T, __float_as_uint(val[1]), T};
      const v4u s1 = {__float_as_uint(val[2]), T, __float_as_uint(val[3]), T};
      __builtin_amdgcn_raw_buffer_store_b128(s0, rs, off, 0, AUX_SC1);
      __builtin_amdgcn_raw_buffer_store_b128(s1, rs, off + 1024, 0, AUX_SC1);
    }
    PIN();
    (void)arrive(cnt + C_PUB3, lane);
    STAMP(8);
  }
  STAMP_DUMP();
}

extern "C" void kernel_launch(void* const* d_in, const int* in_sizes, int n_in,
                              void* d_out, int out_size, void* d_ws, size_t ws_size,
                              hipStream_t stream) {
  (void)in_sizes; (void)n_in; (void)out_size; (void)ws_size;
  fused_kernel<<<256, 512, 0, stream>>>(
      (const u16*)d_in[0], (const u16*)d_in[1], (const u16*)d_in[2], (const u16*)d_in[3], (const u16*)d_in[4],
      (const u16*)d_in[6], (const u16*)d_in[7], (const u16*)d_in[8], (const u16*)d_in[9], (const u16*)d_in[10],
      (u16*)d_out, (unsigned char*)d_ws);
}
```

```cpp
#define AWIN 8
#define PWIN 4
#define PRIO 1
#include <hip/hip_runtime.h>
#include <hip/hip_bf16.h>
#include <stdint.h>

#define GAS __attribute__((address_space(1)))
#define LAS __attribute__((address_space(3)))
typedef unsigned short u16;
typedef unsigned v4u __attribute__((ext_vector_type(4)));
typedef unsigned v2u __attribute__((ext_vector_type(2)));
typedef float f32x4 __attribute__((ext_vector_type(4)));
typedef float f32x2 __attribute__((ext_vector_type(2)));
typedef __bf16 bf16x8 __attribute__((ext_vector_type(8)));
typedef short s16x4 __attribute__((ext_vector_type(4)));
typedef GAS unsigned gu32;

constexpr int DM = 512, SK = 4096, DH = 128, WINDOW = 1024, FF = 1792, NQ = 4096;
constexpr unsigned WS_QG = 4096;
constexpr unsigned WS_P2 = WS_QG + 65536;
constexpr unsigned WS_ML = WS_P2 + (1u << 20);
constexpr unsigned WS_P3 = WS_ML + 16384;
constexpr unsigned WS_END = WS_P3 + (2u << 20);
constexpr unsigned SPIN_CAP = 1u << 17;
constexpr unsigned LSPIN_CAP = 1u << 22;
constexpr int AUX_SC1 = 16, AUX_NT = 2;
constexpr float LOG2E = 1.4426950408889634f;

#define SETPRIO(n) do { if (PRIO) __builtin_amdgcn_s_setprio(n); } while (0)
#define DLY2 8
#define DLY3 10

__device__ __forceinline__ float bfr(float f) { return (float)(__bf16)f; }
__device__ __forceinline__ unsigned bfbits(float f) { return (unsigned)__builtin_bit_cast(u16, (__bf16)f); }
__device__ __forceinline__ unsigned pack2(float lo, float hi) { return bfbits(lo) | (bfbits(hi) << 16); }
__device__ __forceinline__ float lo2f(unsigned u) { return __builtin_bit_cast(float, u << 16); }
__device__ __forceinline__ float hi2f(unsigned u) { return __builtin_bit_cast(float, u & 0xffff0000u); }
__device__ __forceinline__ unsigned perm_lo(unsigned a, unsigned b) { return __builtin_amdgcn_perm(b, a, 0x05040100u); }
__device__ __forceinline__ unsigned perm_hi(unsigned a, unsigned b) { return __builtin_amdgcn_perm(b, a, 0x07060302u); }

template <int CTRL> __device__ __forceinline__ float dpp(float x) {
  return __builtin_bit_cast(float, __builtin_amdgcn_mov_dpp(__builtin_bit_cast(int, x), CTRL, 0xf, 0xf, true));
}
constexpr int XOR1 = 0xB1, XOR2 = 0x4E, ROR8 = 0x128, ROR4 = 0x124;
__device__ __forceinline__ float x16_max(float x) {
  auto s = __builtin_amdgcn_permlane16_swap(__float_as_uint(x), __float_as_uint(x), false, false);
  return fmaxf(__uint_as_float(s[0]), __uint_as_float(s[1]));
}
__device__ __forceinline__ float x32_max(float x) {
  auto s = __builtin_amdgcn_permlane32_swap(__float_as_uint(x), __float_as_uint(x), false, false);
  return fmaxf(__uint_as_float(s[0]), __uint_as_float(s[1]));
}
__device__ __forceinline__ float x16_sum(float x) {
  auto s = __builtin_amdgcn_permlane16_swap(__float_as_uint(x), __float_as_uint(x), false, false);
  return __uint_as_float(s[0]) + __uint_as_float(s[1]);
}
__device__ __forceinline__ float x32_sum(float x) {
  auto s = __builtin_amdgcn_permlane32_swap(__float_as_uint(x), __float_as_uint(x), false, false);
  return __uint_as_float(s[0]) + __uint_as_float(s[1]);
}
__device__ __forceinline__ float wave_sum(float x) {
  x += dpp<XOR1>(x); x += dpp<XOR2>(x); x += dpp<ROR4>(x); x += dpp<ROR8>(x);
  return x32_sum(x16_sum(x));
}
__device__ __forceinline__ f32x4 mfma32(v4u a, v4u b, f32x4 c) {
  return __builtin_amdgcn_mfma_f32_16x16x32_bf16(__builtin_bit_cast(bf16x8, a), __builtin_bit_cast(bf16x8, b), c, 0, 0, 0);
}
__device__ __forceinline__ f32x4 mfma16(v2u a, v2u b, f32x4 c) {
  return __builtin_amdgcn_mfma_f32_16x16x16bf16_1k(__builtin_bit_cast(s16x4, a), __builtin_bit_cast(s16x4, b), c, 0, 0, 0);
}
#define PIN() __builtin_amdgcn_sched_barrier(0)
#define BAR() do { PIN(); asm volatile("s_waitcnt lgkmcnt(0)" ::: "memory"); __builtin_amdgcn_s_barrier(); asm volatile("" ::: "memory"); PIN(); } while (0)
#define KEEP(x) asm volatile("" : "+v"(x))
#define STAMP(i)
#define STAMP_INIT()
#define STAMP_DUMP()

template <typename T> __device__ __forceinline__ T ldg(const u16* p) { return *(const GAS T*)p; }
template <typename T> __device__ __forceinline__ T ldnt(const u16* p) { return __builtin_nontemporal_load((const GAS T*)p); }

constexpr int L_ST = 0;
constexpr int L_CNT = 16;
constexpr int L_RQ = 32;
constexpr int L_RH = L_RQ + 1024;
constexpr int L_HB = L_RH + 1024;
constexpr int L_MO = L_HB + 32;
constexpr int L_ML = L_MO + 2048;
constexpr int L_OB = L_ML + 32;
constexpr int L_QB = L_OB + 64;
constexpr int L_FR = L_QB + 256;
constexpr int L_KS = L_FR + 32;
constexpr int L_END = L_KS + 8192;
enum { C_Q = 0, C_QRDY, C_ISS, C_ATT, C_PUB2, C_H, C_HRDY, C_O, C_PUB3, C_FIN, C_ST };

__device__ __forceinline__ unsigned arrive(LAS unsigned* c, int lane) {
  asm volatile("s_waitcnt lgkmcnt(0)" ::: "memory");
  unsigned old = 0;
  if (lane == 0) old = __hip_atomic_fetch_add(c, 1u, __ATOMIC_RELAXED, __HIP_MEMORY_SCOPE_WORKGROUP);
  old = __builtin_amdgcn_readfirstlane(old);
  asm volatile("" ::: "memory");
  return old;
}
template <int SLEEP> __device__ __forceinline__ void await(LAS unsigned* c, unsigned n) {
  for (unsigned s = 0; s < LSPIN_CAP; ++s) {
    if (__builtin_amdgcn_readfirstlane(__hip_atomic_load(c, __ATOMIC_RELAXED, __HIP_MEMORY_SCOPE_WORKGROUP)) >= n) break;
    if (SLEEP) __builtin_amdgcn_s_sleep(SLEEP);
  }
  asm volatile("" ::: "memory");
}

__device__ __forceinline__ v4u tr8x4(const v2u (&W)[8], int i) {
  v4u f;
#pragma unroll
  for (int jj = 0; jj < 4; ++jj)
    f[jj] = (i & 1) ? perm_hi(W[2 * jj][i >> 1], W[2 * jj + 1][i >> 1]) : perm_lo(W[2 * jj][i >> 1], W[2 * jj + 1][i >> 1]);
  return f;
}

__global__ void __launch_bounds__(512, 2)
fused_kernel(const u16* __restrict__ x, const u16* __restrict__ kcache, const u16* __restrict__ vcache,
             const u16* __restrict__ wq, const u16* __restrict__ wo, const u16* __restrict__ w1,
             const u16* __restrict__ b1, const u16* __restrict__ w2, const u16* __restrict__ b2,
             const u16* __restrict__ gw, u16* __restrict__ out, unsigned char* ws) {
  __shared__ __attribute__((aligned(16))) float lds[L_END];
  const int tid = threadIdx.x, lane = tid & 63, w = __builtin_amdgcn_readfirstlane(tid >> 6);
  const int g = lane >> 4, c = lane & 15;
  const int bid = blockIdx.x, hk = bid & 7, idx = bid >> 3;
  const int ab = idx >> 3, as = idx & 7;
  const int kgl = idx >> 1, cg = idx & 1, kg = hk * 16 + kgl;
  const __amdgpu_buffer_rsrc_t rs = __builtin_amdgcn_make_buffer_rsrc((void*)ws, 0, (int)WS_END, 0x00020000);
  LAS unsigned* cnt = (LAS unsigned*)(lds + L_CNT);
  STAMP_INIT();
  STAMP(0);
  const unsigned T = *(const GAS unsigned*)ws + 1u;

  if (w >= 4) {
    const int aw = w - 4;
    BAR();
    v4u kr[8], vf[8];
    {
      const size_t kvoff = ((size_t)(ab * 8 + hk) * SK + (SK - WINDOW) + 128 * as + 32 * aw) * DH;
      const u16* kp = kcache + kvoff + 8 * lane;
      const u16* vp = vcache + kvoff + 4 * g * DH + 8 * c;
#pragma unroll
      for (int i = 0; i < 16; ++i) {
        if (i >= AWIN) {
          PIN();
          asm volatile("s_waitcnt vmcnt(%0)" :: "n"(AWIN - 1) : "memory");
          PIN();
        }
        if (i < 8) kr[i] = ldnt<v4u>(kp + i * 512);
        else { const int j = i - 8; vf[j] = ldnt<v4u>(vp + (16 * (j >> 2) + (j & 3)) * DH); }
      }
    }
    PIN();
    SETPRIO(2);
    (void)arrive(cnt + C_ISS, lane);
    STAMP(1);
    v4u kf[8];
    {
      unsigned char* ks_ = (unsigned char*)(lds + L_KS) + aw * 8192;
#pragma unroll
      for (int i = 0; i < 8; ++i) { const int row = 4 * i + g; *(v4u*)(ks_ + row * 256 + ((c ^ (row & 15)) << 4)) = kr[i]; }
      asm volatile("s_waitcnt lgkmcnt(0)" ::: "memory");
#pragma unroll
      for (int t = 0; t < 2; ++t)
#pragma unroll
        for (int ks = 0; ks < 4; ++ks) kf[t * 4 + ks] = *(const v4u*)(ks_ + (16 * t + c) * 256 + (((4 * ks + g) ^ c) << 4));
#pragma unroll
      for (int i = 0; i < 8; ++i) KEEP(kf[i]);
    }
    PIN();
    STAMP(2);
    await<0>(cnt + C_QRDY, 1u);
    STAMP(3);
    float m, l;
    v4u pf;
    {
      f32x4 s0 = {0.f, 0.f, 0.f, 0.f}, s1 = {0.f, 0.f, 0.f, 0.f};
      const u16* qb = (const u16*)(lds + L_QB) + (c >> 2) * 128 + 8 * g;
#pragma unroll
      for (int ks = 0; ks < 4; ++ks) {
        const v4u qf = *(const v4u*)(qb + 32 * ks);
        s0 = mfma32(kf[ks], qf, s0);
        s1 = mfma32(kf[4 + ks], qf, s1);
      }
      float sc[8];
#pragma unroll
      for (int j = 0; j < 4; ++j) { sc[j] = bfr(bfr(s0[j]) * 0.08838834764831845f); sc[4 + j] = bfr(bfr(s1[j]) * 0.08838834764831845f); }
      m = fmaxf(fmaxf(fmaxf(sc[0], sc[1]), fmaxf(sc[2], sc[3])), fmaxf(fmaxf(sc[4], sc[5]), fmaxf(sc[6], sc[7])));
      m = x32_max(x16_max(m));
      float p[8];
      l = 0.f;
#pragma unroll
      for (int j = 0; j < 8; ++j) { p[j] = __builtin_amdgcn_exp2f((sc[j] - m) * LOG2E); l += p[j]; }
      pf = v4u{pack2(p[0], p[1]), pack2(p[2], p[3]), pack2(p[4], p[5]), pack2(p[6], p[7])};
      l = x32_sum(x16_sum(l));
      if (g == 0 && (c & 3) == 0) *(f32x2*)(lds + L_ML + (aw * 4 + (c >> 2)) * 2) = f32x2{m, l};
      KEEP(pf);
    }
    PIN();
    STAMP(4);
    {
      float ov[8];
#pragma unroll
      for (int i = 0; i < 8; ++i) {
        v4u vt;
#pragma unroll
        for (int jj = 0; jj < 4; ++jj)
          vt[jj] = (i & 1) ? perm_hi(vf[2 * jj][i >> 1], vf[2 * jj + 1][i >> 1]) : perm_lo(vf[2 * jj][i >> 1], vf[2 * jj + 1][i >> 1]);
        const f32x4 o = mfma32(pf, vt, f32x4{0.f, 0.f, 0.f, 0.f});
        ov[i] = o[0];
      }
      float* mo = lds + L_MO + (aw * 4 + g) * 128 + 8 * c;
      *(f32x4*)(mo) = f32x4{ov[0], ov[1], ov[2], ov[3]};
      *(f32x4*)(mo + 4) = f32x4{ov[4], ov[5], ov[6], ov[7]};
    }
    if (arrive(cnt + C_ATT, lane) != 3u) await<0>(cnt + C_ATT, 4u);
    STAMP(5);
    {
      f32x2 ml[4], ov[4];
#pragma unroll
      for (int u = 0; u < 4; ++u) { ml[u] = *(const f32x2*)(lds + L_ML + (u * 4 + aw) * 2); ov[u] = *(const f32x2*)(lds + L_MO + (u * 4 + aw) * 128 + 2 * lane); }
      const float M = fmaxf(fmaxf(ml[0][0], ml[1][0]), fmaxf(ml[2][0], ml[3][0]));
      float L = 0.f, o0 = 0.f, o1 = 0.f;
#pragma unroll
      for (int u = 0; u < 4; ++u) {
        const float f = __builtin_amdgcn_exp2f((ml[u][0] - M) * LOG2E);
        L = fmaf(f, ml[u][1], L); o0 = fmaf(f, ov[u][0], o0); o1 = fmaf(f, ov[u][1], o1);
      }
      const int hg = 4 * hk + aw;
      const v4u pv = {__float_as_uint(o0), T, __float_as_uint(o1), T};
      __builtin_amdgcn_raw_buffer_store_b128(pv, rs, WS_P2 + (((hg * 4 + (lane >> 4)) * 4 + ab) * 8 + as) * 256 + (lane & 15) * 16, 0, AUX_SC1);
      if (lane == 0) {
        const v4u mv = {__float_as_uint(M), T, __float_as_uint(L), T};
        __builtin_amdgcn_raw_buffer_store_b128(mv, rs, WS_ML + ((hg * 4 + ab) * 8 + as) * 16, 0, AUX_SC1);
      }
    }
    PIN();
    (void)arrive(cnt + C_PUB2, lane);
    STAMP(6);
    const int ob = bid >> 6, ocw = (bid >> 3) & 7, om = (bid >> 1) & 3, oh = bid & 1;
    const int col = 64 * ocw + 4 * (4 * om + (lane & 3)) + 2 * oh;
    const unsigned b2v = ldg<unsigned>(b2 + col);
    const unsigned off = WS_P3 + ((aw * 32 + (lane >> 2)) * 8 + ocw) * 2048 + oh * 1024 + (ob * 16 + 4 * om + (lane & 3)) * 16;
    await<2>(cnt + C_PUB3, 4u);
    __builtin_amdgcn_s_sleep(DLY3);
    STAMP(8);
    v4u pv[2];
#pragma unroll
    for (int i = 0; i < 2; ++i) pv[i] = __builtin_amdgcn_raw_buffer_load_b128(rs, off + i * (16 * 8 * 2048), 0, AUX_SC1);
    for (unsigned spins = 0; spins < SPIN_CAP; ++spins) {
      const unsigned bad0 = (pv[0][1] ^ T) | (pv[0][3] ^ T), bad1 = (pv[1][1] ^ T) | (pv[1][3] ^ T);
      if (__all((bad0 | bad1) == 0)) break;
      asm volatile("" ::: "memory");
      if (bad0) pv[0] = __builtin_amdgcn_raw_buffer_load_b128(rs, off, 0, AUX_SC1);
      if (bad1) pv[1] = __builtin_amdgcn_raw_buffer_load_b128(rs, off + 16 * 8 * 2048, 0, AUX_SC1);
    }
    STAMP(9);
    float sa = __uint_as_float(pv[0][0]) + __uint_as_float(pv[1][0]), sb = __uint_as_float(pv[0][2]) + __uint_as_float(pv[1][2]);
    sa += dpp<ROR4>(sa); sa += dpp<ROR8>(sa); sa = x32_sum(x16_sum(sa));
    sb += dpp<ROR4>(sb); sb += dpp<ROR8>(sb); sb = x32_sum(x16_sum(sb));
    if (lane < 4) *(f32x2*)(lds + L_FR + aw * 8 + 2 * lane) = f32x2{sa, sb};
    if (arrive(cnt + C_FIN, lane) == 3u) {
      if (lane < 4) {
        const float omg = lds[L_ST + 4 * ob + 3];
        f32x2 t = *(const f32x2*)(lds + L_FR + 2 * lane);
#pragma unroll
        for (int u = 1; u < 4; ++u) t += *(const f32x2*)(lds + L_FR + u * 8 + 2 * lane);
        *(GAS unsigned*)(out + ob * DM + col) = pack2(fmaf(omg, lo2f(b2v), t[0]), fmaf(omg, hi2f(b2v), t[1]));
      }
      if (bid == 0 && lane == 0) __hip_atomic_store((gu32*)ws, T, __ATOMIC_RELAXED, __HIP_MEMORY_SCOPE_AGENT);
    }
  } else {
    const int pw = w;
    SETPRIO(3);
    const bool has_mlp = kgl < 14;
    const int f0 = 224 * hk + 16 * kgl;
    const int kc = c >> 2, ch = c & 3;
    const int krow = 128 * pw + 32 * kc + 8 * g;
    v2u wv[8];
    {
      const u16* p = wq + (size_t)krow * NQ + hk * 512 + idx * 16 + 4 * ch;
#pragma unroll
      for (int j = 0; j < 8; ++j) wv[j] = ldnt<v2u>(p + j * NQ);
    }
    const v4u xa = ldg<v4u>(x + (c & 3) * DM + krow);
    const v4u xs = ldg<v4u>(x + pw * DM + 8 * lane);
    const v4u gwv = ldg<v4u>(gw + 8 * lane);
    if (tid < 16) cnt[tid] = 0u;
    BAR();
    {
      f32x4 d[4];
#pragma unroll
      for (int i = 0; i < 4; ++i) d[i] = mfma32(xa, tr8x4(wv, i), f32x4{0.f, 0.f, 0.f, 0.f});
      if (g == kc) {
#pragma unroll
        for (int r = 0; r < 4; ++r)
          *(f32x4*)(lds + L_RQ + ((pw * 4 + kc) * 4 + r) * 16 + 4 * ch) = f32x4{d[0][r], d[1][r], d[2][r], d[3][r]};
      }
      if (arrive(cnt + C_Q, lane) == 3u) {
        const float* red = lds + L_RQ + (g * 16 + (c >> 2)) * 16 + 4 * ch;
        f32x4 a = *(const f32x4*)(red);
#pragma unroll
        for (int p = 1; p < 4; ++p) a += *(const f32x4*)(red + p * 64);
#pragma unroll
        for (int i = 0; i < 4; ++i) a[i] = x32_sum(x16_sum(a[i]));
        if (g == 0) {
          const v4u qv = {pack2(a[0], a[1]), T, pack2(a[2], a[3]), T};
          __builtin_amdgcn_raw_buffer_store_b128(qv, rs, WS_QG + (hk * 32 + idx) * 256 + c * 16, 0, AUX_SC1);
        }
      }
    }
    STAMP(1);
    {
      float xv[8], gv[8];
#pragma unroll
      for (int k = 0; k < 4; ++k) { xv[2 * k] = lo2f(xs[k]); xv[2 * k + 1] = hi2f(xs[k]); gv[2 * k] = lo2f(gwv[k]); gv[2 * k + 1] = hi2f(gwv[k]); }
      float s = 0.f, gs = 0.f;
#pragma unroll
      for (int k = 0; k < 8; ++k) { s += xv[k]; gs += bfr(xv[k] * gv[k]); }
      s = wave_sum(s); gs = wave_sum(gs);
      const float mean = s * (1.0f / DM);
      float vs = 0.f;
#pragma unroll
      for (int k = 0; k < 8; ++k) { const float d = xv[k] - mean; vs = fmaf(d, d, vs); }
      vs = wave_sum(vs);
      if (lane == 0) {
        const float var_b = bfr(vs * (1.0f / DM));
        const float den = bfr(sqrtf(bfr(var_b + 1e-5f)));
        const float gt = bfr(1.0f / (1.0f + __expf(-bfr(gs))));
        *(f32x4*)(lds + L_ST + 4 * pw) = f32x4{bfr(mean), den, gt, bfr(1.0f - gt)};
      }
      (void)arrive(cnt + C_ST, lane);
    }
    STAMP(2);
    if (pw == 0) {
      const unsigned qoff = WS_QG + ((hk * 32 + (lane >> 1)) * 4 + ab) * 64 + (lane & 1) * 32;
      v4u q0 = __builtin_amdgcn_raw_buffer_load_b128(rs, qoff, 0, AUX_SC1);
      v4u q1 = __builtin_amdgcn_raw_buffer_load_b128(rs, qoff + 16, 0, AUX_SC1);
      for (unsigned spins = 0; spins < SPIN_CAP; ++spins) {
        const bool ok = ((q0[1] ^ T) | (q0[3] ^ T) | (q1[1] ^ T) | (q1[3] ^ T)) == 0;
        if (__all(ok)) break;
        asm volatile("" ::: "memory");
        if (!ok) {
          q0 = __builtin_amdgcn_raw_buffer_load_b128(rs, qoff, 0, AUX_SC1);
          q1 = __builtin_amdgcn_raw_buffer_load_b128(rs, qoff + 16, 0, AUX_SC1);
        }
      }
      *(v4u*)((u16*)(lds + L_QB) + 8 * lane) = v4u{q0[0], q0[2], q1[0], q1[2]};
      (void)arrive(cnt + C_QRDY, lane);
    }
    STAMP(3);
    await<4>(cnt + C_ISS, 4u);
    SETPRIO(0);
    const __amdgpu_buffer_rsrc_t rw1 = __builtin_amdgcn_make_buffer_rsrc((void*)w1, 0, has_mlp ? DM * FF * 2 : 0, 0x00020000);
    const __amdgpu_buffer_rsrc_t rb1 = __builtin_amdgcn_make_buffer_rsrc((void*)b1, 0, has_mlp ? FF * 2 : 0, 0x00020000);
    const __amdgpu_buffer_rsrc_t rw2 = __builtin_amdgcn_make_buffer_rsrc((void*)w2, 0, has_mlp ? DM * FF * 2 : 0, 0x00020000);
    v2u wov[8], w2v[4], b1v;
    {
      const int o = (krow * FF + f0 + 4 * ch) * 2;
      const u16* p = wo + (size_t)(32 * kg + 8 * g) * DM + 256 * cg + 64 * pw + 4 * c;
      const int o2 = ((f0 + 4 * g) * DM + 256 * cg + 64 * pw + 4 * c) * 2;
#pragma unroll
      for (int j = 0; j < 8; ++j) {
        if (j >= PWIN) { PIN(); asm volatile("s_waitcnt vmcnt(%0)" :: "n"(PWIN - 1) : "memory"); PIN(); }
        wv[j] = __builtin_amdgcn_raw_buffer_load_b64(rw1, o + j * FF * 2, 0, AUX_NT);
      }
      PIN();
      b1v = __builtin_amdgcn_raw_buffer_load_b64(rb1, (f0 + 4 * ch) * 2, 0, 0);
#pragma unroll
      for (int j = 0; j < 8; ++j) wov[j] = ldnt<v2u>(p + j * DM);
#pragma unroll
      for (int j = 0; j < 4; ++j) w2v[j] = __builtin_amdgcn_raw_buffer_load_b64(rw2, o2 + j * DM * 2, 0, AUX_NT);
    }
    PIN();
    SETPRIO(3);
    await<1>(cnt + C_ST, 4u);
    v4u ya;
    {
      const f32x4 st = *(const f32x4*)(lds + L_ST + 4 * (c & 3));
#pragma unroll
      for (int k = 0; k < 4; ++k) {
        const float y0 = bfr(bfr(lo2f(xa[k]) - st[0]) / st[1]), y1 = bfr(bfr(hi2f(xa[k]) - st[0]) / st[1]);
        ya[k] = pack2(y0, y1);
      }
    }
    f32x4 stg = *(const f32x4*)(lds + L_ST + 4 * g);
    KEEP(stg);
    STAMP(4);
    {
      f32x4 d[4];
#pragma unroll
      for (int i = 0; i < 4; ++i) d[i] = mfma32(ya, tr8x4(wv, i), f32x4{0.f, 0.f, 0.f, 0.f});
      if (g == kc) {
#pragma unroll
        for (int r = 0; r < 4; ++r)
          *(f32x4*)(lds + L_RH + ((pw * 4 + kc) * 4 + r) * 16 + 4 * ch) = f32x4{d[0][r], d[1][r], d[2][r], d[3][r]};
      }
      if (arrive(cnt + C_H, lane) == 3u) {
        const float* red = lds + L_RH + (g * 16 + (c >> 2)) * 16 + 4 * ch;
        f32x4 a = *(const f32x4*)(red);
#pragma unroll
        for (int p = 1; p < 4; ++p) a += *(const f32x4*)(red + p * 64);
#pragma unroll
        for (int i = 0; i < 4; ++i) a[i] = x32_sum(x16_sum(a[i]));
        const float bb[4] = {lo2f(b1v[0]), hi2f(b1v[0]), lo2f(b1v[1]), hi2f(b1v[1])};
        const float av = g == 0 ? a[0] : g == 1 ? a[1] : g == 2 ? a[2] : a[3];
        const float bv = g == 0 ? bb[0] : g == 1 ? bb[1] : g == 2 ? bb[2] : bb[3];
        const float u = bfr(bfr(av) + bv);
        const float inner = 0.7978845608028654f * (u + 0.044715f * u * u * u);
        const float hv = 0.5f * u * (1.0f + tanhf(inner));
        ((u16*)(lds + L_HB))[(c >> 2) * 16 + 4 * ch + g] = (u16)bfbits(hv);
        (void)arrive(cnt + C_HRDY, lane);
      }
    }
    v4u wof[4];
    float accm[4];
    {
      v2u w2f[4];
#pragma unroll
      for (int i = 0; i < 4; ++i) {
        wof[i] = tr8x4(wov, i);
#pragma unroll
        for (int jj = 0; jj < 2; ++jj)
          w2f[i][jj] = (i & 1) ? perm_hi(w2v[2 * jj][i >> 1], w2v[2 * jj + 1][i >> 1]) : perm_lo(w2v[2 * jj][i >> 1], w2v[2 * jj + 1][i >> 1]);
        KEEP(wof[i]);
      }
      await<1>(cnt + C_HRDY, 1u);
      const v2u ha = *(const v2u*)((const u16*)(lds + L_HB) + (c >> 2) * 16 + 4 * g);
#pragma unroll
      for (int i = 0; i < 4; ++i) {
        const f32x4 e = mfma16(ha, w2f[i], f32x4{0.f, 0.f, 0.f, 0.f});
        accm[i] = stg[3] * e[0];
        KEEP(accm[i]);
      }
    }
    PIN();
    STAMP(5);
    const int h2 = 4 * hk + (kgl >> 2);
    const unsigned p2off = WS_P2 + (((h2 * 4 + (kgl & 3)) * 4 + pw) * 8 + (lane >> 3)) * 256 + (lane & 7) * 32;
    const unsigned mloff = WS_ML + ((h2 * 4 + pw) * 8 + (lane >> 3)) * 16;
    await<1>(cnt + C_PUB2, 4u);
    __builtin_amdgcn_s_sleep(DLY2);
    STAMP(6);
    v4u a0 = __builtin_amdgcn_raw_buffer_load_b128(rs, p2off, 0, AUX_SC1);
    v4u a1 = __builtin_amdgcn_raw_buffer_load_b128(rs, p2off + 16, 0, AUX_SC1);
    v4u am = __builtin_amdgcn_raw_buffer_load_b128(rs, mloff, 0, AUX_SC1);
    for (unsigned spins = 0; spins < SPIN_CAP; ++spins) {
      const bool ok = ((a0[1] ^ T) | (a0[3] ^ T) | (a1[1] ^ T) | (a1[3] ^ T) | (am[1] ^ T) | (am[3] ^ T)) == 0;
      if (__all(ok)) break;
      asm volatile("" ::: "memory");
      if (!ok) {
        a0 = __builtin_amdgcn_raw_buffer_load_b128(rs, p2off, 0, AUX_SC1);
        a1 = __builtin_amdgcn_raw_buffer_load_b128(rs, p2off + 16, 0, AUX_SC1);
        am = __builtin_amdgcn_raw_buffer_load_b128(rs, mloff, 0, AUX_SC1);
      }
    }
    STAMP(7);
    {
      const float m = __uint_as_float(am[0]), l = __uint_as_float(am[2]);
      float M = fmaxf(m, dpp<ROR8>(m)); M = x32_max(x16_max(M));
      const float f = __builtin_amdgcn_exp2f((m - M) * LOG2E);
      float v[5] = {f * __uint_as_float(a0[0]), f * __uint_as_float(a0[2]), f * __uint_as_float(a1[0]), f * __uint_as_float(a1[2]), f * l};
#pragma unroll
      for (int i = 0; i < 5; ++i) { v[i] += dpp<ROR8>(v[i]); v[i] = x32_sum(x16_sum(v[i])); }
      const float inv = __builtin_amdgcn_rcpf(v[4]);
      if (lane < 8) *(v2u*)((u16*)(lds + L_OB) + pw * 32 + 4 * lane) = v2u{pack2(v[0] * inv, v[1] * inv), pack2(v[2] * inv, v[3] * inv)};
    }
    if (arrive(cnt + C_O, lane) != 3u) await<0>(cnt + C_O, 4u);
    {
      const v4u oa = *(const v4u*)((const u16*)(lds + L_OB) + (c >> 2) * 32 + 8 * g);
      float val[4];
#pragma unroll
      for (int i = 0; i < 4; ++i) {
        const f32x4 d = mfma32(oa, wof[i], f32x4{0.f, 0.f, 0.f, 0.f});
        val[i] = fmaf(stg[2], d[0], accm[i]);
      }
      const unsigned off = WS_P3 + ((kg * 2 + cg) * 4 + pw) * 2048 + lane * 16;
      const v4u s0 = {__float_as_uint(val[0]), T, __float_as_uint(val[1]), T};
      const v4u s1 = {__float_as_uint(val[2]), T, __float_as_uint(val[3]), T};
      __builtin_amdgcn_raw_buffer_store_b128(s0, rs, off, 0, AUX_SC1);
      __builtin_amdgcn_raw_buffer_store_b128(s1, rs, off + 1024, 0, AUX_SC1);
    }
    PIN();
    (void)arrive(cnt + C_PUB3, lane);
    STAMP(8);
  }
  STAMP_DUMP();
}

extern "C" void kernel_launch(void* const* d_in, const int* in_sizes, int n_in,
                              void* d_out, int out_size, void* d_ws, size_t ws_size,
                              hipStream_t stream) {
  (void)in_sizes; (void)n_in; (void)out_size; (void)ws_size;
  fused_kernel<<<256, 512, 0, stream>>>(
      (const u16*)d_in[0], (const u16*)d_in[1], (const u16*)d_in[2], (const u16*)d_in[3], (const u16*)d_in[4],
      (const u16*)d_in[6], (const u16*)d_in[7], (const u16*)d_in[8], (const u16*)d_in[9], (const u16*)d_in[10],
      (u16*)d_out, (unsigned char*)d_ws);
}
```

```cpp
#define AWIN 8
#define PWIN 4
#include <hip/hip_runtime.h>
#include <hip/hip_bf16.h>
#include <stdint.h>

#define GAS __attribute__((address_space(1)))
#define LAS __attribute__((address_space(3)))
typedef unsigned short u16;
typedef unsigned v4u __attribute__((ext_vector_type(4)));
typedef unsigned v2u __attribute__((ext_vector_type(2)));
typedef float f32x4 __attribute__((ext_vector_type(4)));
typedef float f32x2 __attribute__((ext_vector_type(2)));
typedef __bf16 bf16x8 __attribute__((ext_vector_type(8)));
typedef short s16x4 __attribute__((ext_vector_type(4)));
typedef GAS unsigned gu32;

constexpr int DM = 512, SK = 4096, DH = 128, WINDOW = 1024, FF = 1792, NQ = 4096;
constexpr unsigned WS_QG = 4096;
constexpr unsigned WS_P2 = WS_QG + 65536;
constexpr unsigned WS_ML = WS_P2 + (1u << 20);
constexpr unsigned WS_P3 = WS_ML + 16384;
constexpr unsigned WS_END = WS_P3 + (2u << 20);
constexpr unsigned SPIN_CAP = 1u << 17;
constexpr unsigned LSPIN_CAP = 1u << 22;
constexpr int AUX_SC1 = 16, AUX_NT = 2;
constexpr float LOG2E = 1.4426950408889634f;

#define PRIO 1
#define SETPRIO(n) do { if (PRIO) __builtin_amdgcn_s_setprio(n); } while (0)
#define DLY2 8
#define DLY3 10

__device__ __forceinline__ float bfr(float f) { return (float)(__bf16)f; }
__device__ __forceinline__ unsigned bfbits(float f) { return (unsigned)__builtin_bit_cast(u16, (__bf16)f); }
__device__ __forceinline__ unsigned pack2(float lo, float hi) { return bfbits(lo) | (bfbits(hi) << 16); }
__device__ __forceinline__ float lo2f(unsigned u) { return __builtin_bit_cast(float, u << 16); }
__device__ __forceinline__ float hi2f(unsigned u) { return __builtin_bit_cast(float, u & 0xffff0000u); }
__device__ __forceinline__ unsigned perm_lo(unsigned a, unsigned b) { return __builtin_amdgcn_perm(b, a, 0x05040100u); }
__device__ __forceinline__ unsigned perm_hi(unsigned a, unsigned b) { return __builtin_amdgcn_perm(b, a, 0x07060302u); }

template <int CTRL> __device__ __forceinline__ float dpp(float x) {
  return __builtin_bit_cast(float, __builtin_amdgcn_mov_dpp(__builtin_bit_cast(int, x), CTRL, 0xf, 0xf, true));
}
constexpr int XOR1 = 0xB1, XOR2 = 0x4E, ROR8 = 0x128, ROR4 = 0x124;
__device__ __forceinline__ float x16_max(float x) {
  auto s = __builtin_amdgcn_permlane16_swap(__float_as_uint(x), __float_as_uint(x), false, false);
  return fmaxf(__uint_as_float(s[0]), __uint_as_float(s[1]));
}
__device__ __forceinline__ float x32_max(float x) {
  auto s = __builtin_amdgcn_permlane32_swap(__float_as_uint(x), __float_as_uint(x), false, false);
  return fmaxf(__uint_as_float(s[0]), __uint_as_float(s[1]));
}
__device__ __forceinline__ float x16_sum(float x) {
  auto s = __builtin_amdgcn_permlane16_swap(__float_as_uint(x), __float_as_uint(x), false, false);
  return __uint_as_float(s[0]) + __uint_as_float(s[1]);
}
__device__ __forceinline__ float x32_sum(float x) {
  auto s = __builtin_amdgcn_permlane32_swap(__float_as_uint(x), __float_as_uint(x), false, false);
  return __uint_as_float(s[0]) + __uint_as_float(s[1]);
}
__device__ __forceinline__ float wave_sum(float x) {
  x += dpp<XOR1>(x); x += dpp<XOR2>(x); x += dpp<ROR4>(x); x += dpp<ROR8>(x);
  return x32_sum(x16_sum(x));
}
__device__ __forceinline__ f32x4 mfma32(v4u a, v4u b, f32x4 c) {
  return __builtin_amdgcn_mfma_f32_16x16x32_bf16(__builtin_bit_cast(bf16x8, a), __builtin_bit_cast(bf16x8, b), c, 0, 0, 0);
}
__device__ __forceinline__ f32x4 mfma16(v2u a, v2u b, f32x4 c) {
  return __builtin_amdgcn_mfma_f32_16x16x16bf16_1k(__builtin_bit_cast(s16x4, a), __builtin_bit_cast(s16x4, b), c, 0, 0, 0);
}
#define PIN() __builtin_amdgcn_sched_barrier(0)
#define BAR() do { PIN(); asm volatile("s_waitcnt lgkmcnt(0)" ::: "memory"); __builtin_amdgcn_s_barrier(); asm volatile("" ::: "memory"); PIN(); } while (0)
#define KEEP(x) asm volatile("" : "+v"(x))
#define STAMP(i)
#define STAMP_INIT()
#define STAMP_DUMP()

template <typename T> __device__ __forceinline__ T ldg(const u16* p) { return *(const GAS T*)p; }
template <typename T> __device__ __forceinline__ T ldnt(const u16* p) { return __builtin_nontemporal_load((const GAS T*)p); }

constexpr int L_ST = 0;
constexpr int L_CNT = 16;
constexpr int L_RQ = 32;
constexpr int L_RH = L_RQ + 1024;
constexpr int L_HB = L_RH + 1024;
constexpr int L_MO = L_HB + 32;
constexpr int L_ML = L_MO + 2048;
constexpr int L_OB = L_ML + 32;
constexpr int L_QB = L_OB + 64;
constexpr int L_FR = L_QB + 256;
constexpr int L_KS = L_FR + 32;
constexpr int L_END = L_KS + 8192;
enum { C_Q = 0, C_QRDY, C_ISS, C_ATT, C_PUB2, C_H, C_HRDY, C_O, C_PUB3, C_FIN, C_ST };

__device__ __forceinline__ unsigned arrive(LAS unsigned* c, int lane) {
  asm volatile("s_waitcnt lgkmcnt(0)" ::: "memory");
  unsigned old = 0;
  if (lane == 0) old = __hip_atomic_fetch_add(c, 1u, __ATOMIC_RELAXED, __HIP_MEMORY_SCOPE_WORKGROUP);
  old = __builtin_amdgcn_readfirstlane(old);
  asm volatile("" ::: "memory");
  return old;
}
template <int SLEEP> __device__ __forceinline__ void await(LAS unsigned* c, unsigned n) {
  for (unsigned s = 0; s < LSPIN_CAP; ++s) {
    if (__builtin_amdgcn_readfirstlane(__hip_atomic_load(c, __ATOMIC_RELAXED, __HIP_MEMORY_SCOPE_WORKGROUP)) >= n) break;
    if (SLEEP) __builtin_amdgcn_s_sleep(SLEEP);
  }
  asm volatile("" ::: "memory");
}

__device__ __forceinline__ v4u tr8x4(const v2u (&W)[8], int i) {
  v4u f;
#pragma unroll
  for (int jj = 0; jj < 4; ++jj)
    f[jj] = (i & 1) ? perm_hi(W[2 * jj][i >> 1], W[2 * jj + 1][i >> 1]) : perm_lo(W[2 * jj][i >> 1], W[2 * jj + 1][i >> 1]);
  return f;
}

__global__ void __launch_bounds__(512, 2)
fused_kernel(const u16* __restrict__ x, const u16* __restrict__ kcache, const u16* __restrict__ vcache,
             const u16* __restrict__ wq, const u16* __restrict__ wo, const u16* __restrict__ w1,
             const u16* __restrict__ b1, const u16* __restrict__ w2, const u16* __restrict__ b2,
             const u16* __restrict__ gw, u16* __restrict__ out, unsigned char* ws) {
  __shared__ __attribute__((aligned(16))) float lds[L_END];
  const int tid = threadIdx.x, lane = tid & 63, w = __builtin_amdgcn_readfirstlane(tid >> 6);
  const int g = lane >> 4, c = lane & 15;
  const int bid = blockIdx.x, hk = bid & 7, idx = bid >> 3;
  const int ab = idx >> 3, as = idx & 7;
  const int kgl = idx >> 1, cg = idx & 1, kg = hk * 16 + kgl;
  const __amdgpu_buffer_rsrc_t rs = __builtin_amdgcn_make_buffer_rsrc((void*)ws, 0, (int)WS_END, 0x00020000);
  LAS unsigned* cnt = (LAS unsigned*)(lds + L_CNT);
  STAMP_INIT();
  STAMP(0);
  const unsigned T = *(const GAS unsigned*)ws + 1u;

  if (w >= 4) {
    const int aw = w - 4;
    BAR();
    unsigned tt;
    {
      const unsigned char* tp = ws;
      tp = lane == 0 ? (const unsigned char*)(out + (bid >> 6) * DM) : tp;
      tp = lane == 1 ? ws + WS_P3 : tp;
      tp = lane == 2 ? ws + WS_END - 64 : tp;
      tp = lane == 3 ? (const unsigned char*)b2 : tp;
      tp = lane == 4 ? (const unsigned char*)(wo + (size_t)(32 * kg) * DM) : tp;
      tp = lane == 5 ? (const unsigned char*)(w2 + (size_t)(224 * hk) * DM) : tp;
      tp = lane == 6 ? (const unsigned char*)w1 : tp;
      tp = lane == 7 ? (const unsigned char*)(w1 + DM * FF - 2) : tp;
      tp = lane == 8 ? (const unsigned char*)b1 : tp;
      tt = *(const GAS unsigned*)tp;
    }
    PIN();
    v4u kr[8], vf[8];
    {
      const size_t kvoff = ((size_t)(ab * 8 + hk) * SK + (SK - WINDOW) + 128 * as + 32 * aw) * DH;
      const u16* kp = kcache + kvoff + 8 * lane;
      const u16* vp = vcache + kvoff + 4 * g * DH + 8 * c;
#pragma unroll
      for (int i = 0; i < 16; ++i) {
        if (i >= AWIN) {
          PIN();
          asm volatile("s_waitcnt vmcnt(%0)" :: "n"(AWIN - 1) : "memory");
          PIN();
        }
        if (i < 8) kr[i] = ldnt<v4u>(kp + i * 512);
        else { const int j = i - 8; vf[j] = ldnt<v4u>(vp + (16 * (j >> 2) + (j & 3)) * DH); }
      }
    }
    PIN();
    SETPRIO(2);
    asm volatile("" :: "v"(tt));
    (void)arrive(cnt + C_ISS, lane);
    STAMP(1);
    v4u kf[8];
    {
      unsigned char* ks_ = (unsigned char*)(lds + L_KS) + aw * 8192;
#pragma unroll
      for (int i = 0; i < 8; ++i) { const int row = 4 * i + g; *(v4u*)(ks_ + row * 256 + ((c ^ (row & 15)) << 4)) = kr[i]; }
      asm volatile("s_waitcnt lgkmcnt(0)" ::: "memory");
#pragma unroll
      for (int t = 0; t < 2; ++t)
#pragma unroll
        for (int ks = 0; ks < 4; ++ks) kf[t * 4 + ks] = *(const v4u*)(ks_ + (16 * t + c) * 256 + (((4 * ks + g) ^ c) << 4));
#pragma unroll
      for (int i = 0; i < 8; ++i) KEEP(kf[i]);
    }
    PIN();
    STAMP(2);
    await<0>(cnt + C_QRDY, 1u);
    STAMP(3);
    float m, l;
    v4u pf;
    {
      f32x4 s0 = {0.f, 0.f, 0.f, 0.f}, s1 = {0.f, 0.f, 0.f, 0.f};
      const u16* qb = (const u16*)(lds + L_QB) + (c >> 2) * 128 + 8 * g;
#pragma unroll
      for (int ks = 0; ks < 4; ++ks) {
        const v4u qf = *(const v4u*)(qb + 32 * ks);
        s0 = mfma32(kf[ks], qf, s0);
        s1 = mfma32(kf[4 + ks], qf, s1);
      }
      float sc[8];
#pragma unroll
      for (int j = 0; j < 4; ++j) { sc[j] = bfr(bfr(s0[j]) * 0.08838834764831845f); sc[4 + j] = bfr(bfr(s1[j]) * 0.08838834764831845f); }
      m = fmaxf(fmaxf(fmaxf(sc[0], sc[1]), fmaxf(sc[2], sc[3])), fmaxf(fmaxf(sc[4], sc[5]), fmaxf(sc[6], sc[7])));
      m = x32_max(x16_max(m));
      float p[8];
      l = 0.f;
#pragma unroll
      for (int j = 0; j < 8; ++j) { p[j] = __builtin_amdgcn_exp2f((sc[j] - m) * LOG2E); l += p[j]; }
      pf = v4u{pack2(p[0], p[1]), pack2(p[2], p[3]), pack2(p[4], p[5]), pack2(p[6], p[7])};
      l = x32_sum(x16_sum(l));
      if (g == 0 && (c & 3) == 0) *(f32x2*)(lds + L_ML + (aw * 4 + (c >> 2)) * 2) = f32x2{m, l};
      KEEP(pf);
    }
    PIN();
    STAMP(4);
    {
      float ov[8];
#pragma unroll
      for (int i = 0; i < 8; ++i) {
        v4u vt;
#pragma unroll
        for (int jj = 0; jj < 4; ++jj)
          vt[jj] = (i & 1) ? perm_hi(vf[2 * jj][i >> 1], vf[2 * jj + 1][i >> 1]) : perm_lo(vf[2 * jj][i >> 1], vf[2 * jj + 1][i >> 1]);
        const f32x4 o = mfma32(pf, vt, f32x4{0.f, 0.f, 0.f, 0.f});
        ov[i] = o[0];
      }
      float* mo = lds + L_MO + (aw * 4 + g) * 128 + 8 * c;
      *(f32x4*)(mo) = f32x4{ov[0], ov[1], ov[2], ov[3]};
      *(f32x4*)(mo + 4) = f32x4{ov[4], ov[5], ov[6], ov[7]};
    }
    if (arrive(cnt + C_ATT, lane) != 3u) await<0>(cnt + C_ATT, 4u);
    STAMP(5);
    {
      f32x2 ml[4], ov[4];
#pragma unroll
      for (int u = 0; u < 4; ++u) { ml[u] = *(const f32x2*)(lds + L_ML + (u * 4 + aw) * 2); ov[u] = *(const f32x2*)(lds + L_MO + (u * 4 + aw) * 128 + 2 * lane); }
      const float M = fmaxf(fmaxf(ml[0][0], ml[1][0]), fmaxf(ml[2][0], ml[3][0]));
      float L = 0.f, o0 = 0.f, o1 = 0.f;
#pragma unroll
      for (int u = 0; u < 4; ++u) {
        const float f = __builtin_amdgcn_exp2f((ml[u][0] - M) * LOG2E);
        L = fmaf(f, ml[u][1], L); o0 = fmaf(f, ov[u][0], o0); o1 = fmaf(f, ov[u][1], o1);
      }
      const int hg = 4 * hk + aw;
      const v4u pv = {__float_as_uint(o0), T, __float_as_uint(o1), T};
      __builtin_amdgcn_raw_buffer_store_b128(pv, rs, WS_P2 + (((hg * 4 + (lane >> 4)) * 4 + ab) * 8 + as) * 256 + (lane & 15) * 16, 0, AUX_SC1);
      if (lane == 0) {
        const v4u mv = {__float_as_uint(M), T, __float_as_uint(L), T};
        __builtin_amdgcn_raw_buffer_store_b128(mv, rs, WS_ML + ((hg * 4 + ab) * 8 + as) * 16, 0, AUX_SC1);
      }
    }
    PIN();
    (void)arrive(cnt + C_PUB2, lane);
    STAMP(6);
    const int ob = bid >> 6, ocw = (bid >> 3) & 7, om = (bid >> 1) & 3, oh = bid & 1;
    const int col = 64 * ocw + 4 * (4 * om + (lane & 3)) + 2 * oh;
    const unsigned b2v = ldg<unsigned>(b2 + col);
    const unsigned off = WS_P3 + ((aw * 32 + (lane >> 2)) * 8 + ocw) * 2048 + oh * 1024 + (ob * 16 + 4 * om + (lane & 3)) * 16;
    await<2>(cnt + C_PUB3, 4u);
    __builtin_amdgcn_s_sleep(DLY3);
    STAMP(8);
    v4u pv[2];
#pragma unroll
    for (int i = 0; i < 2; ++i) pv[i] = __builtin_amdgcn_raw_buffer_load_b128(rs, off + i * (16 * 8 * 2048), 0, AUX_SC1);
    for (unsigned spins = 0; spins < SPIN_CAP; ++spins) {
      const unsigned bad0 = (pv[0][1] ^ T) | (pv[0][3] ^ T), bad1 = (pv[1][1] ^ T) | (pv[1][3] ^ T);
      if (__all((bad0 | bad1) == 0)) break;
      asm volatile("" ::: "memory");
      if (bad0) pv[0] = __builtin_amdgcn_raw_buffer_load_b128(rs, off, 0, AUX_SC1);
      if (bad1) pv[1] = __builtin_amdgcn_raw_buffer_load_b128(rs, off + 16 * 8 * 2048, 0, AUX_SC1);
    }
    STAMP(9);
    float sa = __uint_as_float(pv[0][0]) + __uint_as_float(pv[1][0]), sb = __uint_as_float(pv[0][2]) + __uint_as_float(pv[1][2]);
    sa += dpp<ROR4>(sa); sa += dpp<ROR8>(sa); sa = x32_sum(x16_sum(sa));
    sb += dpp<ROR4>(sb); sb += dpp<ROR8>(sb); sb = x32_sum(x16_sum(sb));
    if (lane < 4) *(f32x2*)(lds + L_FR + aw * 8 + 2 * lane) = f32x2{sa, sb};
    if (arrive(cnt + C_FIN, lane) == 3u) {
      if (lane < 4) {
        const float omg = lds[L_ST + 4 * ob + 3];
        f32x2 t = *(const f32x2*)(lds + L_FR + 2 * lane);
#pragma unroll
        for (int u = 1; u < 4; ++u) t += *(const f32x2*)(lds + L_FR + u * 8 + 2 * lane);
        *(GAS unsigned*)(out + ob * DM + col) = pack2(fmaf(omg, lo2f(b2v), t[0]), fmaf(omg, hi2f(b2v), t[1]));
      }
      if (bid == 0 && lane == 0) __hip_atomic_store((gu32*)ws, T, __ATOMIC_RELAXED, __HIP_MEMORY_SCOPE_AGENT);
    }
  } else {
    const int pw = w;
    SETPRIO(3);
    const bool has_mlp = kgl < 14;
    const int f0 = 224 * hk + 16 * kgl;
    const int kc = c >> 2, ch = c & 3;
    const int krow = 128 * pw + 32 * kc + 8 * g;
    v2u wv[8];
    {
      const u16* p = wq + (size_t)krow * NQ + hk * 512 + idx * 16 + 4 * ch;
#pragma unroll
      for (int j = 0; j < 8; ++j) wv[j] = ldnt<v2u>(p + j * NQ);
    }
    const v4u xa = ldg<v4u>(x + (c & 3) * DM + krow);
    const v4u xs = ldg<v4u>(x + pw * DM + 8 * lane);
    const v4u gwv = ldg<v4u>(gw + 8 * lane);
    if (tid < 16) cnt[tid] = 0u;
    BAR();
    {
      f32x4 d[4];
#pragma unroll
      for (int i = 0; i < 4; ++i) d[i] = mfma32(xa, tr8x4(wv, i), f32x4{0.f, 0.f, 0.f, 0.f});
      if (g == kc) {
#pragma unroll
        for (int r = 0; r < 4; ++r)
          *(f32x4*)(lds + L_RQ + ((pw * 4 + kc) * 4 + r) * 16 + 4 * ch) = f32x4{d[0][r], d[1][r], d[2][r], d[3][r]};
      }
      if (arrive(cnt + C_Q, lane) == 3u) {
        const float* red = lds + L_RQ + (g * 16 + (c >> 2)) * 16 + 4 * ch;
        f32x4 a = *(const f32x4*)(red);
#pragma unroll
        for (int p = 1; p < 4; ++p) a += *(const f32x4*)(red + p * 64);
#pragma unroll
        for (int i = 0; i < 4; ++i) a[i] = x32_sum(x16_sum(a[i]));
        if (g == 0) {
          const v4u qv = {pack2(a[0], a[1]), T, pack2(a[2], a[3]), T};
          __builtin_amdgcn_raw_buffer_store_b128(qv, rs, WS_QG + (hk * 32 + idx) * 256 + c * 16, 0, AUX_SC1);
        }
      }
    }
    STAMP(1);
    {
      float xv[8], gv[8];
#pragma unroll
      for (int k = 0; k < 4; ++k) { xv[2 * k] = lo2f(xs[k]); xv[2 * k + 1] = hi2f(xs[k]); gv[2 * k] = lo2f(gwv[k]); gv[2 * k + 1] = hi2f(gwv[k]); }
      float s = 0.f, gs = 0.f;
#pragma unroll
      for (int k = 0; k < 8; ++k) { s += xv[k]; gs += bfr(xv[k] * gv[k]); }
      s = wave_sum(s); gs = wave_sum(gs);
      const float mean = s * (1.0f / DM);
      float vs = 0.f;
#pragma unroll
      for (int k = 0; k < 8; ++k) { const float d = xv[k] - mean; vs = fmaf(d, d, vs); }
      vs = wave_sum(vs);
      if (lane == 0) {
        const float var_b = bfr(vs * (1.0f / DM));
        const float den = bfr(sqrtf(bfr(var_b + 1e-5f)));
        const float gt = bfr(1.0f / (1.0f + __expf(-bfr(gs))));
        *(f32x4*)(lds + L_ST + 4 * pw) = f32x4{bfr(mean), den, gt, bfr(1.0f - gt)};
      }
      (void)arrive(cnt + C_ST, lane);
    }
    STAMP(2);
    if (pw == 0) {
      const unsigned qoff = WS_QG + ((hk * 32 + (lane >> 1)) * 4 + ab) * 64 + (lane & 1) * 32;
      v4u q0 = __builtin_amdgcn_raw_buffer_load_b128(rs, qoff, 0, AUX_SC1);
      v4u q1 = __builtin_amdgcn_raw_buffer_load_b128(rs, qoff + 16, 0, AUX_SC1);
      for (unsigned spins = 0; spins < SPIN_CAP; ++spins) {
        const bool ok = ((q0[1] ^ T) | (q0[3] ^ T) | (q1[1] ^ T) | (q1[3] ^ T)) == 0;
        if (__all(ok)) break;
        asm volatile("" ::: "memory");
        if (!ok) {
          q0 = __builtin_amdgcn_raw_buffer_load_b128(rs, qoff, 0, AUX_SC1);
          q1 = __builtin_amdgcn_raw_buffer_load_b128(rs, qoff + 16, 0, AUX_SC1);
        }
      }
      *(v4u*)((u16*)(lds + L_QB) + 8 * lane) = v4u{q0[0], q0[2], q1[0], q1[2]};
      (void)arrive(cnt + C_QRDY, lane);
    }
    STAMP(3);
    await<4>(cnt + C_ISS, 4u);
    SETPRIO(0);
    const __amdgpu_buffer_rsrc_t rw1 = __builtin_amdgcn_make_buffer_rsrc((void*)w1, 0, has_mlp ? DM * FF * 2 : 0, 0x00020000);
    const __amdgpu_buffer_rsrc_t rb1 = __builtin_amdgcn_make_buffer_rsrc((void*)b1, 0, has_mlp ? FF * 2 : 0, 0x00020000);
    const __amdgpu_buffer_rsrc_t rw2 = __builtin_amdgcn_make_buffer_rsrc((void*)w2, 0, has_mlp ? DM * FF * 2 : 0, 0x00020000);
    v2u wov[8], w2v[4], b1v;
    {
      const int o = (krow * FF + f0 + 4 * ch) * 2;
      const u16* p = wo + (size_t)(32 * kg + 8 * g) * DM + 256 * cg + 64 * pw + 4 * c;
      const int o2 = ((f0 + 4 * g) * DM + 256 * cg + 64 * pw + 4 * c) * 2;
#pragma unroll
      for (int j = 0; j < 8; ++j) {
        if (j >= PWIN) { PIN(); asm volatile("s_waitcnt vmcnt(%0)" :: "n"(PWIN - 1) : "memory"); PIN(); }
        wv[j] = __builtin_amdgcn_raw_buffer_load_b64(rw1, o + j * FF * 2, 0, AUX_NT);
      }
      PIN();
      b1v = __builtin_amdgcn_raw_buffer_load_b64(rb1, (f0 + 4 * ch) * 2, 0, 0);
#pragma unroll
      for (int j = 0; j < 8; ++j) wov[j] = ldnt<v2u>(p + j * DM);
#pragma unroll
      for (int j = 0; j < 4; ++j) w2v[j] = __builtin_amdgcn_raw_buffer_load_b64(rw2, o2 + j * DM * 2, 0, AUX_NT);
    }
    PIN();
    SETPRIO(3);
    await<1>(cnt + C_ST, 4u);
    v4u ya;
    {
      const f32x4 st = *(const f32x4*)(lds + L_ST + 4 * (c & 3));
#pragma unroll
      for (int k = 0; k < 4; ++k) {
        const float y0 = bfr(bfr(lo2f(xa[k]) - st[0]) / st[1]), y1 = bfr(bfr(hi2f(xa[k]) - st[0]) / st[1]);
        ya[k] = pack2(y0, y1);
      }
    }
    f32x4 stg = *(const f32x4*)(lds + L_ST + 4 * g);
    KEEP(stg);
    STAMP(4);
    {
      f32x4 d[4];
#pragma unroll
      for (int i = 0; i < 4; ++i) d[i] = mfma32(ya, tr8x4(wv, i), f32x4{0.f, 0.f, 0.f, 0.f});
      if (g == kc) {
#pragma unroll
        for (int r = 0; r < 4; ++r)
          *(f32x4*)(lds + L_RH + ((pw * 4 + kc) * 4 + r) * 16 + 4 * ch) = f32x4{d[0][r], d[1][r], d[2][r], d[3][r]};
      }
      if (arrive(cnt + C_H, lane) == 3u) {
        const float* red = lds + L_RH + (g * 16 + (c >> 2)) * 16 + 4 * ch;
        f32x4 a = *(const f32x4*)(red);
#pragma unroll
        for (int p = 1; p < 4; ++p) a += *(const f32x4*)(red + p * 64);
#pragma unroll
        for (int i = 0; i < 4; ++i) a[i] = x32_sum(x16_sum(a[i]));
        const float bb[4] = {lo2f(b1v[0]), hi2f(b1v[0]), lo2f(b1v[1]), hi2f(b1v[1])};
        const float av = g == 0 ? a[0] : g == 1 ? a[1] : g == 2 ? a[2] : a[3];
        const float bv = g == 0 ? bb[0] : g == 1 ? bb[1] : g == 2 ? bb[2] : bb[3];
        const float u = bfr(bfr(av) + bv);
        const float inner = 0.7978845608028654f * (u + 0.044715f * u * u * u);
        const float hv = 0.5f * u * (1.0f + tanhf(inner));
        ((u16*)(lds + L_HB))[(c >> 2) * 16 + 4 * ch + g] = (u16)bfbits(hv);
        (void)arrive(cnt + C_HRDY, lane);
      }
    }
    v4u wof[4];
    float accm[4];
    {
      v2u w2f[4];
#pragma unroll
      for (int i = 0; i < 4; ++i) {
        wof[i] = tr8x4(wov, i);
#pragma unroll
        for (int jj = 0; jj < 2; ++jj)
          w2f[i][jj] = (i & 1) ? perm_hi(w2v[2 * jj][i >> 1], w2v[2 * jj + 1][i >> 1]) : perm_lo(w2v[2 * jj][i >> 1], w2v[2 * jj + 1][i >> 1]);
        KEEP(wof[i]);
      }
      await<1>(cnt + C_HRDY, 1u);
      const v2u ha = *(const v2u*)((const u16*)(lds + L_HB) + (c >> 2) * 16 + 4 * g);
#pragma unroll
      for (int i = 0; i < 4; ++i) {
        const f32x4 e = mfma16(ha, w2f[i], f32x4{0.f, 0.f, 0.f, 0.f});
        accm[i] = stg[3] * e[0];
        KEEP(accm[i]);
      }
    }
    PIN();
    STAMP(5);
    const int h2 = 4 * hk + (kgl >> 2);
    const unsigned p2off = WS_P2 + (((h2 * 4 + (kgl & 3)) * 4 + pw) * 8 + (lane >> 3)) * 256 + (lane & 7) * 32;
    const unsigned mloff = WS_ML + ((h2 * 4 + pw) * 8 + (lane >> 3)) * 16;
    await<1>(cnt + C_PUB2, 4u);
    __builtin_amdgcn_s_sleep(DLY2);
    STAMP(6);
    v4u a0 = __builtin_amdgcn_raw_buffer_load_b128(rs, p2off, 0, AUX_SC1);
    v4u a1 = __builtin_amdgcn_raw_buffer_load_b128(rs, p2off + 16, 0, AUX_SC1);
    v4u am = __builtin_amdgcn_raw_buffer_load_b128(rs, mloff, 0, AUX_SC1);
    for (unsigned spins = 0; spins < SPIN_CAP; ++spins) {
      const bool ok = ((a0[1] ^ T) | (a0[3] ^ T) | (a1[1] ^ T) | (a1[3] ^ T) | (am[1] ^ T) | (am[3] ^ T)) == 0;
      if (__all(ok)) break;
      asm volatile("" ::: "memory");
      if (!ok) {
        a0 = __builtin_amdgcn_raw_buffer_load_b128(rs, p2off, 0, AUX_SC1);
        a1 = __builtin_amdgcn_raw_buffer_load_b128(rs, p2off + 16, 0, AUX_SC1);
        am = __builtin_amdgcn_raw_buffer_load_b128(rs, mloff, 0, AUX_SC1);
      }
    }
    STAMP(7);
    {
      const float m = __uint_as_float(am[0]), l = __uint_as_float(am[2]);
      float M = fmaxf(m, dpp<ROR8>(m)); M = x32_max(x16_max(M));
      const float f = __builtin_amdgcn_exp2f((m - M) * LOG2E);
      float v[5] = {f * __uint_as_float(a0[0]), f * __uint_as_float(a0[2]), f * __uint_as_float(a1[0]), f * __uint_as_float(a1[2]), f * l};
#pragma unroll
      for (int i = 0; i < 5; ++i) { v[i] += dpp<ROR8>(v[i]); v[i] = x32_sum(x16_sum(v[i])); }
      const float inv = __builtin_amdgcn_rcpf(v[4]);
      if (lane < 8) *(v2u*)((u16*)(lds + L_OB) + pw * 32 + 4 * lane) = v2u{pack2(v[0] * inv, v[1] * inv), pack2(v[2] * inv, v[3] * inv)};
    }
    if (arrive(cnt + C_O, lane) != 3u) await<0>(cnt + C_O, 4u);
    {
      const v4u oa = *(const v4u*)((const u16*)(lds + L_OB) + (c >> 2) * 32 + 8 * g);
      float val[4];
#pragma unroll
      for (int i = 0; i < 4; ++i) {
        const f32x4 d = mfma32(oa, wof[i], f32x4{0.f, 0.f, 0.f, 0.f});
        val[i] = fmaf(stg[2], d[0], accm[i]);
      }
      const unsigned off = WS_P3 + ((kg * 2 + cg) * 4 + pw) * 2048 + lane * 16;
      const v4u s0 = {__float_as_uint(val[0]), T, __float_as_uint(val[1]), T};
      const v4u s1 = {__float_as_uint(val[2]), T, __float_as_uint(val[3]), T};
      __builtin_amdgcn_raw_buffer_store_b128(s0, rs, off, 0, AUX_SC1);
      __builtin_amdgcn_raw_buffer_store_b128(s1, rs, off + 1024, 0, AUX_SC1);
    }
    PIN();
    (void)arrive(cnt + C_PUB3, lane);
    STAMP(8);
  }
  STAMP_DUMP();
}

extern "C" void kernel_launch(void* const* d_in, const int* in_sizes, int n_in,
                              void* d_out, int out_size, void* d_ws, size_t ws_size,
                              hipStream_t stream) {
  (void)in_sizes; (void)n_in; (void)out_size; (void)ws_size;
  fused_kernel<<<256, 512, 0, stream>>>(
      (const u16*)d_in[0], (const u16*)d_in[1], (const u16*)d_in[2], (const u16*)d_in[3], (const u16*)d_in[4],
      (const u16*)d_in[6], (const u16*)d_in[7], (const u16*)d_in[8], (const u16*)d_in[9], (const u16*)d_in[10],
      (u16*)d_out, (unsigned char*)d_ws);
}
```

```cpp
#include <hip/hip_runtime.h>
#include <hip/hip_bf16.h>
#include <stdint.h>

#define GAS __attribute__((address_space(1)))
#define LAS __attribute__((address_space(3)))
typedef unsigned short u16;
typedef unsigned v4u __attribute__((ext_vector_type(4)));
typedef unsigned v2u __attribute__((ext_vector_type(2)));
typedef float f32x4 __attribute__((ext_vector_type(4)));
typedef float f32x2 __attribute__((ext_vector_type(2)));
typedef __bf16 bf16x8 __attribute__((ext_vector_type(8)));
typedef __bf16 bf16x2 __attribute__((ext_vector_type(2)));
typedef short s16x4 __attribute__((ext_vector_type(4)));
typedef GAS unsigned gu32;

constexpr int DM = 512, SK = 4096, DH = 128, WINDOW = 1024, FF = 1792, NQ = 4096;
constexpr unsigned WS_QP = 4096;
constexpr unsigned WS_HP = WS_QP + (512u << 10);
constexpr unsigned WS_P2 = WS_HP + (256u << 10);
constexpr unsigned WS_ML = WS_P2 + (1u << 20);
constexpr unsigned WS_P3 = WS_ML + 16384;
constexpr unsigned WS_END = WS_P3 + (2u << 20);
constexpr unsigned SPIN_CAP = 1u << 17;
constexpr unsigned LSPIN_CAP = 1u << 22;
constexpr int AUX_SC1 = 16, AUX_NT = 2;
constexpr float LOG2E = 1.4426950408889634f;
constexpr float SCL2 = 0.08838834764831845f * 1.4426950408889634f;

#define AWIN 6
#define DLY1 16
#define DLY2 8
#define DLY3 10
#define SETPRIO(n) __builtin_amdgcn_s_setprio(n)

__device__ __forceinline__ unsigned pack2(float lo, float hi) {
  return __builtin_bit_cast(unsigned, __builtin_convertvector(f32x2{lo, hi}, bf16x2));
}
__device__ __forceinline__ float bfr(float f) { return (float)(__bf16)f; }
__device__ __forceinline__ float lo2f(unsigned u) { return __builtin_bit_cast(float, u << 16); }
__device__ __forceinline__ float hi2f(unsigned u) { return __builtin_bit_cast(float, u & 0xffff0000u); }
__device__ __forceinline__ unsigned perm_lo(unsigned a, unsigned b) { return __builtin_amdgcn_perm(b, a, 0x05040100u); }
__device__ __forceinline__ unsigned perm_hi(unsigned a, unsigned b) { return __builtin_amdgcn_perm(b, a, 0x07060302u); }

template <int CTRL> __device__ __forceinline__ float dpp(float x) {
  return __builtin_bit_cast(float, __builtin_amdgcn_mov_dpp(__builtin_bit_cast(int, x), CTRL, 0xf, 0xf, true));
}
constexpr int XOR1 = 0xB1, XOR2 = 0x4E, ROR8 = 0x128, ROR4 = 0x124;
__device__ __forceinline__ float x16_max(float x) {
  auto s = __builtin_amdgcn_permlane16_swap(__float_as_uint(x), __float_as_uint(x), false, false);
  return __builtin_fmaxf(__uint_as_float(s[0]), __uint_as_float(s[1]));
}
__device__ __forceinline__ float x32_max(float x) {
  auto s = __builtin_amdgcn_permlane32_swap(__float_as_uint(x), __float_as_uint(x), false, false);
  return __builtin_fmaxf(__uint_as_float(s[0]), __uint_as_float(s[1]));
}
__device__ __forceinline__ float x16_sum(float x) {
  auto s = __builtin_amdgcn_permlane16_swap(__float_as_uint(x), __float_as_uint(x), false, false);
  return __uint_as_float(s[0]) + __uint_as_float(s[1]);
}
__device__ __forceinline__ float x32_sum(float x) {
  auto s = __builtin_amdgcn_permlane32_swap(__float_as_uint(x), __float_as_uint(x), false, false);
  return __uint_as_float(s[0]) + __uint_as_float(s[1]);
}
__device__ __forceinline__ float wave_sum(float x) {
  x += dpp<XOR1>(x); x += dpp<XOR2>(x); x += dpp<ROR4>(x); x += dpp<ROR8>(x);
  return x32_sum(x16_sum(x));
}
__device__ __forceinline__ f32x4 mfma32(v4u a, v4u b, f32x4 c) {
  return __builtin_amdgcn_mfma_f32_16x16x32_bf16(__builtin_bit_cast(bf16x8, a), __builtin_bit_cast(bf16x8, b), c, 0, 0, 0);
}
__device__ __forceinline__ f32x4 mfma16(v2u a, v2u b, f32x4 c) {
  return __builtin_amdgcn_mfma_f32_16x16x16bf16_1k(__builtin_bit_cast(s16x4, a), __builtin_bit_cast(s16x4, b), c, 0, 0, 0);
}
#define PIN() __builtin_amdgcn_sched_barrier(0)
#define BAR() do { PIN(); asm volatile("s_waitcnt lgkmcnt(0)" ::: "memory"); __builtin_amdgcn_s_barrier(); asm volatile("" ::: "memory"); PIN(); } while (0)
#define KEEP(x) asm volatile("" : "+v"(x))
#define STAMP(i)
#define STAMP_INIT()
#define STAMP_DUMP()

template <typename T> __device__ __forceinline__ T ldg(const u16* p) { return *(const GAS T*)p; }
template <typename T> __device__ __forceinline__ T ldnt(const u16* p) { return __builtin_nontemporal_load((const GAS T*)p); }

constexpr int L_CNT = 0;
constexpr int L_ST = 16;
constexpr int L_RQ = 32;
constexpr int L_RH = L_RQ + 1024;
constexpr int L_QS = L_RH + 1024;
constexpr int L_HB = L_QS + 2048;
constexpr int L_MO = L_HB + 32;
constexpr int L_ML = L_MO + 2048;
constexpr int L_OB = L_ML + 32;
constexpr int L_QB = L_OB + 64;
constexpr int L_FR = L_QB + 256;
constexpr int L_KS = L_FR + 32;
constexpr int L_END = L_KS + 8192;
enum { C_Q = 0, C_QG, C_QRDY, C_ISS, C_ATT, C_PUB2, C_H, C_HRDY, C_O, C_PUB3, C_FIN, C_ST, C_QPUB };

__device__ __forceinline__ unsigned arrive(unsigned addr, int lane) {
  unsigned old = 0;
  if (lane == 0)
    asm volatile("s_waitcnt lgkmcnt(0)\n\tds_add_rtn_u32 %0, %1, %2\n\ts_waitcnt lgkmcnt(0)" : "=v"(old) : "v"(addr), "v"(1u) : "memory");
  return __builtin_amdgcn_readfirstlane(old);
}
__device__ __forceinline__ void signal(unsigned addr, int lane) {
  if (lane == 0) asm volatile("s_waitcnt lgkmcnt(0)\n\tds_add_u32 %0, %1" :: "v"(addr), "v"(1u) : "memory");
}
template <int SLEEP> __device__ __forceinline__ void await(unsigned addr, unsigned n) {
#pragma clang loop unroll(disable)
  for (unsigned s = 0; s < LSPIN_CAP; ++s) {
    unsigned v;
    asm volatile("ds_read_b32 %0, %1\n\ts_waitcnt lgkmcnt(0)" : "=v"(v) : "v"(addr) : "memory");
    if ((unsigned)__builtin_amdgcn_readfirstlane(v) >= n) break;
    if (SLEEP) __builtin_amdgcn_s_sleep(SLEEP);
  }
}

__device__ __forceinline__ v4u tr8x4(const v2u (&W)[8], int i) {
  v4u f;
#pragma unroll
  for (int jj = 0; jj < 4; ++jj)
    f[jj] = (i & 1) ? perm_hi(W[2 * jj][i >> 1], W[2 * jj + 1][i >> 1]) : perm_lo(W[2 * jj][i >> 1], W[2 * jj + 1][i >> 1]);
  return f;
}
__device__ __forceinline__ unsigned tagbad(v4u v, unsigned T) { return (v[1] ^ T) | (v[3] ^ T); }

__global__ void __launch_bounds__(512, 2)
fused_kernel(const u16* __restrict__ x, const u16* __restrict__ kcache, const u16* __restrict__ vcache,
             const u16* __restrict__ wq, const u16* __restrict__ wo, const u16* __restrict__ w1,
             const u16* __restrict__ b1, const u16* __restrict__ w2, const u16* __restrict__ b2,
             const u16* __restrict__ gw, u16* __restrict__ out, unsigned char* ws) {
  __shared__ __attribute__((aligned(16))) float lds[L_END];
  const int tid = threadIdx.x, lane = tid & 63, w = __builtin_amdgcn_readfirstlane(tid >> 6);
  const int g = lane >> 4, c = lane & 15;
  const int bid = blockIdx.x, hk = bid & 7, idx = bid >> 3;
  const int ab = idx >> 3, as = idx & 7;
  const int kgl = idx >> 1, cg = idx & 1, kg = hk * 16 + kgl;
  const __amdgpu_buffer_rsrc_t rs = __builtin_amdgcn_make_buffer_rsrc((void*)ws, 0, (int)WS_END, 0x00020000);
  const unsigned cn = (unsigned)(size_t)(LAS float*)(lds + L_CNT);
#define CN(i) (cn + 4u * (i))
  STAMP_INIT();
  STAMP(0);
  const unsigned T = *(const GAS unsigned*)ws + 1u;

  if (w >= 4) {
    const int aw = w - 4;
    BAR();
    unsigned tt;
    {
      const unsigned char* tp = ws;
      tp = lane == 0 ? (const unsigned char*)(out + (bid >> 6) * DM) : tp;
      tp = lane == 1 ? ws + WS_P3 : tp;
      tp = lane == 2 ? ws + WS_END - 64 : tp;
      tp = lane == 3 ? (const unsigned char*)b2 : tp;
      tp = lane == 4 ? (const unsigned char*)(wo + (size_t)(32 * kg) * DM) : tp;
      tp = lane == 5 ? (const unsigned char*)(w2 + (size_t)(224 * hk) * DM) : tp;
      tp = lane == 6 ? (const unsigned char*)b1 : tp;
      tp = lane == 7 ? ws + WS_P2 : tp;
      tt = *(const GAS unsigned*)tp;
    }
    PIN();
    v4u kr[8], vf[8];
    {
      const size_t kvoff = ((size_t)(ab * 8 + hk) * SK + (SK - WINDOW) + 128 * as + 32 * aw) * DH;
      const u16* kp = kcache + kvoff + 8 * lane;
      const u16* vp = vcache + kvoff + 4 * g * DH + 8 * c;
#pragma unroll
      for (int i = 0; i < 16; ++i) {
        if (i >= AWIN) {
          PIN();
          asm volatile("s_waitcnt vmcnt(%0)" :: "n"(AWIN - 1) : "memory");
          PIN();
        }
        if (i < 8) kr[i] = ldnt<v4u>(kp + i * 512);
        else { const int j = i - 8; vf[j] = ldnt<v4u>(vp + (16 * (j >> 2) + (j & 3)) * DH); }
      }
    }
    PIN();
    SETPRIO(2);
    asm volatile("" :: "v"(tt));
    signal(CN(C_ISS), lane);
    STAMP(1);
    v4u kf[8];
    {
      unsigned char* ks_ = (unsigned char*)(lds + L_KS) + aw * 8192;
#pragma unroll
      for (int i = 0; i < 8; ++i) { const int row = 4 * i + g; *(v4u*)(ks_ + row * 256 + ((c ^ (row & 15)) << 4)) = kr[i]; }
      asm volatile("s_waitcnt lgkmcnt(0)" ::: "memory");
#pragma unroll
      for (int t = 0; t < 2; ++t)
#pragma unroll
        for (int ks = 0; ks < 4; ++ks) kf[t * 4 + ks] = *(const v4u*)(ks_ + (16 * t + c) * 256 + (((4 * ks + g) ^ c) << 4));
#pragma unroll
      for (int i = 0; i < 8; ++i) KEEP(kf[i]);
    }
    PIN();
    STAMP(2);
    await<0>(CN(C_QRDY), 1u);
    STAMP(3);
    v4u pf;
    {
      f32x4 s0 = {0.f, 0.f, 0.f, 0.f}, s1 = {0.f, 0.f, 0.f, 0.f};
      const u16* qb = (const u16*)(lds + L_QB) + (c >> 2) * 128 + 8 * g;
      v4u qf[4];
#pragma unroll
      for (int ks = 0; ks < 4; ++ks) qf[ks] = *(const v4u*)(qb + 32 * ks);
#pragma unroll
      for (int ks = 0; ks < 4; ++ks) {
        s0 = mfma32(kf[ks], qf[ks], s0);
        s1 = mfma32(kf[4 + ks], qf[ks], s1);
      }
      float m = __builtin_fmaxf(__builtin_fmaxf(__builtin_fmaxf(s0[0], s0[1]), __builtin_fmaxf(s0[2], s0[3])),
                                __builtin_fmaxf(__builtin_fmaxf(s1[0], s1[1]), __builtin_fmaxf(s1[2], s1[3])));
      m = x32_max(x16_max(m));
      float p[8];
#pragma unroll
      for (int j = 0; j < 4; ++j) { p[j] = __builtin_amdgcn_exp2f((s0[j] - m) * SCL2); p[4 + j] = __builtin_amdgcn_exp2f((s1[j] - m) * SCL2); }
      pf = v4u{pack2(p[0], p[1]), pack2(p[2], p[3]), pack2(p[4], p[5]), pack2(p[6], p[7])};
      float l = ((p[0] + p[1]) + (p[2] + p[3])) + ((p[4] + p[5]) + (p[6] + p[7]));
      l = x32_sum(x16_sum(l));
      if (g == 0 && (c & 3) == 0) *(f32x2*)(lds + L_ML + (aw * 4 + (c >> 2)) * 2) = f32x2{m * SCL2, l};
      KEEP(pf);
    }
    PIN();
    STAMP(4);
    {
      v4u vt[8];
#pragma unroll
      for (int i = 0; i < 8; ++i)
#pragma unroll
        for (int jj = 0; jj < 4; ++jj)
          vt[i][jj] = (i & 1) ? perm_hi(vf[2 * jj][i >> 1], vf[2 * jj + 1][i >> 1]) : perm_lo(vf[2 * jj][i >> 1], vf[2 * jj + 1][i >> 1]);
      f32x4 o[8];
#pragma unroll
      for (int i = 0; i < 8; ++i) o[i] = mfma32(pf, vt[i], f32x4{0.f, 0.f, 0.f, 0.f});
#pragma unroll
      for (int i = 0; i < 8; ++i) KEEP(o[i]);
      float* mo = lds + L_MO + (aw * 4 + g) * 128 + 8 * c;
      *(f32x4*)(mo) = f32x4{o[0][0], o[1][0], o[2][0], o[3][0]};
      *(f32x4*)(mo + 4) = f32x4{o[4][0], o[5][0], o[6][0], o[7][0]};
    }
    if (arrive(CN(C_ATT), lane) != 3u) await<0>(CN(C_ATT), 4u);
    STAMP(5);
    {
      f32x2 ml[4], ov[4];
#pragma unroll
      for (int u = 0; u < 4; ++u) { ml[u] = *(const f32x2*)(lds + L_ML + (u * 4 + aw) * 2); ov[u] = *(const f32x2*)(lds + L_MO + (u * 4 + aw) * 128 + 2 * lane); }
      const float M = __builtin_fmaxf(__builtin_fmaxf(ml[0][0], ml[1][0]), __builtin_fmaxf(ml[2][0], ml[3][0]));
      float L = 0.f, o0 = 0.f, o1 = 0.f;
#pragma unroll
      for (int u = 0; u < 4; ++u) {
        const float f = __builtin_amdgcn_exp2f(ml[u][0] - M);
        L = fmaf(f, ml[u][1], L); o0 = fmaf(f, ov[u][0], o0); o1 = fmaf(f, ov[u][1], o1);
      }
      const int hg = 4 * hk + aw;
      const v4u pv = {__float_as_uint(o0), T, __float_as_uint(o1), T};
      __builtin_amdgcn_raw_buffer_store_b128(pv, rs, WS_P2 + (((hg * 4 + (lane >> 4)) * 4 + ab) * 8 + as) * 256 + (lane & 15) * 16, 0, AUX_SC1);
      if (lane == 0) {
        const v4u mv = {__float_as_uint(M), T, __float_as_uint(L), T};
        __builtin_amdgcn_raw_buffer_store_b128(mv, rs, WS_ML + ((hg * 4 + ab) * 8 + as) * 16, 0, AUX_SC1);
      }
    }
    PIN();
    signal(CN(C_PUB2), lane);
    STAMP(6);
    const int ob = bid >> 6, ocw = (bid >> 3) & 7, om = (bid >> 1) & 3, oh = bid & 1;
    const int col = 64 * ocw + 4 * (4 * om + (lane & 3)) + 2 * oh;
    const unsigned b2v = ldg<unsigned>(b2 + col);
    const unsigned off = WS_P3 + ((aw * 32 + (lane >> 2)) * 8 + ocw) * 2048 + oh * 1024 + (ob * 16 + 4 * om + (lane & 3)) * 16;
    await<2>(CN(C_PUB3), 4u);
    __builtin_amdgcn_s_sleep(DLY3);
    STAMP(8);
    v4u pv0 = __builtin_amdgcn_raw_buffer_load_b128(rs, off, 0, AUX_SC1);
    v4u pv1 = __builtin_amdgcn_raw_buffer_load_b128(rs, off + 16 * 8 * 2048, 0, AUX_SC1);
#pragma clang loop unroll(disable)
    for (unsigned spins = 0; spins < SPIN_CAP; ++spins) {
      const unsigned bad0 = tagbad(pv0, T), bad1 = tagbad(pv1, T);
      if (__all((bad0 | bad1) == 0)) break;
      asm volatile("" ::: "memory");
      if (bad0) pv0 = __builtin_amdgcn_raw_buffer_load_b128(rs, off, 0, AUX_SC1);
      if (bad1) pv1 = __builtin_amdgcn_raw_buffer_load_b128(rs, off + 16 * 8 * 2048, 0, AUX_SC1);
    }
    STAMP(9);
    float sa = __uint_as_float(pv0[0]) + __uint_as_float(pv1[0]), sb = __uint_as_float(pv0[2]) + __uint_as_float(pv1[2]);
    sa += dpp<ROR4>(sa); sb += dpp<ROR4>(sb); sa += dpp<ROR8>(sa); sb += dpp<ROR8>(sb);
    sa = x16_sum(sa); sb = x16_sum(sb); sa = x32_sum(sa); sb = x32_sum(sb);
    if (lane < 4) *(f32x2*)(lds + L_FR + aw * 8 + 2 * lane) = f32x2{sa, sb};
    if (arrive(CN(C_FIN), lane) == 3u) {
      if (lane < 4) {
        const float omg = lds[L_ST + 4 * ob + 3];
        f32x2 t = *(const f32x2*)(lds + L_FR + 2 * lane);
#pragma unroll
        for (int u = 1; u < 4; ++u) t += *(const f32x2*)(lds + L_FR + u * 8 + 2 * lane);
        *(GAS unsigned*)(out + ob * DM + col) = pack2(fmaf(omg, lo2f(b2v), t[0]), fmaf(omg, hi2f(b2v), t[1]));
      }
      if (bid == 0 && lane == 0) __hip_atomic_store((gu32*)ws, T, __ATOMIC_RELAXED, __HIP_MEMORY_SCOPE_AGENT);
    }
  } else {
    const int pw = w;
    SETPRIO(3);
    const int kq = idx & 3, cq = idx >> 2;
    const bool has_ht = idx < 14;
    const int th = 14 * hk + idx;
    const int qrow = 128 * kq + 32 * pw + 8 * g, hrow = 128 * (th & 3) + 32 * pw + 8 * g;
    v2u wv[8], w1v[8];
    {
      const u16* p = wq + (size_t)qrow * NQ + hk * 512 + 64 * cq + 4 * c;
#pragma unroll
      for (int j = 0; j < 8; ++j) wv[j] = ldnt<v2u>(p + j * NQ);
    }
    const v4u xa = ldg<v4u>(x + (c >> 2) * DM + qrow);
    const v4u xs = ldg<v4u>(x + pw * DM + 8 * lane);
    const v4u gwv = ldg<v4u>(gw + 8 * lane);
    const v4u xh = ldg<v4u>(x + (c >> 2) * DM + hrow);
    {
      const __amdgpu_buffer_rsrc_t rw1 = __builtin_amdgcn_make_buffer_rsrc((void*)w1, 0, has_ht ? DM * FF * 2 : 0, 0x00020000);
      const int o = (hrow * FF + 64 * (th >> 2) + 4 * c) * 2;
#pragma unroll
      for (int j = 0; j < 8; ++j) w1v[j] = __builtin_amdgcn_raw_buffer_load_b64(rw1, o + j * FF * 2, 0, AUX_NT);
    }
    if (tid < 16) ((LAS unsigned*)(lds + L_CNT))[tid] = 0u;
    BAR();
    {
      f32x4 d[4];
#pragma unroll
      for (int i = 0; i < 4; ++i) d[i] = mfma32(xa, tr8x4(wv, i), f32x4{0.f, 0.f, 0.f, 0.f});
      *(f32x4*)(lds + L_RQ + (pw * 64 + lane) * 4) = f32x4{d[0][0], d[1][0], d[2][0], d[3][0]};
      if (arrive(CN(C_Q), lane) == 3u) {
        f32x4 a = *(const f32x4*)(lds + L_RQ + lane * 4);
#pragma unroll
        for (int p = 1; p < 4; ++p) a += *(const f32x4*)(lds + L_RQ + (p * 64 + lane) * 4);
        const unsigned off = WS_QP + ((hk * 8 + cq) * 4 + kq) * 2048 + lane * 16;
        __builtin_amdgcn_raw_buffer_store_b128(v4u{__float_as_uint(a[0]), T, __float_as_uint(a[1]), T}, rs, off, 0, AUX_SC1);
        __builtin_amdgcn_raw_buffer_store_b128(v4u{__float_as_uint(a[2]), T, __float_as_uint(a[3]), T}, rs, off + 1024, 0, AUX_SC1);
      }
    }
    PIN();
    STAMP(1);
    {
      float xv[8], gv[8];
#pragma unroll
      for (int k = 0; k < 4; ++k) { xv[2 * k] = lo2f(xs[k]); xv[2 * k + 1] = hi2f(xs[k]); gv[2 * k] = lo2f(gwv[k]); gv[2 * k + 1] = hi2f(gwv[k]); }
      float s = 0.f, gs = 0.f;
#pragma unroll
      for (int k = 0; k < 8; ++k) { s += xv[k]; gs += bfr(xv[k] * gv[k]); }
      s = wave_sum(s); gs = wave_sum(gs);
      const float mean = s * (1.0f / DM);
      float vs = 0.f;
#pragma unroll
      for (int k = 0; k < 8; ++k) { const float d = xv[k] - mean; vs = fmaf(d, d, vs); }
      vs = wave_sum(vs);
      if (lane == 0) {
        const float var_b = bfr(vs * (1.0f / DM));
        const float den = bfr(sqrtf(bfr(var_b + 1e-5f)));
        const float gt = bfr(1.0f / (1.0f + __expf(-bfr(gs))));
        *(f32x4*)(lds + L_ST + 4 * pw) = f32x4{bfr(mean), den, gt, bfr(1.0f - gt)};
      }
      if (arrive(CN(C_ST), lane) != 3u) await<0>(CN(C_ST), 4u);
    }
    if (has_ht) {
      const f32x4 st = *(const f32x4*)(lds + L_ST + 4 * (c >> 2));
      v4u ya;
#pragma unroll
      for (int k = 0; k < 4; ++k) ya[k] = pack2(bfr(lo2f(xh[k]) - st[0]) / st[1], bfr(hi2f(xh[k]) - st[0]) / st[1]);
      f32x4 d[4];
#pragma unroll
      for (int i = 0; i < 4; ++i) d[i] = mfma32(ya, tr8x4(w1v, i), f32x4{0.f, 0.f, 0.f, 0.f});
      *(f32x4*)(lds + L_RH + (pw * 64 + lane) * 4) = f32x4{d[0][0], d[1][0], d[2][0], d[3][0]};
      if (arrive(CN(C_H), lane) == 3u) {
        f32x4 a = *(const f32x4*)(lds + L_RH + lane * 4);
#pragma unroll
        for (int p = 1; p < 4; ++p) a += *(const f32x4*)(lds + L_RH + (p * 64 + lane) * 4);
        const unsigned off = WS_HP + th * 2048 + lane * 16;
        __builtin_amdgcn_raw_buffer_store_b128(v4u{__float_as_uint(a[0]), T, __float_as_uint(a[1]), T}, rs, off, 0, AUX_SC1);
        __builtin_amdgcn_raw_buffer_store_b128(v4u{__float_as_uint(a[2]), T, __float_as_uint(a[3]), T}, rs, off + 1024, 0, AUX_SC1);
      }
    }
    f32x4 stg = *(const f32x4*)(lds + L_ST + 4 * g);
    KEEP(stg);
    PIN();
    STAMP(2);
    __builtin_amdgcn_s_sleep(DLY1);
    {
      const unsigned qoff = WS_QP + ((hk * 8 + (lane >> 5)) * 4 + pw) * 2048 + ((lane >> 4) & 1) * 1024 + (16 * ab + c) * 16;
      v4u qv[4];
#pragma unroll
      for (int i = 0; i < 4; ++i) qv[i] = __builtin_amdgcn_raw_buffer_load_b128(rs, qoff + i * 16384, 0, AUX_SC1);
#pragma clang loop unroll(disable)
      for (unsigned spins = 0; spins < SPIN_CAP; ++spins) {
        unsigned bad[4];
#pragma unroll
        for (int i = 0; i < 4; ++i) bad[i] = tagbad(qv[i], T);
        if (__all((bad[0] | bad[1] | bad[2] | bad[3]) == 0)) break;
        asm volatile("" ::: "memory");
#pragma unroll
        for (int i = 0; i < 4; ++i) if (bad[i]) qv[i] = __builtin_amdgcn_raw_buffer_load_b128(rs, qoff + i * 16384, 0, AUX_SC1);
      }
      float* qs = lds + L_QS + (pw * 64 + lane) * 8;
      *(v4u*)(qs) = v4u{qv[0][0], qv[0][2], qv[1][0], qv[1][2]};
      *(v4u*)(qs + 4) = v4u{qv[2][0], qv[2][2], qv[3][0], qv[3][2]};
      if (arrive(CN(C_QG), lane) == 3u) {
        f32x4 a0 = *(const f32x4*)(lds + L_QS + lane * 8), a1 = *(const f32x4*)(lds + L_QS + lane * 8 + 4);
#pragma unroll
        for (int p = 1; p < 4; ++p) { a0 += *(const f32x4*)(lds + L_QS + (p * 64 + lane) * 8); a1 += *(const f32x4*)(lds + L_QS + (p * 64 + lane) * 8 + 4); }
        unsigned* qb = (unsigned*)(lds + L_QB) + 32 * (lane >> 5) + 2 * c + ((lane >> 4) & 1);
        qb[0] = pack2(a0[0], a0[1]); qb[64] = pack2(a0[2], a0[3]); qb[128] = pack2(a1[0], a1[1]); qb[192] = pack2(a1[2], a1[3]);
        signal(CN(C_QRDY), lane);
      }
    }
    STAMP(3);
    await<4>(CN(C_ISS), 4u);
    SETPRIO(0);
    const bool has_mlp = kgl < 14;
    const int fs = 14 * hk + kgl, f0 = 16 * fs;
    v2u wov[8], w2v[4];
    {
      const __amdgpu_buffer_rsrc_t rw2 = __builtin_amdgcn_make_buffer_rsrc((void*)w2, 0, has_mlp ? DM * FF * 2 : 0, 0x00020000);
      const u16* p = wo + (size_t)(32 * kg + 8 * g) * DM + 256 * cg + 64 * pw + 4 * c;
      const int o2 = ((f0 + 4 * g) * DM + 256 * cg + 64 * pw + 4 * c) * 2;
#pragma unroll
      for (int j = 0; j < 8; ++j) wov[j] = ldnt<v2u>(p + j * DM);
#pragma unroll
      for (int j = 0; j < 4; ++j) w2v[j] = __builtin_amdgcn_raw_buffer_load_b64(rw2, o2 + j * DM * 2, 0, AUX_NT);
    }
    PIN();
    SETPRIO(3);
    STAMP(4);
    if (pw == 3) {
      float hv = 0.f;
      if (has_mlp) {
        const float bv = lo2f((unsigned)ldg<u16>(b1 + f0 + 4 * (c & 3) + g));
        const unsigned hoff = WS_HP + ((fs >> 2) * 4 + g) * 2048 + (16 * (c >> 2) + 4 * (fs & 3) + (c & 3)) * 16;
        v4u h0 = __builtin_amdgcn_raw_buffer_load_b128(rs, hoff, 0, AUX_SC1);
        v4u h1 = __builtin_amdgcn_raw_buffer_load_b128(rs, hoff + 1024, 0, AUX_SC1);
#pragma clang loop unroll(disable)
        for (unsigned spins = 0; spins < SPIN_CAP; ++spins) {
          const unsigned bad = tagbad(h0, T) | tagbad(h1, T);
          if (__all(bad == 0)) break;
          asm volatile("" ::: "memory");
          if (bad) {
            h0 = __builtin_amdgcn_raw_buffer_load_b128(rs, hoff, 0, AUX_SC1);
            h1 = __builtin_amdgcn_raw_buffer_load_b128(rs, hoff + 1024, 0, AUX_SC1);
          }
        }
        float a[4] = {__uint_as_float(h0[0]), __uint_as_float(h0[2]), __uint_as_float(h1[0]), __uint_as_float(h1[2])};
#pragma unroll
        for (int i = 0; i < 4; ++i) a[i] = x32_sum(x16_sum(a[i]));
        const float av = g == 0 ? a[0] : g == 1 ? a[1] : g == 2 ? a[2] : a[3];
        const float u = bfr(bfr(av) + bv);
        const float inner = 0.7978845608028654f * (u + 0.044715f * u * u * u);
        hv = 0.5f * u * (1.0f + tanhf(inner));
      }
      ((u16*)(lds + L_HB))[(c >> 2) * 16 + 4 * (c & 3) + g] = (u16)pack2(hv, 0.f);
      signal(CN(C_HRDY), lane);
    }
    v4u wof[4];
    float accm[4];
    {
      v2u w2f[4];
#pragma unroll
      for (int i = 0; i < 4; ++i) {
        wof[i] = tr8x4(wov, i);
#pragma unroll
        for (int jj = 0; jj < 2; ++jj)
          w2f[i][jj] = (i & 1) ? perm_hi(w2v[2 * jj][i >> 1], w2v[2 * jj + 1][i >> 1]) : perm_lo(w2v[2 * jj][i >> 1], w2v[2 * jj + 1][i >> 1]);
        KEEP(wof[i]);
      }
      await<1>(CN(C_HRDY), 1u);
      const v2u ha = *(const v2u*)((const u16*)(lds + L_HB) + (c >> 2) * 16 + 4 * g);
#pragma unroll
      for (int i = 0; i < 4; ++i) {
        const f32x4 e = mfma16(ha, w2f[i], f32x4{0.f, 0.f, 0.f, 0.f});
        accm[i] = stg[3] * e[0];
        KEEP(accm[i]);
      }
    }
    PIN();
    STAMP(5);
    const int h2 = 4 * hk + (kgl >> 2);
    const unsigned p2off = WS_P2 + (((h2 * 4 + (kgl & 3)) * 4 + pw) * 8 + (lane >> 3)) * 256 + (lane & 7) * 32;
    const unsigned mloff = WS_ML + ((h2 * 4 + pw) * 8 + (lane >> 3)) * 16;
    await<1>(CN(C_PUB2), 4u);
    __builtin_amdgcn_s_sleep(DLY2);
    STAMP(6);
    v4u a0 = __builtin_amdgcn_raw_buffer_load_b128(rs, p2off, 0, AUX_SC1);
    v4u a1 = __builtin_amdgcn_raw_buffer_load_b128(rs, p2off + 16, 0, AUX_SC1);
    v4u am = __builtin_amdgcn_raw_buffer_load_b128(rs, mloff, 0, AUX_SC1);
#pragma clang loop unroll(disable)
    for (unsigned spins = 0; spins < SPIN_CAP; ++spins) {
      const unsigned bad = tagbad(a0, T) | tagbad(a1, T) | tagbad(am, T);
      if (__all(bad == 0)) break;
      asm volatile("" ::: "memory");
      if (bad) {
        a0 = __builtin_amdgcn_raw_buffer_load_b128(rs, p2off, 0, AUX_SC1);
        a1 = __builtin_amdgcn_raw_buffer_load_b128(rs, p2off + 16, 0, AUX_SC1);
        am = __builtin_amdgcn_raw_buffer_load_b128(rs, mloff, 0, AUX_SC1);
      }
    }
    STAMP(7);
    {
      const float m = __uint_as_float(am[0]), l = __uint_as_float(am[2]);
      float M = __builtin_fmaxf(m, dpp<ROR8>(m)); M = x32_max(x16_max(M));
      const float f = __builtin_amdgcn_exp2f(m - M);
      float v[5] = {f * __uint_as_float(a0[0]), f * __uint_as_float(a0[2]), f * __uint_as_float(a1[0]), f * __uint_as_float(a1[2]), f * l};
#pragma unroll
      for (int i = 0; i < 5; ++i) v[i] += dpp<ROR8>(v[i]);
#pragma unroll
      for (int i = 0; i < 5; ++i) v[i] = x16_sum(v[i]);
#pragma unroll
      for (int i = 0; i < 5; ++i) v[i] = x32_sum(v[i]);
      const float inv = __builtin_amdgcn_rcpf(v[4]);
      if (lane < 8) *(v2u*)((u16*)(lds + L_OB) + pw * 32 + 4 * lane) = v2u{pack2(v[0] * inv, v[1] * inv), pack2(v[2] * inv, v[3] * inv)};
    }
    if (arrive(CN(C_O), lane) != 3u) await<0>(CN(C_O), 4u);
    {
      const v4u oa = *(const v4u*)((const u16*)(lds + L_OB) + (c >> 2) * 32 + 8 * g);
      f32x4 d[4];
#pragma unroll
      for (int i = 0; i < 4; ++i) d[i] = mfma32(oa, wof[i], f32x4{0.f, 0.f, 0.f, 0.f});
#pragma unroll
      for (int i = 0; i < 4; ++i) KEEP(d[i]);
      const unsigned off = WS_P3 + ((kg * 2 + cg) * 4 + pw) * 2048 + lane * 16;
      const v4u s0 = {__float_as_uint(fmaf(stg[2], d[0][0], accm[0])), T, __float_as_uint(fmaf(stg[2], d[1][0], accm[1])), T};
      const v4u s1 = {__float_as_uint(fmaf(stg[2], d[2][0], accm[2])), T, __float_as_uint(fmaf(stg[2], d[3][0], accm[3])), T};
      __builtin_amdgcn_raw_buffer_store_b128(s0, rs, off, 0, AUX_SC1);
      __builtin_amdgcn_raw_buffer_store_b128(s1, rs, off + 1024, 0, AUX_SC1);
    }
    PIN();
    signal(CN(C_PUB3), lane);
    STAMP(8);
  }
  STAMP_DUMP();
}

extern "C" void kernel_launch(void* const* d_in, const int* in_sizes, int n_in,
                              void* d_out, int out_size, void* d_ws, size_t ws_size,
                              hipStream_t stream) {
  (void)in_sizes; (void)n_in; (void)out_size; (void)ws_size;
  fused_kernel<<<256, 512, 0, stream>>>(
      (const u16*)d_in[0], (const u16*)d_in[1], (const u16*)d_in[2], (const u16*)d_in[3], (const u16*)d_in[4],
      (const u16*)d_in[6], (const u16*)d_in[7], (const u16*)d_in[8], (const u16*)d_in[9], (const u16*)d_in[10],
      (u16*)d_out, (unsigned char*)d_ws);
}
```

```cpp
#include <hip/hip_runtime.h>
#include <hip/hip_bf16.h>
#include <stdint.h>

#define GAS __attribute__((address_space(1)))
#define LAS __attribute__((address_space(3)))
typedef unsigned short u16;
typedef unsigned v4u __attribute__((ext_vector_type(4)));
typedef unsigned v2u __attribute__((ext_vector_type(2)));
typedef float f32x4 __attribute__((ext_vector_type(4)));
typedef float f32x2 __attribute__((ext_vector_type(2)));
typedef __bf16 bf16x8 __attribute__((ext_vector_type(8)));
typedef __bf16 bf16x2 __attribute__((ext_vector_type(2)));
typedef short s16x4 __attribute__((ext_vector_type(4)));
typedef GAS unsigned gu32;

constexpr int DM = 512, SK = 4096, DH = 128, WINDOW = 1024, FF = 1792, NQ = 4096;
constexpr unsigned WS_QP = 4096;
constexpr unsigned WS_HP = WS_QP + (512u << 10);
constexpr unsigned WS_P2 = WS_HP + (256u << 10);
constexpr unsigned WS_ML = WS_P2 + (1u << 20);
constexpr unsigned WS_P3 = WS_ML + 16384;
constexpr unsigned WS_END = WS_P3 + (2u << 20);
constexpr unsigned SPIN_CAP = 1u << 17;
constexpr unsigned LSPIN_CAP = 1u << 22;
constexpr int AUX_SC1 = 16, AUX_NT = 2;
constexpr float LOG2E = 1.4426950408889634f;
constexpr float SCL2 = 0.08838834764831845f * 1.4426950408889634f;

#define AWIN 6
#define DLY1 16
#define DLY2 8
#define DLY3 10
#define SETPRIO(n) __builtin_amdgcn_s_setprio(n)

__device__ __forceinline__ unsigned pack2(float lo, float hi) {
  return __builtin_bit_cast(unsigned, __builtin_convertvector(f32x2{lo, hi}, bf16x2));
}
__device__ __forceinline__ float bfr(float f) { return (float)(__bf16)f; }
__device__ __forceinline__ float lo2f(unsigned u) { return __builtin_bit_cast(float, u << 16); }
__device__ __forceinline__ float hi2f(unsigned u) { return __builtin_bit_cast(float, u & 0xffff0000u); }
__device__ __forceinline__ unsigned perm_lo(unsigned a, unsigned b) { return __builtin_amdgcn_perm(b, a, 0x05040100u); }
__device__ __forceinline__ unsigned perm_hi(unsigned a, unsigned b) { return __builtin_amdgcn_perm(b, a, 0x07060302u); }

template <int CTRL> __device__ __forceinline__ float dpp(float x) {
  return __builtin_bit_cast(float, __builtin_amdgcn_mov_dpp(__builtin_bit_cast(int, x), CTRL, 0xf, 0xf, true));
}
constexpr int XOR1 = 0xB1, XOR2 = 0x4E, ROR8 = 0x128, ROR4 = 0x124;
__device__ __forceinline__ float x16_max(float x) {
  auto s = __builtin_amdgcn_permlane16_swap(__float_as_uint(x), __float_as_uint(x), false, false);
  return __builtin_fmaxf(__uint_as_float(s[0]), __uint_as_float(s[1]));
}
__device__ __forceinline__ float x32_max(float x) {
  auto s = __builtin_amdgcn_permlane32_swap(__float_as_uint(x), __float_as_uint(x), false, false);
  return __builtin_fmaxf(__uint_as_float(s[0]), __uint_as_float(s[1]));
}
__device__ __forceinline__ float x16_sum(float x) {
  auto s = __builtin_amdgcn_permlane16_swap(__float_as_uint(x), __float_as_uint(x), false, false);
  return __uint_as_float(s[0]) + __uint_as_float(s[1]);
}
__device__ __forceinline__ float x32_sum(float x) {
  auto s = __builtin_amdgcn_permlane32_swap(__float_as_uint(x), __float_as_uint(x), false, false);
  return __uint_as_float(s[0]) + __uint_as_float(s[1]);
}
__device__ __forceinline__ float wave_sum(float x) {
  x += dpp<XOR1>(x); x += dpp<XOR2>(x); x += dpp<ROR4>(x); x += dpp<ROR8>(x);
  return x32_sum(x16_sum(x));
}
__device__ __forceinline__ f32x4 mfma32(v4u a, v4u b, f32x4 c) {
  return __builtin_amdgcn_mfma_f32_16x16x32_bf16(__builtin_bit_cast(bf16x8, a), __builtin_bit_cast(bf16x8, b), c, 0, 0, 0);
}
__device__ __forceinline__ f32x4 mfma16(v2u a, v2u b, f32x4 c) {
  return __builtin_amdgcn_mfma_f32_16x16x16bf16_1k(__builtin_bit_cast(s16x4, a), __builtin_bit_cast(s16x4, b), c, 0, 0, 0);
}
#define PIN() __builtin_amdgcn_sched_barrier(0)
#define BAR() do { PIN(); asm volatile("s_waitcnt lgkmcnt(0)" ::: "memory"); __builtin_amdgcn_s_barrier(); asm volatile("" ::: "memory"); PIN(); } while (0)
#define KEEP(x) asm volatile("" : "+v"(x))
#define STAMP(i) do { PIN(); if ((w == 0 || w == 4) && lane == 0) __hip_atomic_store((GAS unsigned long long*)(ws + (8u << 20)) + (((w >> 2) * 16 + (i)) * 256 + bid), __builtin_amdgcn_s_memrealtime(), __ATOMIC_RELAXED, __HIP_MEMORY_SCOPE_AGENT); PIN(); } while (0)
#define STAMP_INIT()
#define STAMP_DUMP()

template <typename T> __device__ __forceinline__ T ldg(const u16* p) { return *(const GAS T*)p; }
template <typename T> __device__ __forceinline__ T ldnt(const u16* p) { return __builtin_nontemporal_load((const GAS T*)p); }

constexpr int L_CNT = 0;
constexpr int L_ST = 16;
constexpr int L_RQ = 32;
constexpr int L_RH = L_RQ + 1024;
constexpr int L_QS = L_RH + 1024;
constexpr int L_HB = L_QS + 2048;
constexpr int L_MO = L_HB + 32;
constexpr int L_ML = L_MO + 2048;
constexpr int L_OB = L_ML + 32;
constexpr int L_QB = L_OB + 64;
constexpr int L_FR = L_QB + 256;
constexpr int L_KS = L_FR + 32;
constexpr int L_END = L_KS + 8192;
enum { C_Q = 0, C_QG, C_QRDY, C_ISS, C_ATT, C_PUB2, C_H, C_HRDY, C_O, C_PUB3, C_FIN, C_ST, C_QPUB };

__device__ __forceinline__ unsigned arrive(unsigned addr, int lane) {
  unsigned old = 0;
  if (lane == 0)
    asm volatile("s_waitcnt lgkmcnt(0)\n\tds_add_rtn_u32 %0, %1, %2\n\ts_waitcnt lgkmcnt(0)" : "=v"(old) : "v"(addr), "v"(1u) : "memory");
  return __builtin_amdgcn_readfirstlane(old);
}
__device__ __forceinline__ void signal(unsigned addr, int lane) {
  if (lane == 0) asm volatile("s_waitcnt lgkmcnt(0)\n\tds_add_u32 %0, %1" :: "v"(addr), "v"(1u) : "memory");
}
template <int SLEEP> __device__ __forceinline__ void await(unsigned addr, unsigned n) {
#pragma clang loop unroll(disable)
  for (unsigned s = 0; s < LSPIN_CAP; ++s) {
    unsigned v;
    asm volatile("ds_read_b32 %0, %1\n\ts_waitcnt lgkmcnt(0)" : "=v"(v) : "v"(addr) : "memory");
    if ((unsigned)__builtin_amdgcn_readfirstlane(v) >= n) break;
    if (SLEEP) __builtin_amdgcn_s_sleep(SLEEP);
  }
}

__device__ __forceinline__ v4u tr8x4(const v2u (&W)[8], int i) {
  v4u f;
#pragma unroll
  for (int jj = 0; jj < 4; ++jj)
    f[jj] = (i & 1) ? perm_hi(W[2 * jj][i >> 1], W[2 * jj + 1][i >> 1]) : perm_lo(W[2 * jj][i >> 1], W[2 * jj + 1][i >> 1]);
  return f;
}
__device__ __forceinline__ unsigned tagbad(v4u v, unsigned T) { return (v[1] ^ T) | (v[3] ^ T); }

__global__ void __launch_bounds__(512, 2)
fused_kernel(const u16* __restrict__ x, const u16* __restrict__ kcache, const u16* __restrict__ vcache,
             const u16* __restrict__ wq, const u16* __restrict__ wo, const u16* __restrict__ w1,
             const u16* __restrict__ b1, const u16* __restrict__ w2, const u16* __restrict__ b2,
             const u16* __restrict__ gw, u16* __restrict__ out, unsigned char* ws) {
  __shared__ __attribute__((aligned(16))) float lds[L_END];
  const int tid = threadIdx.x, lane = tid & 63, w = __builtin_amdgcn_readfirstlane(tid >> 6);
  const int g = lane >> 4, c = lane & 15;
  const int bid = blockIdx.x, hk = bid & 7, idx = bid >> 3;
  const int ab = idx >> 3, as = idx & 7;
  const int kgl = idx >> 1, cg = idx & 1, kg = hk * 16 + kgl;
  const __amdgpu_buffer_rsrc_t rs = __builtin_amdgcn_make_buffer_rsrc((void*)ws, 0, (int)WS_END, 0x00020000);
  const unsigned cn = (unsigned)(size_t)(LAS float*)(lds + L_CNT);
#define CN(i) (cn + 4u * (i))
  STAMP_INIT();
  STAMP(0);
  const unsigned T = *(const GAS unsigned*)ws + 1u;

  if (w >= 4) {
    const int aw = w - 4;
    BAR();
    unsigned tt;
    {
      const unsigned char* tp = ws;
      tp = lane == 0 ? (const unsigned char*)(out + (bid >> 6) * DM) : tp;
      tp = lane == 1 ? ws + WS_P3 : tp;
      tp = lane == 2 ? ws + WS_END - 64 : tp;
      tp = lane == 3 ? (const unsigned char*)b2 : tp;
      tp = lane == 4 ? (const unsigned char*)(wo + (size_t)(32 * kg) * DM) : tp;
      tp = lane == 5 ? (const unsigned char*)(w2 + (size_t)(224 * hk) * DM) : tp;
      tp = lane == 6 ? (const unsigned char*)b1 : tp;
      tp = lane == 7 ? ws + WS_P2 : tp;
      tt = *(const GAS unsigned*)tp;
    }
    PIN();
    v4u kr[8], vf[8];
    {
      const size_t kvoff = ((size_t)(ab * 8 + hk) * SK + (SK - WINDOW) + 128 * as + 32 * aw) * DH;
      const u16* kp = kcache + kvoff + 8 * lane;
      const u16* vp = vcache + kvoff + 4 * g * DH + 8 * c;
#pragma unroll
      for (int i = 0; i < 16; ++i) {
        if (i >= AWIN) {
          PIN();
          asm volatile("s_waitcnt vmcnt(%0)" :: "n"(AWIN - 1) : "memory");
          PIN();
        }
        if (i < 8) kr[i] = ldnt<v4u>(kp + i * 512);
        else { const int j = i - 8; vf[j] = ldnt<v4u>(vp + (16 * (j >> 2) + (j & 3)) * DH); }
      }
    }
    PIN();
    SETPRIO(2);
    asm volatile("" :: "v"(tt));
    signal(CN(C_ISS), lane);
    STAMP(1);
    v4u kf[8];
    {
      unsigned char* ks_ = (unsigned char*)(lds + L_KS) + aw * 8192;
#pragma unroll
      for (int i = 0; i < 8; ++i) { const int row = 4 * i + g; *(v4u*)(ks_ + row * 256 + ((c ^ (row & 15)) << 4)) = kr[i]; }
      asm volatile("s_waitcnt lgkmcnt(0)" ::: "memory");
#pragma unroll
      for (int t = 0; t < 2; ++t)
#pragma unroll
        for (int ks = 0; ks < 4; ++ks) kf[t * 4 + ks] = *(const v4u*)(ks_ + (16 * t + c) * 256 + (((4 * ks + g) ^ c) << 4));
#pragma unroll
      for (int i = 0; i < 8; ++i) KEEP(kf[i]);
    }
    PIN();
    STAMP(2);
    await<0>(CN(C_QRDY), 1u);
    STAMP(3);
    v4u pf;
    {
      f32x4 s0 = {0.f, 0.f, 0.f, 0.f}, s1 = {0.f, 0.f, 0.f, 0.f};
      const u16* qb = (const u16*)(lds + L_QB) + (c >> 2) * 128 + 8 * g;
      v4u qf[4];
#pragma unroll
      for (int ks = 0; ks < 4; ++ks) qf[ks] = *(const v4u*)(qb + 32 * ks);
#pragma unroll
      for (int ks = 0; ks < 4; ++ks) {
        s0 = mfma32(kf[ks], qf[ks], s0);
        s1 = mfma32(kf[4 + ks], qf[ks], s1);
      }
      float m = __builtin_fmaxf(__builtin_fmaxf(__builtin_fmaxf(s0[0], s0[1]), __builtin_fmaxf(s0[2], s0[3])),
                                __builtin_fmaxf(__builtin_fmaxf(s1[0], s1[1]), __builtin_fmaxf(s1[2], s1[3])));
      m = x32_max(x16_max(m));
      float p[8];
#pragma unroll
      for (int j = 0; j < 4; ++j) { p[j] = __builtin_amdgcn_exp2f((s0[j] - m) * SCL2); p[4 + j] = __builtin_amdgcn_exp2f((s1[j] - m) * SCL2); }
      pf = v4u{pack2(p[0], p[1]), pack2(p[2], p[3]), pack2(p[4], p[5]), pack2(p[6], p[7])};
      float l = ((p[0] + p[1]) + (p[2] + p[3])) + ((p[4] + p[5]) + (p[6] + p[7]));
      l = x32_sum(x16_sum(l));
      if (g == 0 && (c & 3) == 0) *(f32x2*)(lds + L_ML + (aw * 4 + (c >> 2)) * 2) = f32x2{m * SCL2, l};
      KEEP(pf);
    }
    PIN();
    STAMP(4);
    {
      v4u vt[8];
#pragma unroll
      for (int i = 0; i < 8; ++i)
#pragma unroll
        for (int jj = 0; jj < 4; ++jj)
          vt[i][jj] = (i & 1) ? perm_hi(vf[2 * jj][i >> 1], vf[2 * jj + 1][i >> 1]) : perm_lo(vf[2 * jj][i >> 1], vf[2 * jj + 1][i >> 1]);
      f32x4 o[8];
#pragma unroll
      for (int i = 0; i < 8; ++i) o[i] = mfma32(pf, vt[i], f32x4{0.f, 0.f, 0.f, 0.f});
#pragma unroll
      for (int i = 0; i < 8; ++i) KEEP(o[i]);
      float* mo = lds + L_MO + (aw * 4 + g) * 128 + 8 * c;
      *(f32x4*)(mo) = f32x4{o[0][0], o[1][0], o[2][0], o[3][0]};
      *(f32x4*)(mo + 4) = f32x4{o[4][0], o[5][0], o[6][0], o[7][0]};
    }
    if (arrive(CN(C_ATT), lane) != 3u) await<0>(CN(C_ATT), 4u);
    STAMP(5);
    {
      f32x2 ml[4], ov[4];
#pragma unroll
      for (int u = 0; u < 4; ++u) { ml[u] = *(const f32x2*)(lds + L_ML + (u * 4 + aw) * 2); ov[u] = *(const f32x2*)(lds + L_MO + (u * 4 + aw) * 128 + 2 * lane); }
      const float M = __builtin_fmaxf(__builtin_fmaxf(ml[0][0], ml[1][0]), __builtin_fmaxf(ml[2][0], ml[3][0]));
      float L = 0.f, o0 = 0.f, o1 = 0.f;
#pragma unroll
      for (int u = 0; u < 4; ++u) {
        const float f = __builtin_amdgcn_exp2f(ml[u][0] - M);
        L = fmaf(f, ml[u][1], L); o0 = fmaf(f, ov[u][0], o0); o1 = fmaf(f, ov[u][1], o1);
      }
      const int hg = 4 * hk + aw;
      const v4u pv = {__float_as_uint(o0), T, __float_as_uint(o1), T};
      __builtin_amdgcn_raw_buffer_store_b128(pv, rs, WS_P2 + (((hg * 4 + (lane >> 4)) * 4 + ab) * 8 + as) * 256 + (lane & 15) * 16, 0, AUX_SC1);
      if (lane == 0) {
        const v4u mv = {__float_as_uint(M), T, __float_as_uint(L), T};
        __builtin_amdgcn_raw_buffer_store_b128(mv, rs, WS_ML + ((hg * 4 + ab) * 8 + as) * 16, 0, AUX_SC1);
      }
    }
    PIN();
    signal(CN(C_PUB2), lane);
    STAMP(6);
    const int ob = bid >> 6, ocw = (bid >> 3) & 7, om = (bid >> 1) & 3, oh = bid & 1;
    const int col = 64 * ocw + 4 * (4 * om + (lane & 3)) + 2 * oh;
    const unsigned b2v = ldg<unsigned>(b2 + col);
    const unsigned off = WS_P3 + ((aw * 32 + (lane >> 2)) * 8 + ocw) * 2048 + oh * 1024 + (ob * 16 + 4 * om + (lane & 3)) * 16;
    await<2>(CN(C_PUB3), 4u);
    __builtin_amdgcn_s_sleep(DLY3);
    STAMP(8);
    v4u pv0 = __builtin_amdgcn_raw_buffer_load_b128(rs, off, 0, AUX_SC1);
    v4u pv1 = __builtin_amdgcn_raw_buffer_load_b128(rs, off + 16 * 8 * 2048, 0, AUX_SC1);
#pragma clang loop unroll(disable)
    for (unsigned spins = 0; spins < SPIN_CAP; ++spins) {
      const unsigned bad0 = tagbad(pv0, T), bad1 = tagbad(pv1, T);
      if (__all((bad0 | bad1) == 0)) break;
      asm volatile("" ::: "memory");
      if (bad0) pv0 = __builtin_amdgcn_raw_buffer_load_b128(rs, off, 0, AUX_SC1);
      if (bad1) pv1 = __builtin_amdgcn_raw_buffer_load_b128(rs, off + 16 * 8 * 2048, 0, AUX_SC1);
    }
    STAMP(9);
    float sa = __uint_as_float(pv0[0]) + __uint_as_float(pv1[0]), sb = __uint_as_float(pv0[2]) + __uint_as_float(pv1[2]);
    sa += dpp<ROR4>(sa); sb += dpp<ROR4>(sb); sa += dpp<ROR8>(sa); sb += dpp<ROR8>(sb);
    sa = x16_sum(sa); sb = x16_sum(sb); sa = x32_sum(sa); sb = x32_sum(sb);
    if (lane < 4) *(f32x2*)(lds + L_FR + aw * 8 + 2 * lane) = f32x2{sa, sb};
    if (arrive(CN(C_FIN), lane) == 3u) {
      if (lane < 4) {
        const float omg = lds[L_ST + 4 * ob + 3];
        f32x2 t = *(const f32x2*)(lds + L_FR + 2 * lane);
#pragma unroll
        for (int u = 1; u < 4; ++u) t += *(const f32x2*)(lds + L_FR + u * 8 + 2 * lane);
        *(GAS unsigned*)(out + ob * DM + col) = pack2(fmaf(omg, lo2f(b2v), t[0]), fmaf(omg, hi2f(b2v), t[1]));
      }
      if (bid == 0) {
        GAS unsigned long long* sp = (GAS unsigned long long*)(ws + (8u << 20));
        unsigned long long mn = ~0ull, mx = ~0ull;
        for (int k = 0; k < 4; ++k) {
          const unsigned long long a = __hip_atomic_load(sp + lane * 4 + k, __ATOMIC_RELAXED, __HIP_MEMORY_SCOPE_AGENT);
          const unsigned long long b = __hip_atomic_load(sp + (0 * 256) + lane * 4 + k, __ATOMIC_RELAXED, __HIP_MEMORY_SCOPE_AGENT);
          mn = a < mn ? a : mn; mx = b < mx ? b : mx;
        }
        for (int o = 32; o; o >>= 1) { unsigned long long a = __shfl_xor(mn, o), b = __shfl_xor(mx, o); mn = a < mn ? a : mn; mx = b < mx ? b : mx; }
        unsigned long long d_ = mx - mn; if (d_ > 3000) d_ = 3000;
        const unsigned long long n_ = __builtin_amdgcn_s_memrealtime(); while (__builtin_amdgcn_s_memrealtime() - n_ < d_) __builtin_amdgcn_s_sleep(1);
      }
      if (bid == 0 && lane == 0) __hip_atomic_store((gu32*)ws, T, __ATOMIC_RELAXED, __HIP_MEMORY_SCOPE_AGENT);
    }
  } else {
    const int pw = w;
    SETPRIO(3);
    const int kq = idx & 3, cq = idx >> 2;
    const bool has_ht = idx < 14;
    const int th = 14 * hk + idx;
    const int qrow = 128 * kq + 32 * pw + 8 * g, hrow = 128 * (th & 3) + 32 * pw + 8 * g;
    v2u wv[8], w1v[8];
    {
      const u16* p = wq + (size_t)qrow * NQ + hk * 512 + 64 * cq + 4 * c;
#pragma unroll
      for (int j = 0; j < 8; ++j) wv[j] = ldnt<v2u>(p + j * NQ);
    }
    const v4u xa = ldg<v4u>(x + (c >> 2) * DM + qrow);
    const v4u xs = ldg<v4u>(x + pw * DM + 8 * lane);
    const v4u gwv = ldg<v4u>(gw + 8 * lane);
    const v4u xh = ldg<v4u>(x + (c >> 2) * DM + hrow);
    {
      const __amdgpu_buffer_rsrc_t rw1 = __builtin_amdgcn_make_buffer_rsrc((void*)w1, 0, has_ht ? DM * FF * 2 : 0, 0x00020000);
      const int o = (hrow * FF + 64 * (th >> 2) + 4 * c) * 2;
#pragma unroll
      for (int j = 0; j < 8; ++j) w1v[j] = __builtin_amdgcn_raw_buffer_load_b64(rw1, o + j * FF * 2, 0, AUX_NT);
    }
    if (tid < 16) ((LAS unsigned*)(lds + L_CNT))[tid] = 0u;
    BAR();
    {
      f32x4 d[4];
#pragma unroll
      for (int i = 0; i < 4; ++i) d[i] = mfma32(xa, tr8x4(wv, i), f32x4{0.f, 0.f, 0.f, 0.f});
      *(f32x4*)(lds + L_RQ + (pw * 64 + lane) * 4) = f32x4{d[0][0], d[1][0], d[2][0], d[3][0]};
      if (arrive(CN(C_Q), lane) == 3u) {
        f32x4 a = *(const f32x4*)(lds + L_RQ + lane * 4);
#pragma unroll
        for (int p = 1; p < 4; ++p) a += *(const f32x4*)(lds + L_RQ + (p * 64 + lane) * 4);
        const unsigned off = WS_QP + ((hk * 8 + cq) * 4 + kq) * 2048 + lane * 16;
        __builtin_amdgcn_raw_buffer_store_b128(v4u{__float_as_uint(a[0]), T, __float_as_uint(a[1]), T}, rs, off, 0, AUX_SC1);
        __builtin_amdgcn_raw_buffer_store_b128(v4u{__float_as_uint(a[2]), T, __float_as_uint(a[3]), T}, rs, off + 1024, 0, AUX_SC1);
      }
    }
    PIN();
    STAMP(1);
    {
      float xv[8], gv[8];
#pragma unroll
      for (int k = 0; k < 4; ++k) { xv[2 * k] = lo2f(xs[k]); xv[2 * k + 1] = hi2f(xs[k]); gv[2 * k] = lo2f(gwv[k]); gv[2 * k + 1] = hi2f(gwv[k]); }
      float s = 0.f, gs = 0.f;
#pragma unroll
      for (int k = 0; k < 8; ++k) { s += xv[k]; gs += bfr(xv[k] * gv[k]); }
      s = wave_sum(s); gs = wave_sum(gs);
      const float mean = s * (1.0f / DM);
      float vs = 0.f;
#pragma unroll
      for (int k = 0; k < 8; ++k) { const float d = xv[k] - mean; vs = fmaf(d, d, vs); }
      vs = wave_sum(vs);
      if (lane == 0) {
        const float var_b = bfr(vs * (1.0f / DM));
        const float den = bfr(sqrtf(bfr(var_b + 1e-5f)));
        const float gt = bfr(1.0f / (1.0f + __expf(-bfr(gs))));
        *(f32x4*)(lds + L_ST + 4 * pw) = f32x4{bfr(mean), den, gt, bfr(1.0f - gt)};
      }
      if (arrive(CN(C_ST), lane) != 3u) await<0>(CN(C_ST), 4u);
    }
    if (has_ht) {
      const f32x4 st = *(const f32x4*)(lds + L_ST + 4 * (c >> 2));
      v4u ya;
#pragma unroll
      for (int k = 0; k < 4; ++k) ya[k] = pack2(bfr(lo2f(xh[k]) - st[0]) / st[1], bfr(hi2f(xh[k]) - st[0]) / st[1]);
      f32x4 d[4];
#pragma unroll
      for (int i = 0; i < 4; ++i) d[i] = mfma32(ya, tr8x4(w1v, i), f32x4{0.f, 0.f, 0.f, 0.f});
      *(f32x4*)(lds + L_RH + (pw * 64 + lane) * 4) = f32x4{d[0][0], d[1][0], d[2][0], d[3][0]};
      if (arrive(CN(C_H), lane) == 3u) {
        f32x4 a = *(const f32x4*)(lds + L_RH + lane * 4);
#pragma unroll
        for (int p = 1; p < 4; ++p) a += *(const f32x4*)(lds + L_RH + (p * 64 + lane) * 4);
        const unsigned off = WS_HP + th * 2048 + lane * 16;
        __builtin_amdgcn_raw_buffer_store_b128(v4u{__float_as_uint(a[0]), T, __float_as_uint(a[1]), T}, rs, off, 0, AUX_SC1);
        __builtin_amdgcn_raw_buffer_store_b128(v4u{__float_as_uint(a[2]), T, __float_as_uint(a[3]), T}, rs, off + 1024, 0, AUX_SC1);
      }
    }
    f32x4 stg = *(const f32x4*)(lds + L_ST + 4 * g);
    KEEP(stg);
    PIN();
    STAMP(2);
    __builtin_amdgcn_s_sleep(DLY1);
    {
      const unsigned qoff = WS_QP + ((hk * 8 + (lane >> 5)) * 4 + pw) * 2048 + ((lane >> 4) & 1) * 1024 + (16 * ab + c) * 16;
      v4u qv[4];
#pragma unroll
      for (int i = 0; i < 4; ++i) qv[i] = __builtin_amdgcn_raw_buffer_load_b128(rs, qoff + i * 16384, 0, AUX_SC1);
#pragma clang loop unroll(disable)
      for (unsigned spins = 0; spins < SPIN_CAP; ++spins) {
        unsigned bad[4];
#pragma unroll
        for (int i = 0; i < 4; ++i) bad[i] = tagbad(qv[i], T);
        if (__all((bad[0] | bad[1] | bad[2] | bad[3]) == 0)) break;
        asm volatile("" ::: "memory");
#pragma unroll
        for (int i = 0; i < 4; ++i) if (bad[i]) qv[i] = __builtin_amdgcn_raw_buffer_load_b128(rs, qoff + i * 16384, 0, AUX_SC1);
      }
      float* qs = lds + L_QS + (pw * 64 + lane) * 8;
      *(v4u*)(qs) = v4u{qv[0][0], qv[0][2], qv[1][0], qv[1][2]};
      *(v4u*)(qs + 4) = v4u{qv[2][0], qv[2][2], qv[3][0], qv[3][2]};
      if (arrive(CN(C_QG), lane) == 3u) {
        f32x4 a0 = *(const f32x4*)(lds + L_QS + lane * 8), a1 = *(const f32x4*)(lds + L_QS + lane * 8 + 4);
#pragma unroll
        for (int p = 1; p < 4; ++p) { a0 += *(const f32x4*)(lds + L_QS + (p * 64 + lane) * 8); a1 += *(const f32x4*)(lds + L_QS + (p * 64 + lane) * 8 + 4); }
        unsigned* qb = (unsigned*)(lds + L_QB) + 32 * (lane >> 5) + 2 * c + ((lane >> 4) & 1);
        qb[0] = pack2(a0[0], a0[1]); qb[64] = pack2(a0[2], a0[3]); qb[128] = pack2(a1[0], a1[1]); qb[192] = pack2(a1[2], a1[3]);
        signal(CN(C_QRDY), lane);
      }
    }
    STAMP(3);
    await<4>(CN(C_ISS), 4u);
    SETPRIO(0);
    const bool has_mlp = kgl < 14;
    const int fs = 14 * hk + kgl, f0 = 16 * fs;
    v2u wov[8], w2v[4];
    {
      const __amdgpu_buffer_rsrc_t rw2 = __builtin_amdgcn_make_buffer_rsrc((void*)w2, 0, has_mlp ? DM * FF * 2 : 0, 0x00020000);
      const u16* p = wo + (size_t)(32 * kg + 8 * g) * DM + 256 * cg + 64 * pw + 4 * c;
      const int o2 = ((f0 + 4 * g) * DM + 256 * cg + 64 * pw + 4 * c) * 2;
#pragma unroll
      for (int j = 0; j < 8; ++j) wov[j] = ldnt<v2u>(p + j * DM);
#pragma unroll
      for (int j = 0; j < 4; ++j) w2v[j] = __builtin_amdgcn_raw_buffer_load_b64(rw2, o2 + j * DM * 2, 0, AUX_NT);
    }
    PIN();
    SETPRIO(3);
    STAMP(4);
    if (pw == 3) {
      float hv = 0.f;
      if (has_mlp) {
        const float bv = lo2f((unsigned)ldg<u16>(b1 + f0 + 4 * (c & 3) + g));
        const unsigned hoff = WS_HP + ((fs >> 2) * 4 + g) * 2048 + (16 * (c >> 2) + 4 * (fs & 3) + (c & 3)) * 16;
        v4u h0 = __builtin_amdgcn_raw_buffer_load_b128(rs, hoff, 0, AUX_SC1);
        v4u h1 = __builtin_amdgcn_raw_buffer_load_b128(rs, hoff + 1024, 0, AUX_SC1);
#pragma clang loop unroll(disable)
        for (unsigned spins = 0; spins < SPIN_CAP; ++spins) {
          const unsigned bad = tagbad(h0, T) | tagbad(h1, T);
          if (__all(bad == 0)) break;
          asm volatile("" ::: "memory");
          if (bad) {
            h0 = __builtin_amdgcn_raw_buffer_load_b128(rs, hoff, 0, AUX_SC1);
            h1 = __builtin_amdgcn_raw_buffer_load_b128(rs, hoff + 1024, 0, AUX_SC1);
          }
        }
        float a[4] = {__uint_as_float(h0[0]), __uint_as_float(h0[2]), __uint_as_float(h1[0]), __uint_as_float(h1[2])};
#pragma unroll
        for (int i = 0; i < 4; ++i) a[i] = x32_sum(x16_sum(a[i]));
        const float av = g == 0 ? a[0] : g == 1 ? a[1] : g == 2 ? a[2] : a[3];
        const float u = bfr(bfr(av) + bv);
        const float inner = 0.7978845608028654f * (u + 0.044715f * u * u * u);
        hv = 0.5f * u * (1.0f + tanhf(inner));
      }
      ((u16*)(lds + L_HB))[(c >> 2) * 16 + 4 * (c & 3) + g] = (u16)pack2(hv, 0.f);
      signal(CN(C_HRDY), lane);
    }
    v4u wof[4];
    float accm[4];
    {
      v2u w2f[4];
#pragma unroll
      for (int i = 0; i < 4; ++i) {
        wof[i] = tr8x4(wov, i);
#pragma unroll
        for (int jj = 0; jj < 2; ++jj)
          w2f[i][jj] = (i & 1) ? perm_hi(w2v[2 * jj][i >> 1], w2v[2 * jj + 1][i >> 1]) : perm_lo(w2v[2 * jj][i >> 1], w2v[2 * jj + 1][i >> 1]);
        KEEP(wof[i]);
      }
      await<1>(CN(C_HRDY), 1u);
      const v2u ha = *(const v2u*)((const u16*)(lds + L_HB) + (c >> 2) * 16 + 4 * g);
#pragma unroll
      for (int i = 0; i < 4; ++i) {
        const f32x4 e = mfma16(ha, w2f[i], f32x4{0.f, 0.f, 0.f, 0.f});
        accm[i] = stg[3] * e[0];
        KEEP(accm[i]);
      }
    }
    PIN();
    STAMP(5);
    const int h2 = 4 * hk + (kgl >> 2);
    const unsigned p2off = WS_P2 + (((h2 * 4 + (kgl & 3)) * 4 + pw) * 8 + (lane >> 3)) * 256 + (lane & 7) * 32;
    const unsigned mloff = WS_ML + ((h2 * 4 + pw) * 8 + (lane >> 3)) * 16;
    await<1>(CN(C_PUB2), 4u);
    __builtin_amdgcn_s_sleep(DLY2);
    STAMP(6);
    v4u a0 = __builtin_amdgcn_raw_buffer_load_b128(rs, p2off, 0, AUX_SC1);
    v4u a1 = __builtin_amdgcn_raw_buffer_load_b128(rs, p2off + 16, 0, AUX_SC1);
    v4u am = __builtin_amdgcn_raw_buffer_load_b128(rs, mloff, 0, AUX_SC1);
#pragma clang loop unroll(disable)
    for (unsigned spins = 0; spins < SPIN_CAP; ++spins) {
      const unsigned bad = tagbad(a0, T) | tagbad(a1, T) | tagbad(am, T);
      if (__all(bad == 0)) break;
      asm volatile("" ::: "memory");
      if (bad) {
        a0 = __builtin_amdgcn_raw_buffer_load_b128(rs, p2off, 0, AUX_SC1);
        a1 = __builtin_amdgcn_raw_buffer_load_b128(rs, p2off + 16, 0, AUX_SC1);
        am = __builtin_amdgcn_raw_buffer_load_b128(rs, mloff, 0, AUX_SC1);
      }
    }
    STAMP(7);
    {
      const float m = __uint_as_float(am[0]), l = __uint_as_float(am[2]);
      float M = __builtin_fmaxf(m, dpp<ROR8>(m)); M = x32_max(x16_max(M));
      const float f = __builtin_amdgcn_exp2f(m - M);
      float v[5] = {f * __uint_as_float(a0[0]), f * __uint_as_float(a0[2]), f * __uint_as_float(a1[0]), f * __uint_as_float(a1[2]), f * l};
#pragma unroll
      for (int i = 0; i < 5; ++i) v[i] += dpp<ROR8>(v[i]);
#pragma unroll
      for (int i = 0; i < 5; ++i) v[i] = x16_sum(v[i]);
#pragma unroll
      for (int i = 0; i < 5; ++i) v[i] = x32_sum(v[i]);
      const float inv = __builtin_amdgcn_rcpf(v[4]);
      if (lane < 8) *(v2u*)((u16*)(lds + L_OB) + pw * 32 + 4 * lane) = v2u{pack2(v[0] * inv, v[1] * inv), pack2(v[2] * inv, v[3] * inv)};
    }
    if (arrive(CN(C_O), lane) != 3u) await<0>(CN(C_O), 4u);
    {
      const v4u oa = *(const v4u*)((const u16*)(lds + L_OB) + (c >> 2) * 32 + 8 * g);
      f32x4 d[4];
#pragma unroll
      for (int i = 0; i < 4; ++i) d[i] = mfma32(oa, wof[i], f32x4{0.f, 0.f, 0.f, 0.f});
#pragma unroll
      for (int i = 0; i < 4; ++i) KEEP(d[i]);
      const unsigned off = WS_P3 + ((kg * 2 + cg) * 4 + pw) * 2048 + lane * 16;
      const v4u s0 = {__float_as_uint(fmaf(stg[2], d[0][0], accm[0])), T, __float_as_uint(fmaf(stg[2], d[1][0], accm[1])), T};
      const v4u s1 = {__float_as_uint(fmaf(stg[2], d[2][0], accm[2])), T, __float_as_uint(fmaf(stg[2], d[3][0], accm[3])), T};
      __builtin_amdgcn_raw_buffer_store_b128(s0, rs, off, 0, AUX_SC1);
      __builtin_amdgcn_raw_buffer_store_b128(s1, rs, off + 1024, 0, AUX_SC1);
    }
    PIN();
    signal(CN(C_PUB3), lane);
    STAMP(8);
  }
  STAMP_DUMP();
}

extern "C" void kernel_launch(void* const* d_in, const int* in_sizes, int n_in,
                              void* d_out, int out_size, void* d_ws, size_t ws_size,
                              hipStream_t stream) {
  (void)in_sizes; (void)n_in; (void)out_size; (void)ws_size;
  fused_kernel<<<256, 512, 0, stream>>>(
      (const u16*)d_in[0], (const u16*)d_in[1], (const u16*)d_in[2], (const u16*)d_in[3], (const u16*)d_in[4],
      (const u16*)d_in[6], (const u16*)d_in[7], (const u16*)d_in[8], (const u16*)d_in[9], (const u16*)d_in[10],
      (u16*)d_out, (unsigned char*)d_ws);
}
```

```cpp
#define ADLY 16
#define AWIN 6
#include <hip/hip_runtime.h>
#include <hip/hip_bf16.h>
#include <stdint.h>

#define GAS __attribute__((address_space(1)))
#define LAS __attribute__((address_space(3)))
typedef unsigned short u16;
typedef unsigned v4u __attribute__((ext_vector_type(4)));
typedef unsigned v2u __attribute__((ext_vector_type(2)));
typedef float f32x4 __attribute__((ext_vector_type(4)));
typedef float f32x2 __attribute__((ext_vector_type(2)));
typedef __bf16 bf16x8 __attribute__((ext_vector_type(8)));
typedef __bf16 bf16x2 __attribute__((ext_vector_type(2)));
typedef short s16x4 __attribute__((ext_vector_type(4)));
typedef GAS unsigned gu32;

constexpr int DM = 512, SK = 4096, DH = 128, WINDOW = 1024, FF = 1792, NQ = 4096;
constexpr unsigned WS_QP = 4096;
constexpr unsigned WS_HP = WS_QP + (512u << 10);
constexpr unsigned WS_P2 = WS_HP + (256u << 10);
constexpr unsigned WS_ML = WS_P2 + (1u << 20);
constexpr unsigned WS_P3 = WS_ML + 16384;
constexpr unsigned WS_END = WS_P3 + (2u << 20);
constexpr unsigned SPIN_CAP = 1u << 17;
constexpr unsigned LSPIN_CAP = 1u << 22;
constexpr int AUX_SC1 = 16, AUX_NT = 2;
constexpr float LOG2E = 1.4426950408889634f;
constexpr float SCL2 = 0.08838834764831845f * 1.4426950408889634f;

#define DLY1 16
#define DLY2 8
#define DLY3 10
#define SETPRIO(n) __builtin_amdgcn_s_setprio(n)

__device__ __forceinline__ unsigned pack2(float lo, float hi) {
  return __builtin_bit_cast(unsigned, __builtin_convertvector(f32x2{lo, hi}, bf16x2));
}
__device__ __forceinline__ float bfr(float f) { return (float)(__bf16)f; }
__device__ __forceinline__ float lo2f(unsigned u) { return __builtin_bit_cast(float, u << 16); }
__device__ __forceinline__ float hi2f(unsigned u) { return __builtin_bit_cast(float, u & 0xffff0000u); }
__device__ __forceinline__ unsigned perm_lo(unsigned a, unsigned b) { return __builtin_amdgcn_perm(b, a, 0x05040100u); }
__device__ __forceinline__ unsigned perm_hi(unsigned a, unsigned b) { return __builtin_amdgcn_perm(b, a, 0x07060302u); }

template <int CTRL> __device__ __forceinline__ float dpp(float x) {
  return __builtin_bit_cast(float, __builtin_amdgcn_mov_dpp(__builtin_bit_cast(int, x), CTRL, 0xf, 0xf, true));
}
constexpr int XOR1 = 0xB1, XOR2 = 0x4E, ROR8 = 0x128, ROR4 = 0x124;
__device__ __forceinline__ float x16_max(float x) {
  auto s = __builtin_amdgcn_permlane16_swap(__float_as_uint(x), __float_as_uint(x), false, false);
  return __builtin_fmaxf(__uint_as_float(s[0]), __uint_as_float(s[1]));
}
__device__ __forceinline__ float x32_max(float x) {
  auto s = __builtin_amdgcn_permlane32_swap(__float_as_uint(x), __float_as_uint(x), false, false);
  return __builtin_fmaxf(__uint_as_float(s[0]), __uint_as_float(s[1]));
}
__device__ __forceinline__ float x16_sum(float x) {
  auto s = __builtin_amdgcn_permlane16_swap(__float_as_uint(x), __float_as_uint(x), false, false);
  return __uint_as_float(s[0]) + __uint_as_float(s[1]);
}
__device__ __forceinline__ float x32_sum(float x) {
  auto s = __builtin_amdgcn_permlane32_swap(__float_as_uint(x), __float_as_uint(x), false, false);
  return __uint_as_float(s[0]) + __uint_as_float(s[1]);
}
__device__ __forceinline__ float wave_sum(float x) {
  x += dpp<XOR1>(x); x += dpp<XOR2>(x); x += dpp<ROR4>(x); x += dpp<ROR8>(x);
  return x32_sum(x16_sum(x));
}
__device__ __forceinline__ f32x4 mfma32(v4u a, v4u b, f32x4 c) {
  return __builtin_amdgcn_mfma_f32_16x16x32_bf16(__builtin_bit_cast(bf16x8, a), __builtin_bit_cast(bf16x8, b), c, 0, 0, 0);
}
__device__ __forceinline__ f32x4 mfma16(v2u a, v2u b, f32x4 c) {
  return __builtin_amdgcn_mfma_f32_16x16x16bf16_1k(__builtin_bit_cast(s16x4, a), __builtin_bit_cast(s16x4, b), c, 0, 0, 0);
}
#define PIN() __builtin_amdgcn_sched_barrier(0)
#define BAR() do { PIN(); asm volatile("s_waitcnt lgkmcnt(0)" ::: "memory"); __builtin_amdgcn_s_barrier(); asm volatile("" ::: "memory"); PIN(); } while (0)
#define KEEP(x) asm volatile("" : "+v"(x))
#define STAMP(i)
#define STAMP_INIT()
#define STAMP_DUMP()

template <typename T> __device__ __forceinline__ T ldg(const u16* p) { return *(const GAS T*)p; }
template <typename T> __device__ __forceinline__ T ldnt(const u16* p) { return __builtin_nontemporal_load((const GAS T*)p); }

constexpr int L_CNT = 0;
constexpr int L_ST = 16;
constexpr int L_RQ = 32;
constexpr int L_RH = L_RQ + 1024;
constexpr int L_QS = L_RH + 1024;
constexpr int L_HB = L_QS + 2048;
constexpr int L_MO = L_HB + 32;
constexpr int L_ML = L_MO + 2048;
constexpr int L_OB = L_ML + 32;
constexpr int L_QB = L_OB + 64;
constexpr int L_FR = L_QB + 256;
constexpr int L_KS = L_FR + 32;
constexpr int L_END = L_KS + 8192;
enum { C_Q = 0, C_QG, C_QRDY, C_ISS, C_ATT, C_PUB2, C_H, C_HRDY, C_O, C_PUB3, C_FIN, C_ST, C_QPUB };

__device__ __forceinline__ unsigned arrive(unsigned addr, int lane) {
  unsigned old = 0;
  if (lane == 0)
    asm volatile("s_waitcnt lgkmcnt(0)\n\tds_add_rtn_u32 %0, %1, %2\n\ts_waitcnt lgkmcnt(0)" : "=v"(old) : "v"(addr), "v"(1u) : "memory");
  return __builtin_amdgcn_readfirstlane(old);
}
__device__ __forceinline__ void signal(unsigned addr, int lane) {
  if (lane == 0) asm volatile("s_waitcnt lgkmcnt(0)\n\tds_add_u32 %0, %1" :: "v"(addr), "v"(1u) : "memory");
}
template <int SLEEP> __device__ __forceinline__ void await(unsigned addr, unsigned n) {
#pragma clang loop unroll(disable)
  for (unsigned s = 0; s < LSPIN_CAP; ++s) {
    unsigned v;
    asm volatile("ds_read_b32 %0, %1\n\ts_waitcnt lgkmcnt(0)" : "=v"(v) : "v"(addr) : "memory");
    if ((unsigned)__builtin_amdgcn_readfirstlane(v) >= n) break;
    if (SLEEP) __builtin_amdgcn_s_sleep(SLEEP);
  }
}

__device__ __forceinline__ v4u tr8x4(const v2u (&W)[8], int i) {
  v4u f;
#pragma unroll
  for (int jj = 0; jj < 4; ++jj)
    f[jj] = (i & 1) ? perm_hi(W[2 * jj][i >> 1], W[2 * jj + 1][i >> 1]) : perm_lo(W[2 * jj][i >> 1], W[2 * jj + 1][i >> 1]);
  return f;
}
__device__ __forceinline__ unsigned tagbad(v4u v, unsigned T) { return (v[1] ^ T) | (v[3] ^ T); }

__global__ void __launch_bounds__(512, 2)
fused_kernel(const u16* __restrict__ x, const u16* __restrict__ kcache, const u16* __restrict__ vcache,
             const u16* __restrict__ wq, const u16* __restrict__ wo, const u16* __restrict__ w1,
             const u16* __restrict__ b1, const u16* __restrict__ w2, const u16* __restrict__ b2,
             const u16* __restrict__ gw, u16* __restrict__ out, unsigned char* ws) {
  __shared__ __attribute__((aligned(16))) float lds[L_END];
  asm volatile("" :: "s"(x), "s"(kcache), "s"(vcache), "s"(wq), "s"(wo), "s"(w1), "s"(b1), "s"(w2), "s"(b2), "s"(gw), "s"(out), "s"(ws));
  const int tid = threadIdx.x, lane = tid & 63, w = __builtin_amdgcn_readfirstlane(tid >> 6);
  const int g = lane >> 4, c = lane & 15;
  const int bid = blockIdx.x, hk = bid & 7, idx = bid >> 3;
  const int ab = idx >> 3, as = idx & 7;
  const int kgl = idx >> 1, cg = idx & 1, kg = hk * 16 + kgl;
  const __amdgpu_buffer_rsrc_t rs = __builtin_amdgcn_make_buffer_rsrc((void*)ws, 0, (int)WS_END, 0x00020000);
  const unsigned cn = (unsigned)(size_t)(LAS float*)(lds + L_CNT);
#define CN(i) (cn + 4u * (i))
  STAMP_INIT();
  STAMP(0);

  if (w >= 4) {
    const int aw = w - 4;
    BAR();
    unsigned tt;
    {
      const unsigned char* tp = ws;
      tp = lane == 0 ? (const unsigned char*)(out + (bid >> 6) * DM) : tp;
      tp = lane == 1 ? ws + WS_P3 : tp;
      tp = lane == 2 ? ws + WS_END - 64 : tp;
      tp = lane == 3 ? (const unsigned char*)b2 : tp;
      tp = lane == 4 ? (const unsigned char*)(wo + (size_t)(32 * kg) * DM) : tp;
      tp = lane == 5 ? (const unsigned char*)(w2 + (size_t)(224 * hk) * DM) : tp;
      tp = lane == 6 ? (const unsigned char*)b1 : tp;
      tp = lane == 7 ? ws + WS_P2 : tp;
      tt = *(const GAS unsigned*)tp;
    }
    PIN();
    if (ADLY) __builtin_amdgcn_s_sleep(ADLY);
    v4u kr[8], vf[8];
    {
      const size_t kvoff = ((size_t)(ab * 8 + hk) * SK + (SK - WINDOW) + 128 * as + 32 * aw) * DH;
      const u16* kp = kcache + kvoff + 8 * lane;
      const u16* vp = vcache + kvoff + 4 * g * DH + 8 * c;
#pragma unroll
      for (int i = 0; i < 16; ++i) {
        if (i >= AWIN) {
          PIN();
          asm volatile("s_waitcnt vmcnt(%0)" :: "n"(AWIN - 1) : "memory");
          PIN();
        }
        if (i < 8) kr[i] = ldnt<v4u>(kp + i * 512);
        else { const int j = i - 8; vf[j] = ldnt<v4u>(vp + (16 * (j >> 2) + (j & 3)) * DH); }
      }
    }
    PIN();
    SETPRIO(2);
    const unsigned T = (unsigned)__builtin_amdgcn_readlane(tt, 63) + 1u;
    signal(CN(C_ISS), lane);
    STAMP(1);
    v4u kf[8];
    {
      unsigned char* ks_ = (unsigned char*)(lds + L_KS) + aw * 8192;
#pragma unroll
      for (int i = 0; i < 8; ++i) { const int row = 4 * i + g; *(v4u*)(ks_ + row * 256 + ((c ^ (row & 15)) << 4)) = kr[i]; }
      asm volatile("s_waitcnt lgkmcnt(0)" ::: "memory");
#pragma unroll
      for (int t = 0; t < 2; ++t)
#pragma unroll
        for (int ks = 0; ks < 4; ++ks) kf[t * 4 + ks] = *(const v4u*)(ks_ + (16 * t + c) * 256 + (((4 * ks + g) ^ c) << 4));
#pragma unroll
      for (int i = 0; i < 8; ++i) KEEP(kf[i]);
    }
    PIN();
    STAMP(2);
    await<0>(CN(C_QRDY), 1u);
    STAMP(3);
    v4u pf;
    {
      f32x4 s0 = {0.f, 0.f, 0.f, 0.f}, s1 = {0.f, 0.f, 0.f, 0.f};
      const u16* qb = (const u16*)(lds + L_QB) + (c >> 2) * 128 + 8 * g;
      v4u qf[4];
#pragma unroll
      for (int ks = 0; ks < 4; ++ks) qf[ks] = *(const v4u*)(qb + 32 * ks);
#pragma unroll
      for (int ks = 0; ks < 4; ++ks) {
        s0 = mfma32(kf[ks], qf[ks], s0);
        s1 = mfma32(kf[4 + ks], qf[ks], s1);
      }
      float m = __builtin_fmaxf(__builtin_fmaxf(__builtin_fmaxf(s0[0], s0[1]), __builtin_fmaxf(s0[2], s0[3])),
                                __builtin_fmaxf(__builtin_fmaxf(s1[0], s1[1]), __builtin_fmaxf(s1[2], s1[3])));
      m = x32_max(x16_max(m));
      float p[8];
#pragma unroll
      for (int j = 0; j < 4; ++j) { p[j] = __builtin_amdgcn_exp2f((s0[j] - m) * SCL2); p[4 + j] = __builtin_amdgcn_exp2f((s1[j] - m) * SCL2); }
      pf = v4u{pack2(p[0], p[1]), pack2(p[2], p[3]), pack2(p[4], p[5]), pack2(p[6], p[7])};
      float l = ((p[0] + p[1]) + (p[2] + p[3])) + ((p[4] + p[5]) + (p[6] + p[7]));
      l = x32_sum(x16_sum(l));
      if (g == 0 && (c & 3) == 0) *(f32x2*)(lds + L_ML + (aw * 4 + (c >> 2)) * 2) = f32x2{m * SCL2, l};
      KEEP(pf);
    }
    PIN();
    STAMP(4);
    {
      v4u vt[8];
#pragma unroll
      for (int i = 0; i < 8; ++i)
#pragma unroll
        for (int jj = 0; jj < 4; ++jj)
          vt[i][jj] = (i & 1) ? perm_hi(vf[2 * jj][i >> 1], vf[2 * jj + 1][i >> 1]) : perm_lo(vf[2 * jj][i >> 1], vf[2 * jj + 1][i >> 1]);
      f32x4 o[8];
#pragma unroll
      for (int i = 0; i < 8; ++i) o[i] = mfma32(pf, vt[i], f32x4{0.f, 0.f, 0.f, 0.f});
#pragma unroll
      for (int i = 0; i < 8; ++i) KEEP(o[i]);
      float* mo = lds + L_MO + (aw * 4 + g) * 128 + 8 * c;
      *(f32x4*)(mo) = f32x4{o[0][0], o[1][0], o[2][0], o[3][0]};
      *(f32x4*)(mo + 4) = f32x4{o[4][0], o[5][0], o[6][0], o[7][0]};
    }
    if (arrive(CN(C_ATT), lane) != 3u) await<0>(CN(C_ATT), 4u);
    STAMP(5);
    {
      f32x2 ml[4], ov[4];
#pragma unroll
      for (int u = 0; u < 4; ++u) { ml[u] = *(const f32x2*)(lds + L_ML + (u * 4 + aw) * 2); ov[u] = *(const f32x2*)(lds + L_MO + (u * 4 + aw) * 128 + 2 * lane); }
      const float M = __builtin_fmaxf(__builtin_fmaxf(ml[0][0], ml[1][0]), __builtin_fmaxf(ml[2][0], ml[3][0]));
      float L = 0.f, o0 = 0.f, o1 = 0.f;
#pragma unroll
      for (int u = 0; u < 4; ++u) {
        const float f = __builtin_amdgcn_exp2f(ml[u][0] - M);
        L = fmaf(f, ml[u][1], L); o0 = fmaf(f, ov[u][0], o0); o1 = fmaf(f, ov[u][1], o1);
      }
      const int hg = 4 * hk + aw;
      const v4u pv = {__float_as_uint(o0), T, __float_as_uint(o1), T};
      __builtin_amdgcn_raw_buffer_store_b128(pv, rs, WS_P2 + (((hg * 4 + (lane >> 4)) * 4 + ab) * 8 + as) * 256 + (lane & 15) * 16, 0, AUX_SC1);
      if (lane == 0) {
        const v4u mv = {__float_as_uint(M), T, __float_as_uint(L), T};
        __builtin_amdgcn_raw_buffer_store_b128(mv, rs, WS_ML + ((hg * 4 + ab) * 8 + as) * 16, 0, AUX_SC1);
      }
    }
    PIN();
    signal(CN(C_PUB2), lane);
    STAMP(6);
    const int ob = bid >> 6, ocw = (bid >> 3) & 7, om = (bid >> 1) & 3, oh = bid & 1;
    const int col = 64 * ocw + 4 * (4 * om + (lane & 3)) + 2 * oh;
    const unsigned b2v = ldg<unsigned>(b2 + col);
    const unsigned off = WS_P3 + ((aw * 32 + (lane >> 2)) * 8 + ocw) * 2048 + oh * 1024 + (ob * 16 + 4 * om + (lane & 3)) * 16;
    await<2>(CN(C_PUB3), 4u);
    __builtin_amdgcn_s_sleep(DLY3);
    STAMP(8);
    v4u pv0 = __builtin_amdgcn_raw_buffer_load_b128(rs, off, 0, AUX_SC1);
    v4u pv1 = __builtin_amdgcn_raw_buffer_load_b128(rs, off + 16 * 8 * 2048, 0, AUX_SC1);
#pragma clang loop unroll(disable)
    for (unsigned spins = 0; spins < SPIN_CAP; ++spins) {
      const unsigned bad0 = tagbad(pv0, T), bad1 = tagbad(pv1, T);
      if (__all((bad0 | bad1) == 0)) break;
      asm volatile("" ::: "memory");
      if (bad0) pv0 = __builtin_amdgcn_raw_buffer_load_b128(rs, off, 0, AUX_SC1);
      if (bad1) pv1 = __builtin_amdgcn_raw_buffer_load_b128(rs, off + 16 * 8 * 2048, 0, AUX_SC1);
    }
    STAMP(9);
    float sa = __uint_as_float(pv0[0]) + __uint_as_float(pv1[0]), sb = __uint_as_float(pv0[2]) + __uint_as_float(pv1[2]);
    sa += dpp<ROR4>(sa); sb += dpp<ROR4>(sb); sa += dpp<ROR8>(sa); sb += dpp<ROR8>(sb);
    sa = x16_sum(sa); sb = x16_sum(sb); sa = x32_sum(sa); sb = x32_sum(sb);
    if (lane < 4) *(f32x2*)(lds + L_FR + aw * 8 + 2 * lane) = f32x2{sa, sb};
    if (arrive(CN(C_FIN), lane) == 3u) {
      if (lane < 4) {
        const float omg = lds[L_ST + 4 * ob + 3];
        f32x2 t = *(const f32x2*)(lds + L_FR + 2 * lane);
#pragma unroll
        for (int u = 1; u < 4; ++u) t += *(const f32x2*)(lds + L_FR + u * 8 + 2 * lane);
        *(GAS unsigned*)(out + ob * DM + col) = pack2(fmaf(omg, lo2f(b2v), t[0]), fmaf(omg, hi2f(b2v), t[1]));
      }
      if (bid == 0 && lane == 0) __hip_atomic_store((gu32*)ws, T, __ATOMIC_RELAXED, __HIP_MEMORY_SCOPE_AGENT);
    }
  } else {
    const int pw = w;
    SETPRIO(3);
    const int kq = idx & 3, cq = idx >> 2;
    const bool has_ht = idx < 14;
    const int th = 14 * hk + idx;
    const int qrow = 128 * kq + 32 * pw + 8 * g, hrow = 128 * (th & 3) + 32 * pw + 8 * g;
    v2u wv[8], w1v[8];
    {
      const u16* p = wq + (size_t)qrow * NQ + hk * 512 + 64 * cq + 4 * c;
#pragma unroll
      for (int j = 0; j < 8; ++j) wv[j] = ldnt<v2u>(p + j * NQ);
    }
    const v4u xa = ldg<v4u>(x + (c >> 2) * DM + qrow);
    const unsigned ev = __builtin_amdgcn_raw_buffer_load_b32(rs, 0, 0, 0);
    PIN();
    const v4u xs = ldg<v4u>(x + pw * DM + 8 * lane);
    const v4u gwv = ldg<v4u>(gw + 8 * lane);
    const v4u xh = ldg<v4u>(x + (c >> 2) * DM + hrow);
    {
      const __amdgpu_buffer_rsrc_t rw1 = __builtin_amdgcn_make_buffer_rsrc((void*)w1, 0, has_ht ? DM * FF * 2 : 0, 0x00020000);
      const int o = (hrow * FF + 64 * (th >> 2) + 4 * c) * 2;
#pragma unroll
      for (int j = 0; j < 8; ++j) w1v[j] = __builtin_amdgcn_raw_buffer_load_b64(rw1, o + j * FF * 2, 0, AUX_NT);
    }
    if (tid < 16) ((LAS unsigned*)(lds + L_CNT))[tid] = 0u;
    BAR();
    PIN();
    unsigned T;
    {
      f32x4 d[4];
#pragma unroll
      for (int i = 0; i < 4; ++i) d[i] = mfma32(xa, tr8x4(wv, i), f32x4{0.f, 0.f, 0.f, 0.f});
      *(f32x4*)(lds + L_RQ + (pw * 64 + lane) * 4) = f32x4{d[0][0], d[1][0], d[2][0], d[3][0]};
      T = (unsigned)__builtin_amdgcn_readfirstlane(ev) + 1u;
      if (arrive(CN(C_Q), lane) == 3u) {
        f32x4 a = *(const f32x4*)(lds + L_RQ + lane * 4);
#pragma unroll
        for (int p = 1; p < 4; ++p) a += *(const f32x4*)(lds + L_RQ + (p * 64 + lane) * 4);
        const unsigned off = WS_QP + ((hk * 8 + cq) * 4 + kq) * 2048 + lane * 16;
        __builtin_amdgcn_raw_buffer_store_b128(v4u{__float_as_uint(a[0]), T, __float_as_uint(a[1]), T}, rs, off, 0, AUX_SC1);
        __builtin_amdgcn_raw_buffer_store_b128(v4u{__float_as_uint(a[2]), T, __float_as_uint(a[3]), T}, rs, off + 1024, 0, AUX_SC1);
      }
    }
    PIN();
    STAMP(1);
    {
      float xv[8], gv[8];
#pragma unroll
      for (int k = 0; k < 4; ++k) { xv[2 * k] = lo2f(xs[k]); xv[2 * k + 1] = hi2f(xs[k]); gv[2 * k] = lo2f(gwv[k]); gv[2 * k + 1] = hi2f(gwv[k]); }
      float s = 0.f, gs = 0.f;
#pragma unroll
      for (int k = 0; k < 8; ++k) { s += xv[k]; gs += bfr(xv[k] * gv[k]); }
      s = wave_sum(s); gs = wave_sum(gs);
      const float mean = s * (1.0f / DM);
      float vs = 0.f;
#pragma unroll
      for (int k = 0; k < 8; ++k) { const float d = xv[k] - mean; vs = fmaf(d, d, vs); }
      vs = wave_sum(vs);
      if (lane == 0) {
        const float var_b = bfr(vs * (1.0f / DM));
        const float den = bfr(sqrtf(bfr(var_b + 1e-5f)));
        const float gt = bfr(1.0f / (1.0f + __expf(-bfr(gs))));
        *(f32x4*)(lds + L_ST + 4 * pw) = f32x4{bfr(mean), den, gt, bfr(1.0f - gt)};
      }
      if (arrive(CN(C_ST), lane) != 3u) await<0>(CN(C_ST), 4u);
    }
    if (has_ht) {
      const f32x4 st = *(const f32x4*)(lds + L_ST + 4 * (c >> 2));
      v4u ya;
#pragma unroll
      for (int k = 0; k < 4; ++k) ya[k] = pack2(bfr(lo2f(xh[k]) - st[0]) / st[1], bfr(hi2f(xh[k]) - st[0]) / st[1]);
      f32x4 d[4];
#pragma unroll
      for (int i = 0; i < 4; ++i) d[i] = mfma32(ya, tr8x4(w1v, i), f32x4{0.f, 0.f, 0.f, 0.f});
      *(f32x4*)(lds + L_RH + (pw * 64 + lane) * 4) = f32x4{d[0][0], d[1][0], d[2][0], d[3][0]};
      if (arrive(CN(C_H), lane) == 3u) {
        f32x4 a = *(const f32x4*)(lds + L_RH + lane * 4);
#pragma unroll
        for (int p = 1; p < 4; ++p) a += *(const f32x4*)(lds + L_RH + (p * 64 + lane) * 4);
        const unsigned off = WS_HP + th * 2048 + lane * 16;
        __builtin_amdgcn_raw_buffer_store_b128(v4u{__float_as_uint(a[0]), T, __float_as_uint(a[1]), T}, rs, off, 0, AUX_SC1);
        __builtin_amdgcn_raw_buffer_store_b128(v4u{__float_as_uint(a[2]), T, __float_as_uint(a[3]), T}, rs, off + 1024, 0, AUX_SC1);
      }
    }
    f32x4 stg = *(const f32x4*)(lds + L_ST + 4 * g);
    KEEP(stg);
    PIN();
    STAMP(2);
    __builtin_amdgcn_s_sleep(DLY1);
    {
      const unsigned qoff = WS_QP + ((hk * 8 + (lane >> 5)) * 4 + pw) * 2048 + ((lane >> 4) & 1) * 1024 + (16 * ab + c) * 16;
      v4u qv[4];
#pragma unroll
      for (int i = 0; i < 4; ++i) qv[i] = __builtin_amdgcn_raw_buffer_load_b128(rs, qoff + i * 16384, 0, AUX_SC1);
#pragma clang loop unroll(disable)
      for (unsigned spins = 0; spins < SPIN_CAP; ++spins) {
        unsigned bad[4];
#pragma unroll
        for (int i = 0; i < 4; ++i) bad[i] = tagbad(qv[i], T);
        if (__all((bad[0] | bad[1] | bad[2] | bad[3]) == 0)) break;
        asm volatile("" ::: "memory");
#pragma unroll
        for (int i = 0; i < 4; ++i) if (bad[i]) qv[i] = __builtin_amdgcn_raw_buffer_load_b128(rs, qoff + i * 16384, 0, AUX_SC1);
      }
      float* qs = lds + L_QS + (pw * 64 + lane) * 8;
      *(v4u*)(qs) = v4u{qv[0][0], qv[0][2], qv[1][0], qv[1][2]};
      *(v4u*)(qs + 4) = v4u{qv[2][0], qv[2][2], qv[3][0], qv[3][2]};
      if (arrive(CN(C_QG), lane) == 3u) {
        f32x4 a0 = *(const f32x4*)(lds + L_QS + lane * 8), a1 = *(const f32x4*)(lds + L_QS + lane * 8 + 4);
#pragma unroll
        for (int p = 1; p < 4; ++p) { a0 += *(const f32x4*)(lds + L_QS + (p * 64 + lane) * 8); a1 += *(const f32x4*)(lds + L_QS + (p * 64 + lane) * 8 + 4); }
        unsigned* qb = (unsigned*)(lds + L_QB) + 32 * (lane >> 5) + 2 * c + ((lane >> 4) & 1);
        qb[0] = pack2(a0[0], a0[1]); qb[64] = pack2(a0[2], a0[3]); qb[128] = pack2(a1[0], a1[1]); qb[192] = pack2(a1[2], a1[3]);
        signal(CN(C_QRDY), lane);
      }
    }
    STAMP(3);
    await<4>(CN(C_ISS), 4u);
    SETPRIO(0);
    const bool has_mlp = kgl < 14;
    const int fs = 14 * hk + kgl, f0 = 16 * fs;
    v2u wov[8], w2v[4];
    {
      const __amdgpu_buffer_rsrc_t rw2 = __builtin_amdgcn_make_buffer_rsrc((void*)w2, 0, has_mlp ? DM * FF * 2 : 0, 0x00020000);
      const u16* p = wo + (size_t)(32 * kg + 8 * g) * DM + 256 * cg + 64 * pw + 4 * c;
      const int o2 = ((f0 + 4 * g) * DM + 256 * cg + 64 * pw + 4 * c) * 2;
#pragma unroll
      for (int j = 0; j < 8; ++j) wov[j] = ldnt<v2u>(p + j * DM);
#pragma unroll
      for (int j = 0; j < 4; ++j) w2v[j] = __builtin_amdgcn_raw_buffer_load_b64(rw2, o2 + j * DM * 2, 0, AUX_NT);
    }
    PIN();
    SETPRIO(3);
    STAMP(4);
    if (pw == 3) {
      float hv = 0.f;
      if (has_mlp) {
        const float bv = lo2f((unsigned)ldg<u16>(b1 + f0 + 4 * (c & 3) + g));
        const unsigned hoff = WS_HP + ((fs >> 2) * 4 + g) * 2048 + (16 * (c >> 2) + 4 * (fs & 3) + (c & 3)) * 16;
        v4u h0 = __builtin_amdgcn_raw_buffer_load_b128(rs, hoff, 0, AUX_SC1);
        v4u h1 = __builtin_amdgcn_raw_buffer_load_b128(rs, hoff + 1024, 0, AUX_SC1);
#pragma clang loop unroll(disable)
        for (unsigned spins = 0; spins < SPIN_CAP; ++spins) {
          const unsigned bad = tagbad(h0, T) | tagbad(h1, T);
          if (__all(bad == 0)) break;
          asm volatile("" ::: "memory");
          if (bad) {
            h0 = __builtin_amdgcn_raw_buffer_load_b128(rs, hoff, 0, AUX_SC1);
            h1 = __builtin_amdgcn_raw_buffer_load_b128(rs, hoff + 1024, 0, AUX_SC1);
          }
        }
        float a[4] = {__uint_as_float(h0[0]), __uint_as_float(h0[2]), __uint_as_float(h1[0]), __uint_as_float(h1[2])};
#pragma unroll
        for (int i = 0; i < 4; ++i) a[i] = x32_sum(x16_sum(a[i]));
        const float av = g == 0 ? a[0] : g == 1 ? a[1] : g == 2 ? a[2] : a[3];
        const float u = bfr(bfr(av) + bv);
        const float inner = 0.7978845608028654f * (u + 0.044715f * u * u * u);
        hv = 0.5f * u * (1.0f + tanhf(inner));
      }
      ((u16*)(lds + L_HB))[(c >> 2) * 16 + 4 * (c & 3) + g] = (u16)pack2(hv, 0.f);
      signal(CN(C_HRDY), lane);
    }
    v4u wof[4];
    float accm[4];
    {
      v2u w2f[4];
#pragma unroll
      for (int i = 0; i < 4; ++i) {
        wof[i] = tr8x4(wov, i);
#pragma unroll
        for (int jj = 0; jj < 2; ++jj)
          w2f[i][jj] = (i & 1) ? perm_hi(w2v[2 * jj][i >> 1], w2v[2 * jj + 1][i >> 1]) : perm_lo(w2v[2 * jj][i >> 1], w2v[2 * jj + 1][i >> 1]);
        KEEP(wof[i]);
      }
      await<1>(CN(C_HRDY), 1u);
      const v2u ha = *(const v2u*)((const u16*)(lds + L_HB) + (c >> 2) * 16 + 4 * g);
#pragma unroll
      for (int i = 0; i < 4; ++i) {
        const f32x4 e = mfma16(ha, w2f[i], f32x4{0.f, 0.f, 0.f, 0.f});
        accm[i] = stg[3] * e[0];
        KEEP(accm[i]);
      }
    }
    PIN();
    STAMP(5);
    const int h2 = 4 * hk + (kgl >> 2);
    const unsigned p2off = WS_P2 + (((h2 * 4 + (kgl & 3)) * 4 + pw) * 8 + (lane >> 3)) * 256 + (lane & 7) * 32;
    const unsigned mloff = WS_ML + ((h2 * 4 + pw) * 8 + (lane >> 3)) * 16;
    await<1>(CN(C_PUB2), 4u);
    __builtin_amdgcn_s_sleep(DLY2);
    STAMP(6);
    v4u a0 = __builtin_amdgcn_raw_buffer_load_b128(rs, p2off, 0, AUX_SC1);
    v4u a1 = __builtin_amdgcn_raw_buffer_load_b128(rs, p2off + 16, 0, AUX_SC1);
    v4u am = __builtin_amdgcn_raw_buffer_load_b128(rs, mloff, 0, AUX_SC1);
#pragma clang loop unroll(disable)
    for (unsigned spins = 0; spins < SPIN_CAP; ++spins) {
      const unsigned bad = tagbad(a0, T) | tagbad(a1, T) | tagbad(am, T);
      if (__all(bad == 0)) break;
      asm volatile("" ::: "memory");
      if (bad) {
        a0 = __builtin_amdgcn_raw_buffer_load_b128(rs, p2off, 0, AUX_SC1);
        a1 = __builtin_amdgcn_raw_buffer_load_b128(rs, p2off + 16, 0, AUX_SC1);
        am = __builtin_amdgcn_raw_buffer_load_b128(rs, mloff, 0, AUX_SC1);
      }
    }
    STAMP(7);
    {
      const float m = __uint_as_float(am[0]), l = __uint_as_float(am[2]);
      float M = __builtin_fmaxf(m, dpp<ROR8>(m)); M = x32_max(x16_max(M));
      const float f = __builtin_amdgcn_exp2f(m - M);
      float v[5] = {f * __uint_as_float(a0[0]), f * __uint_as_float(a0[2]), f * __uint_as_float(a1[0]), f * __uint_as_float(a1[2]), f * l};
#pragma unroll
      for (int i = 0; i < 5; ++i) v[i] += dpp<ROR8>(v[i]);
#pragma unroll
      for (int i = 0; i < 5; ++i) v[i] = x16_sum(v[i]);
#pragma unroll
      for (int i = 0; i < 5; ++i) v[i] = x32_sum(v[i]);
      const float inv = __builtin_amdgcn_rcpf(v[4]);
      if (lane < 8) *(v2u*)((u16*)(lds + L_OB) + pw * 32 + 4 * lane) = v2u{pack2(v[0] * inv, v[1] * inv), pack2(v[2] * inv, v[3] * inv)};
    }
    if (arrive(CN(C_O), lane) != 3u) await<0>(CN(C_O), 4u);
    {
      const v4u oa = *(const v4u*)((const u16*)(lds + L_OB) + (c >> 2) * 32 + 8 * g);
      f32x4 d[4];
#pragma unroll
      for (int i = 0; i < 4; ++i) d[i] = mfma32(oa, wof[i], f32x4{0.f, 0.f, 0.f, 0.f});
#pragma unroll
      for (int i = 0; i < 4; ++i) KEEP(d[i]);
      const unsigned off = WS_P3 + ((kg * 2 + cg) * 4 + pw) * 2048 + lane * 16;
      const v4u s0 = {__float_as_uint(fmaf(stg[2], d[0][0], accm[0])), T, __float_as_uint(fmaf(stg[2], d[1][0], accm[1])), T};
      const v4u s1 = {__float_as_uint(fmaf(stg[2], d[2][0], accm[2])), T, __float_as_uint(fmaf(stg[2], d[3][0], accm[3])), T};
      __builtin_amdgcn_raw_buffer_store_b128(s0, rs, off, 0, AUX_SC1);
      __builtin_amdgcn_raw_buffer_store_b128(s1, rs, off + 1024, 0, AUX_SC1);
    }
    PIN();
    signal(CN(C_PUB3), lane);
    STAMP(8);
  }
  STAMP_DUMP();
}

extern "C" void kernel_launch(void* const* d_in, const int* in_sizes, int n_in,
                              void* d_out, int out_size, void* d_ws, size_t ws_size,
                              hipStream_t stream) {
  (void)in_sizes; (void)n_in; (void)out_size; (void)ws_size;
  fused_kernel<<<256, 512, 0, stream>>>(
      (const u16*)d_in[0], (const u16*)d_in[1], (const u16*)d_in[2], (const u16*)d_in[3], (const u16*)d_in[4],
      (const u16*)d_in[6], (const u16*)d_in[7], (const u16*)d_in[8], (const u16*)d_in[9], (const u16*)d_in[10],
      (u16*)d_out, (unsigned char*)d_ws);
}
```

```cpp
#define ADLY 0
#define AWIN 6
#include <hip/hip_runtime.h>
#include <hip/hip_bf16.h>
#include <stdint.h>

#define GAS __attribute__((address_space(1)))
#define LAS __attribute__((address_space(3)))
typedef unsigned short u16;
typedef unsigned v4u __attribute__((ext_vector_type(4)));
typedef unsigned v2u __attribute__((ext_vector_type(2)));
typedef float f32x4 __attribute__((ext_vector_type(4)));
typedef float f32x2 __attribute__((ext_vector_type(2)));
typedef __bf16 bf16x8 __attribute__((ext_vector_type(8)));
typedef __bf16 bf16x2 __attribute__((ext_vector_type(2)));
typedef short s16x4 __attribute__((ext_vector_type(4)));
typedef GAS unsigned gu32;

constexpr int DM = 512, SK = 4096, DH = 128, WINDOW = 1024, FF = 1792, NQ = 4096;
constexpr unsigned WS_QP = 4096;
constexpr unsigned WS_HP = WS_QP + (512u << 10);
constexpr unsigned WS_P2 = WS_HP + (256u << 10);
constexpr unsigned WS_ML = WS_P2 + (1u << 20);
constexpr unsigned WS_P3 = WS_ML + 16384;
constexpr unsigned WS_END = WS_P3 + (2u << 20);
constexpr unsigned SPIN_CAP = 1u << 17;
constexpr unsigned LSPIN_CAP = 1u << 22;
constexpr int AUX_SC1 = 16, AUX_NT = 2;
constexpr float LOG2E = 1.4426950408889634f;
constexpr float SCL2 = 0.08838834764831845f * 1.4426950408889634f;

#define DLY1 16
#define DLY2 8
#define DLY3 10
#define SETPRIO(n) __builtin_amdgcn_s_setprio(n)

__device__ __forceinline__ unsigned pack2(float lo, float hi) {
  return __builtin_bit_cast(unsigned, __builtin_convertvector(f32x2{lo, hi}, bf16x2));
}
__device__ __forceinline__ float bfr(float f) { return (float)(__bf16)f; }
__device__ __forceinline__ float lo2f(unsigned u) { return __builtin_bit_cast(float, u << 16); }
__device__ __forceinline__ float hi2f(unsigned u) { return __builtin_bit_cast(float, u & 0xffff0000u); }
__device__ __forceinline__ unsigned perm_lo(unsigned a, unsigned b) { return __builtin_amdgcn_perm(b, a, 0x05040100u); }
__device__ __forceinline__ unsigned perm_hi(unsigned a, unsigned b) { return __builtin_amdgcn_perm(b, a, 0x07060302u); }

template <int CTRL> __device__ __forceinline__ float dpp(float x) {
  return __builtin_bit_cast(float, __builtin_amdgcn_mov_dpp(__builtin_bit_cast(int, x), CTRL, 0xf, 0xf, true));
}
constexpr int XOR1 = 0xB1, XOR2 = 0x4E, ROR8 = 0x128, ROR4 = 0x124;
__device__ __forceinline__ float x16_max(float x) {
  auto s = __builtin_amdgcn_permlane16_swap(__float_as_uint(x), __float_as_uint(x), false, false);
  return __builtin_fmaxf(__uint_as_float(s[0]), __uint_as_float(s[1]));
}
__device__ __forceinline__ float x32_max(float x) {
  auto s = __builtin_amdgcn_permlane32_swap(__float_as_uint(x), __float_as_uint(x), false, false);
  return __builtin_fmaxf(__uint_as_float(s[0]), __uint_as_float(s[1]));
}
__device__ __forceinline__ float x16_sum(float x) {
  auto s = __builtin_amdgcn_permlane16_swap(__float_as_uint(x), __float_as_uint(x), false, false);
  return __uint_as_float(s[0]) + __uint_as_float(s[1]);
}
__device__ __forceinline__ float x32_sum(float x) {
  auto s = __builtin_amdgcn_permlane32_swap(__float_as_uint(x), __float_as_uint(x), false, false);
  return __uint_as_float(s[0]) + __uint_as_float(s[1]);
}
__device__ __forceinline__ float wave_sum(float x) {
  x += dpp<XOR1>(x); x += dpp<XOR2>(x); x += dpp<ROR4>(x); x += dpp<ROR8>(x);
  return x32_sum(x16_sum(x));
}
__device__ __forceinline__ f32x4 mfma32(v4u a, v4u b, f32x4 c) {
  return __builtin_amdgcn_mfma_f32_16x16x32_bf16(__builtin_bit_cast(bf16x8, a), __builtin_bit_cast(bf16x8, b), c, 0, 0, 0);
}
__device__ __forceinline__ f32x4 mfma16(v2u a, v2u b, f32x4 c) {
  return __builtin_amdgcn_mfma_f32_16x16x16bf16_1k(__builtin_bit_cast(s16x4, a), __builtin_bit_cast(s16x4, b), c, 0, 0, 0);
}
#define PIN() __builtin_amdgcn_sched_barrier(0)
#define BAR() do { PIN(); asm volatile("s_waitcnt lgkmcnt(0)" ::: "memory"); __builtin_amdgcn_s_barrier(); asm volatile("" ::: "memory"); PIN(); } while (0)
#define KEEP(x) asm volatile("" : "+v"(x))
#define STAMP(i)
#define STAMP_INIT()
#define STAMP_DUMP()

template <typename T> __device__ __forceinline__ T ldg(const u16* p) { return *(const GAS T*)p; }
template <typename T> __device__ __forceinline__ T ldnt(const u16* p) { return __builtin_nontemporal_load((const GAS T*)p); }

constexpr int L_CNT = 0;
constexpr int L_ST = 16;
constexpr int L_RQ = 32;
constexpr int L_RH = L_RQ + 1024;
constexpr int L_QS = L_RH + 1024;
constexpr int L_HB = L_QS + 2048;
constexpr int L_MO = L_HB + 32;
constexpr int L_ML = L_MO + 2048;
constexpr int L_OB = L_ML + 32;
constexpr int L_QB = L_OB + 64;
constexpr int L_FR = L_QB + 256;
constexpr int L_KS = L_FR + 32;
constexpr int L_END = L_KS + 8192;
enum { C_Q = 0, C_QG, C_QRDY, C_ISS, C_ATT, C_PUB2, C_H, C_HRDY, C_O, C_PUB3, C_FIN, C_ST, C_QPUB };

__device__ __forceinline__ unsigned arrive(unsigned addr, int lane) {
  unsigned old = 0;
  if (lane == 0)
    asm volatile("s_waitcnt lgkmcnt(0)\n\tds_add_rtn_u32 %0, %1, %2\n\ts_waitcnt lgkmcnt(0)" : "=v"(old) : "v"(addr), "v"(1u) : "memory");
  return __builtin_amdgcn_readfirstlane(old);
}
__device__ __forceinline__ void signal(unsigned addr, int lane) {
  if (lane == 0) asm volatile("s_waitcnt lgkmcnt(0)\n\tds_add_u32 %0, %1" :: "v"(addr), "v"(1u) : "memory");
}
template <int SLEEP> __device__ __forceinline__ void await(unsigned addr, unsigned n) {
#pragma clang loop unroll(disable)
  for (unsigned s = 0; s < LSPIN_CAP; ++s) {
    unsigned v;
    asm volatile("ds_read_b32 %0, %1\n\ts_waitcnt lgkmcnt(0)" : "=v"(v) : "v"(addr) : "memory");
    if ((unsigned)__builtin_amdgcn_readfirstlane(v) >= n) break;
    if (SLEEP) __builtin_amdgcn_s_sleep(SLEEP);
  }
}

__device__ __forceinline__ v4u tr8x4(const v2u (&W)[8], int i) {
  v4u f;
#pragma unroll
  for (int jj = 0; jj < 4; ++jj)
    f[jj] = (i & 1) ? perm_hi(W[2 * jj][i >> 1], W[2 * jj + 1][i >> 1]) : perm_lo(W[2 * jj][i >> 1], W[2 * jj + 1][i >> 1]);
  return f;
}
__device__ __forceinline__ unsigned tagbad(v4u v, unsigned T) { return (v[1] ^ T) | (v[3] ^ T); }

__global__ void __launch_bounds__(512, 2)
fused_kernel(const u16* __restrict__ x, const u16* __restrict__ kcache, const u16* __restrict__ vcache,
             const u16* __restrict__ wq, const u16* __restrict__ wo, const u16* __restrict__ w1,
             const u16* __restrict__ b1, const u16* __restrict__ w2, const u16* __restrict__ b2,
             const u16* __restrict__ gw, u16* __restrict__ out, unsigned char* ws) {
  __shared__ __attribute__((aligned(16))) float lds[L_END];
  asm volatile("" :: "s"(x), "s"(kcache), "s"(vcache), "s"(wq), "s"(wo), "s"(w1), "s"(b1), "s"(w2), "s"(b2), "s"(gw), "s"(out), "s"(ws));
  const int tid = threadIdx.x, lane = tid & 63, w = __builtin_amdgcn_readfirstlane(tid >> 6);
  const int g = lane >> 4, c = lane & 15;
  const int bid = blockIdx.x, hk = bid & 7, idx = bid >> 3;
  const int ab = idx >> 3, as = idx & 7;
  const int kgl = idx >> 1, cg = idx & 1, kg = hk * 16 + kgl;
  const __amdgpu_buffer_rsrc_t rs = __builtin_amdgcn_make_buffer_rsrc((void*)ws, 0, (int)WS_END, 0x00020000);
  const unsigned cn = (unsigned)(size_t)(LAS float*)(lds + L_CNT);
#define CN(i) (cn + 4u * (i))
  STAMP_INIT();
  STAMP(0);

  if (w >= 4) {
    const int aw = w - 4;
    BAR();
    unsigned tt;
    {
      const unsigned char* tp = ws;
      tp = lane == 0 ? (const unsigned char*)(out + (bid >> 6) * DM) : tp;
      tp = lane == 1 ? ws + WS_P3 : tp;
      tp = lane == 2 ? ws + WS_END - 64 : tp;
      tp = lane == 3 ? (const unsigned char*)b2 : tp;
      tp = lane == 4 ? (const unsigned char*)(wo + (size_t)(32 * kg) * DM) : tp;
      tp = lane == 5 ? (const unsigned char*)(w2 + (size_t)(224 * hk) * DM) : tp;
      tp = lane == 6 ? (const unsigned char*)b1 : tp;
      tp = lane == 7 ? ws + WS_P2 : tp;
      tp = lane == 8 ? (const unsigned char*)(vcache + ((size_t)(ab * 8 + hk) * SK + (SK - WINDOW) + 128 * as + 32 * aw) * DH) : tp;
      tp = lane == 9 ? (const unsigned char*)(wo + (size_t)(32 * kg + 31) * DM + 511) - 2 : tp;
      tp = lane == 10 ? ws + WS_HP : tp;
      tp = lane == 11 ? ws + WS_ML : tp;
      tt = *(const GAS unsigned*)tp;
    }
    PIN();
    if (ADLY) __builtin_amdgcn_s_sleep(ADLY);
    v4u kr[8], vf[8];
    {
      const size_t kvoff = ((size_t)(ab * 8 + hk) * SK + (SK - WINDOW) + 128 * as + 32 * aw) * DH;
      const u16* kp = kcache + kvoff + 8 * lane;
      const u16* vp = vcache + kvoff + 4 * g * DH + 8 * c;
#pragma unroll
      for (int i = 0; i < 16; ++i) {
        if (i >= AWIN) {
          PIN();
          asm volatile("s_waitcnt vmcnt(%0)" :: "n"(AWIN - 1) : "memory");
          PIN();
        }
        if (i < 8) kr[i] = ldnt<v4u>(kp + i * 512);
        else { const int j = i - 8; vf[j] = ldnt<v4u>(vp + (16 * (j >> 2) + (j & 3)) * DH); }
      }
    }
    PIN();
    SETPRIO(2);
    const unsigned T = (unsigned)__builtin_amdgcn_readlane(tt, 63) + 1u;
    signal(CN(C_ISS), lane);
    STAMP(1);
    v4u kf[8];
    {
      unsigned char* ks_ = (unsigned char*)(lds + L_KS) + aw * 8192;
#pragma unroll
      for (int i = 0; i < 8; ++i) { const int row = 4 * i + g; *(v4u*)(ks_ + row * 256 + ((c ^ (row & 15)) << 4)) = kr[i]; }
      asm volatile("s_waitcnt lgkmcnt(0)" ::: "memory");
#pragma unroll
      for (int t = 0; t < 2; ++t)
#pragma unroll
        for (int ks = 0; ks < 4; ++ks) kf[t * 4 + ks] = *(const v4u*)(ks_ + (16 * t + c) * 256 + (((4 * ks + g) ^ c) << 4));
#pragma unroll
      for (int i = 0; i < 8; ++i) KEEP(kf[i]);
    }
    PIN();
    STAMP(2);
    await<0>(CN(C_QRDY), 1u);
    STAMP(3);
    v4u pf;
    {
      f32x4 s0 = {0.f, 0.f, 0.f, 0.f}, s1 = {0.f, 0.f, 0.f, 0.f};
      const u16* qb = (const u16*)(lds + L_QB) + (c >> 2) * 128 + 8 * g;
      v4u qf[4];
#pragma unroll
      for (int ks = 0; ks < 4; ++ks) qf[ks] = *(const v4u*)(qb + 32 * ks);
#pragma unroll
      for (int ks = 0; ks < 4; ++ks) {
        s0 = mfma32(kf[ks], qf[ks], s0);
        s1 = mfma32(kf[4 + ks], qf[ks], s1);
      }
      float m = __builtin_fmaxf(__builtin_fmaxf(__builtin_fmaxf(s0[0], s0[1]), __builtin_fmaxf(s0[2], s0[3])),
                                __builtin_fmaxf(__builtin_fmaxf(s1[0], s1[1]), __builtin_fmaxf(s1[2], s1[3])));
      m = x32_max(x16_max(m));
      float p[8];
#pragma unroll
      for (int j = 0; j < 4; ++j) { p[j] = __builtin_amdgcn_exp2f((s0[j] - m) * SCL2); p[4 + j] = __builtin_amdgcn_exp2f((s1[j] - m) * SCL2); }
      pf = v4u{pack2(p[0], p[1]), pack2(p[2], p[3]), pack2(p[4], p[5]), pack2(p[6], p[7])};
      float l = ((p[0] + p[1]) + (p[2] + p[3])) + ((p[4] + p[5]) + (p[6] + p[7]));
      l = x32_sum(x16_sum(l));
      if (g == 0 && (c & 3) == 0) *(f32x2*)(lds + L_ML + (aw * 4 + (c >> 2)) * 2) = f32x2{m * SCL2, l};
      KEEP(pf);
    }
    PIN();
    STAMP(4);
    {
      v4u vt[8];
#pragma unroll
      for (int i = 0; i < 8; ++i)
#pragma unroll
        for (int jj = 0; jj < 4; ++jj)
          vt[i][jj] = (i & 1) ? perm_hi(vf[2 * jj][i >> 1], vf[2 * jj + 1][i >> 1]) : perm_lo(vf[2 * jj][i >> 1], vf[2 * jj + 1][i >> 1]);
      f32x4 o[8];
#pragma unroll
      for (int i = 0; i < 8; ++i) o[i] = mfma32(pf, vt[i], f32x4{0.f, 0.f, 0.f, 0.f});
#pragma unroll
      for (int i = 0; i < 8; ++i) KEEP(o[i]);
      float* mo = lds + L_MO + (aw * 4 + g) * 128 + 8 * c;
      *(f32x4*)(mo) = f32x4{o[0][0], o[1][0], o[2][0], o[3][0]};
      *(f32x4*)(mo + 4) = f32x4{o[4][0], o[5][0], o[6][0], o[7][0]};
    }
    if (arrive(CN(C_ATT), lane) != 3u) await<0>(CN(C_ATT), 4u);
    STAMP(5);
    {
      f32x2 ml[4], ov[4];
#pragma unroll
      for (int u = 0; u < 4; ++u) { ml[u] = *(const f32x2*)(lds + L_ML + (u * 4 + aw) * 2); ov[u] = *(const f32x2*)(lds + L_MO + (u * 4 + aw) * 128 + 2 * lane); }
      const float M = __builtin_fmaxf(__builtin_fmaxf(ml[0][0], ml[1][0]), __builtin_fmaxf(ml[2][0], ml[3][0]));
      float L = 0.f, o0 = 0.f, o1 = 0.f;
#pragma unroll
      for (int u = 0; u < 4; ++u) {
        const float f = __builtin_amdgcn_exp2f(ml[u][0] - M);
        L = fmaf(f, ml[u][1], L); o0 = fmaf(f, ov[u][0], o0); o1 = fmaf(f, ov[u][1], o1);
      }
      const int hg = 4 * hk + aw;
      const v4u pv = {__float_as_uint(o0), T, __float_as_uint(o1), T};
      __builtin_amdgcn_raw_buffer_store_b128(pv, rs, WS_P2 + (((hg * 4 + (lane >> 4)) * 4 + ab) * 8 + as) * 256 + (lane & 15) * 16, 0, AUX_SC1);
      if (lane == 0) {
        const v4u mv = {__float_as_uint(M), T, __float_as_uint(L), T};
        __builtin_amdgcn_raw_buffer_store_b128(mv, rs, WS_ML + ((hg * 4 + ab) * 8 + as) * 16, 0, AUX_SC1);
      }
    }
    PIN();
    signal(CN(C_PUB2), lane);
    STAMP(6);
    const int ob = bid >> 6, ocw = (bid >> 3) & 7, om = (bid >> 1) & 3, oh = bid & 1;
    const int col = 64 * ocw + 4 * (4 * om + (lane & 3)) + 2 * oh;
    const unsigned b2v = ldg<unsigned>(b2 + col);
    const unsigned off = WS_P3 + ((aw * 32 + (lane >> 2)) * 8 + ocw) * 2048 + oh * 1024 + (ob * 16 + 4 * om + (lane & 3)) * 16;
    await<2>(CN(C_PUB3), 4u);
    __builtin_amdgcn_s_sleep(DLY3);
    STAMP(8);
    v4u pv0 = __builtin_amdgcn_raw_buffer_load_b128(rs, off, 0, AUX_SC1);
    v4u pv1 = __builtin_amdgcn_raw_buffer_load_b128(rs, off + 16 * 8 * 2048, 0, AUX_SC1);
#pragma clang loop unroll(disable)
    for (unsigned spins = 0; spins < SPIN_CAP; ++spins) {
      const unsigned bad0 = tagbad(pv0, T), bad1 = tagbad(pv1, T);
      if (__all((bad0 | bad1) == 0)) break;
      asm volatile("" ::: "memory");
      if (bad0) pv0 = __builtin_amdgcn_raw_buffer_load_b128(rs, off, 0, AUX_SC1);
      if (bad1) pv1 = __builtin_amdgcn_raw_buffer_load_b128(rs, off + 16 * 8 * 2048, 0, AUX_SC1);
    }
    STAMP(9);
    float sa = __uint_as_float(pv0[0]) + __uint_as_float(pv1[0]), sb = __uint_as_float(pv0[2]) + __uint_as_float(pv1[2]);
    sa += dpp<ROR4>(sa); sb += dpp<ROR4>(sb); sa += dpp<ROR8>(sa); sb += dpp<ROR8>(sb);
    sa = x16_sum(sa); sb = x16_sum(sb); sa = x32_sum(sa); sb = x32_sum(sb);
    if (lane < 4) *(f32x2*)(lds + L_FR + aw * 8 + 2 * lane) = f32x2{sa, sb};
    if (arrive(CN(C_FIN), lane) == 3u) {
      if (lane < 4) {
        const float omg = lds[L_ST + 4 * ob + 3];
        f32x2 t = *(const f32x2*)(lds + L_FR + 2 * lane);
#pragma unroll
        for (int u = 1; u < 4; ++u) t += *(const f32x2*)(lds + L_FR + u * 8 + 2 * lane);
        *(GAS unsigned*)(out + ob * DM + col) = pack2(fmaf(omg, lo2f(b2v), t[0]), fmaf(omg, hi2f(b2v), t[1]));
      }
      if (bid == 0 && lane == 0) __hip_atomic_store((gu32*)ws, T, __ATOMIC_RELAXED, __HIP_MEMORY_SCOPE_AGENT);
    }
  } else {
    const int pw = w;
    SETPRIO(3);
    const int kq = idx & 3, cq = idx >> 2;
    const bool has_ht = idx < 14;
    const int th = 14 * hk + idx;
    const int qrow = 128 * kq + 32 * pw + 8 * g, hrow = 128 * (th & 3) + 32 * pw + 8 * g;
    v2u wv[8], w1v[8];
    {
      const u16* p = wq + (size_t)qrow * NQ + hk * 512 + 64 * cq + 4 * c;
#pragma unroll
      for (int j = 0; j < 8; ++j) wv[j] = ldnt<v2u>(p + j * NQ);
    }
    const v4u xa = ldg<v4u>(x + (c >> 2) * DM + qrow);
    const unsigned ev = __builtin_amdgcn_raw_buffer_load_b32(rs, 0, 0, 0);
    PIN();
    const v4u xs = ldg<v4u>(x + pw * DM + 8 * lane);
    const v4u gwv = ldg<v4u>(gw + 8 * lane);
    const v4u xh = ldg<v4u>(x + (c >> 2) * DM + hrow);
    {
      const __amdgpu_buffer_rsrc_t rw1 = __builtin_amdgcn_make_buffer_rsrc((void*)w1, 0, has_ht ? DM * FF * 2 : 0, 0x00020000);
      const int o = (hrow * FF + 64 * (th >> 2) + 4 * c) * 2;
#pragma unroll
      for (int j = 0; j < 8; ++j) w1v[j] = __builtin_amdgcn_raw_buffer_load_b64(rw1, o + j * FF * 2, 0, AUX_NT);
    }
    if (tid < 16) ((LAS unsigned*)(lds + L_CNT))[tid] = 0u;
    BAR();
    PIN();
    unsigned T;
    {
      f32x4 d[4];
#pragma unroll
      for (int i = 0; i < 4; ++i) d[i] = mfma32(xa, tr8x4(wv, i), f32x4{0.f, 0.f, 0.f, 0.f});
      *(f32x4*)(lds + L_RQ + (pw * 64 + lane) * 4) = f32x4{d[0][0], d[1][0], d[2][0], d[3][0]};
      T = (unsigned)__builtin_amdgcn_readfirstlane(ev) + 1u;
      if (arrive(CN(C_Q), lane) == 3u) {
        f32x4 a = *(const f32x4*)(lds + L_RQ + lane * 4);
#pragma unroll
        for (int p = 1; p < 4; ++p) a += *(const f32x4*)(lds + L_RQ + (p * 64 + lane) * 4);
        const unsigned off = WS_QP + ((hk * 8 + cq) * 4 + kq) * 2048 + lane * 16;
        __builtin_amdgcn_raw_buffer_store_b128(v4u{__float_as_uint(a[0]), T, __float_as_uint(a[1]), T}, rs, off, 0, AUX_SC1);
        __builtin_amdgcn_raw_buffer_store_b128(v4u{__float_as_uint(a[2]), T, __float_as_uint(a[3]), T}, rs, off + 1024, 0, AUX_SC1);
      }
    }
    PIN();
    STAMP(1);
    {
      float xv[8], gv[8];
#pragma unroll
      for (int k = 0; k < 4; ++k) { xv[2 * k] = lo2f(xs[k]); xv[2 * k + 1] = hi2f(xs[k]); gv[2 * k] = lo2f(gwv[k]); gv[2 * k + 1] = hi2f(gwv[k]); }
      float s = 0.f, gs = 0.f;
#pragma unroll
      for (int k = 0; k < 8; ++k) { s += xv[k]; gs += bfr(xv[k] * gv[k]); }
      s = wave_sum(s); gs = wave_sum(gs);
      const float mean = s * (1.0f / DM);
      float vs = 0.f;
#pragma unroll
      for (int k = 0; k < 8; ++k) { const float d = xv[k] - mean; vs = fmaf(d, d, vs); }
      vs = wave_sum(vs);
      if (lane == 0) {
        const float var_b = bfr(vs * (1.0f / DM));
        const float den = bfr(sqrtf(bfr(var_b + 1e-5f)));
        const float gt = bfr(1.0f / (1.0f + __expf(-bfr(gs))));
        *(f32x4*)(lds + L_ST + 4 * pw) = f32x4{bfr(mean), den, gt, bfr(1.0f - gt)};
      }
      if (arrive(CN(C_ST), lane) != 3u) await<0>(CN(C_ST), 4u);
    }
    if (has_ht) {
      const f32x4 st = *(const f32x4*)(lds + L_ST + 4 * (c >> 2));
      v4u ya;
#pragma unroll
      for (int k = 0; k < 4; ++k) ya[k] = pack2(bfr(lo2f(xh[k]) - st[0]) / st[1], bfr(hi2f(xh[k]) - st[0]) / st[1]);
      f32x4 d[4];
#pragma unroll
      for (int i = 0; i < 4; ++i) d[i] = mfma32(ya, tr8x4(w1v, i), f32x4{0.f, 0.f, 0.f, 0.f});
      *(f32x4*)(lds + L_RH + (pw * 64 + lane) * 4) = f32x4{d[0][0], d[1][0], d[2][0], d[3][0]};
      if (arrive(CN(C_H), lane) == 3u) {
        f32x4 a = *(const f32x4*)(lds + L_RH + lane * 4);
#pragma unroll
        for (int p = 1; p < 4; ++p) a += *(const f32x4*)(lds + L_RH + (p * 64 + lane) * 4);
        const unsigned off = WS_HP + th * 2048 + lane * 16;
        __builtin_amdgcn_raw_buffer_store_b128(v4u{__float_as_uint(a[0]), T, __float_as_uint(a[1]), T}, rs, off, 0, AUX_SC1);
        __builtin_amdgcn_raw_buffer_store_b128(v4u{__float_as_uint(a[2]), T, __float_as_uint(a[3]), T}, rs, off + 1024, 0, AUX_SC1);
      }
    }
    f32x4 stg = *(const f32x4*)(lds + L_ST + 4 * g);
    KEEP(stg);
    PIN();
    STAMP(2);
    __builtin_amdgcn_s_sleep(DLY1);
    {
      const unsigned qoff = WS_QP + ((hk * 8 + (lane >> 5)) * 4 + pw) * 2048 + ((lane >> 4) & 1) * 1024 + (16 * ab + c) * 16;
      v4u qv[4];
#pragma unroll
      for (int i = 0; i < 4; ++i) qv[i] = __builtin_amdgcn_raw_buffer_load_b128(rs, qoff + i * 16384, 0, AUX_SC1);
#pragma clang loop unroll(disable)
      for (unsigned spins = 0; spins < SPIN_CAP; ++spins) {
        unsigned bad[4];
#pragma unroll
        for (int i = 0; i < 4; ++i) bad[i] = tagbad(qv[i], T);
        if (__all((bad[0] | bad[1] | bad[2] | bad[3]) == 0)) break;
        asm volatile("" ::: "memory");
#pragma unroll
        for (int i = 0; i < 4; ++i) if (bad[i]) qv[i] = __builtin_amdgcn_raw_buffer_load_b128(rs, qoff + i * 16384, 0, AUX_SC1);
      }
      float* qs = lds + L_QS + (pw * 64 + lane) * 8;
      *(v4u*)(qs) = v4u{qv[0][0], qv[0][2], qv[1][0], qv[1][2]};
      *(v4u*)(qs + 4) = v4u{qv[2][0], qv[2][2], qv[3][0], qv[3][2]};
      if (arrive(CN(C_QG), lane) == 3u) {
        f32x4 a0 = *(const f32x4*)(lds + L_QS + lane * 8), a1 = *(const f32x4*)(lds + L_QS + lane * 8 + 4);
#pragma unroll
        for (int p = 1; p < 4; ++p) { a0 += *(const f32x4*)(lds + L_QS + (p * 64 + lane) * 8); a1 += *(const f32x4*)(lds + L_QS + (p * 64 + lane) * 8 + 4); }
        unsigned* qb = (unsigned*)(lds + L_QB) + 32 * (lane >> 5) + 2 * c + ((lane >> 4) & 1);
        qb[0] = pack2(a0[0], a0[1]); qb[64] = pack2(a0[2], a0[3]); qb[128] = pack2(a1[0], a1[1]); qb[192] = pack2(a1[2], a1[3]);
        signal(CN(C_QRDY), lane);
      }
    }
    STAMP(3);
    await<4>(CN(C_ISS), 4u);
    SETPRIO(0);
    const bool has_mlp = kgl < 14;
    const int fs = 14 * hk + kgl, f0 = 16 * fs;
    v2u wov[8], w2v[4];
    {
      const __amdgpu_buffer_rsrc_t rw2 = __builtin_amdgcn_make_buffer_rsrc((void*)w2, 0, has_mlp ? DM * FF * 2 : 0, 0x00020000);
      const u16* p = wo + (size_t)(32 * kg + 8 * g) * DM + 256 * cg + 64 * pw + 4 * c;
      const int o2 = ((f0 + 4 * g) * DM + 256 * cg + 64 * pw + 4 * c) * 2;
#pragma unroll
      for (int j = 0; j < 8; ++j) wov[j] = ldnt<v2u>(p + j * DM);
#pragma unroll
      for (int j = 0; j < 4; ++j) w2v[j] = __builtin_amdgcn_raw_buffer_load_b64(rw2, o2 + j * DM * 2, 0, AUX_NT);
    }
    PIN();
    SETPRIO(3);
    STAMP(4);
    if (pw == 3) {
      float hv = 0.f;
      if (has_mlp) {
        const float bv = lo2f((unsigned)ldg<u16>(b1 + f0 + 4 * (c & 3) + g));
        const unsigned hoff = WS_HP + ((fs >> 2) * 4 + g) * 2048 + (16 * (c >> 2) + 4 * (fs & 3) + (c & 3)) * 16;
        v4u h0 = __builtin_amdgcn_raw_buffer_load_b128(rs, hoff, 0, AUX_SC1);
        v4u h1 = __builtin_amdgcn_raw_buffer_load_b128(rs, hoff + 1024, 0, AUX_SC1);
#pragma clang loop unroll(disable)
        for (unsigned spins = 0; spins < SPIN_CAP; ++spins) {
          const unsigned bad = tagbad(h0, T) | tagbad(h1, T);
          if (__all(bad == 0)) break;
          asm volatile("" ::: "memory");
          if (bad) {
            h0 = __builtin_amdgcn_raw_buffer_load_b128(rs, hoff, 0, AUX_SC1);
            h1 = __builtin_amdgcn_raw_buffer_load_b128(rs, hoff + 1024, 0, AUX_SC1);
          }
        }
        float a[4] = {__uint_as_float(h0[0]), __uint_as_float(h0[2]), __uint_as_float(h1[0]), __uint_as_float(h1[2])};
#pragma unroll
        for (int i = 0; i < 4; ++i) a[i] = x32_sum(x16_sum(a[i]));
        const float av = g == 0 ? a[0] : g == 1 ? a[1] : g == 2 ? a[2] : a[3];
        const float u = bfr(bfr(av) + bv);
        const float inner = 0.7978845608028654f * (u + 0.044715f * u * u * u);
        hv = 0.5f * u * (1.0f + tanhf(inner));
      }
      ((u16*)(lds + L_HB))[(c >> 2) * 16 + 4 * (c & 3) + g] = (u16)pack2(hv, 0.f);
      signal(CN(C_HRDY), lane);
    }
    v4u wof[4];
    float accm[4];
    {
      v2u w2f[4];
#pragma unroll
      for (int i = 0; i < 4; ++i) {
        wof[i] = tr8x4(wov, i);
#pragma unroll
        for (int jj = 0; jj < 2; ++jj)
          w2f[i][jj] = (i & 1) ? perm_hi(w2v[2 * jj][i >> 1], w2v[2 * jj + 1][i >> 1]) : perm_lo(w2v[2 * jj][i >> 1], w2v[2 * jj + 1][i >> 1]);
        KEEP(wof[i]);
      }
      await<1>(CN(C_HRDY), 1u);
      const v2u ha = *(const v2u*)((const u16*)(lds + L_HB) + (c >> 2) * 16 + 4 * g);
#pragma unroll
      for (int i = 0; i < 4; ++i) {
        const f32x4 e = mfma16(ha, w2f[i], f32x4{0.f, 0.f, 0.f, 0.f});
        accm[i] = stg[3] * e[0];
        KEEP(accm[i]);
      }
    }
    PIN();
    STAMP(5);
    const int h2 = 4 * hk + (kgl >> 2);
    const unsigned p2off = WS_P2 + (((h2 * 4 + (kgl & 3)) * 4 + pw) * 8 + (lane >> 3)) * 256 + (lane & 7) * 32;
    const unsigned mloff = WS_ML + ((h2 * 4 + pw) * 8 + (lane >> 3)) * 16;
    await<1>(CN(C_PUB2), 4u);
    __builtin_amdgcn_s_sleep(DLY2);
    STAMP(6);
    v4u a0 = __builtin_amdgcn_raw_buffer_load_b128(rs, p2off, 0, AUX_SC1);
    v4u a1 = __builtin_amdgcn_raw_buffer_load_b128(rs, p2off + 16, 0, AUX_SC1);
    v4u am = __builtin_amdgcn_raw_buffer_load_b128(rs, mloff, 0, AUX_SC1);
#pragma clang loop unroll(disable)
    for (unsigned spins = 0; spins < SPIN_CAP; ++spins) {
      const unsigned bad = tagbad(a0, T) | tagbad(a1, T) | tagbad(am, T);
      if (__all(bad == 0)) break;
      asm volatile("" ::: "memory");
      if (bad) {
        a0 = __builtin_amdgcn_raw_buffer_load_b128(rs, p2off, 0, AUX_SC1);
        a1 = __builtin_amdgcn_raw_buffer_load_b128(rs, p2off + 16, 0, AUX_SC1);
        am = __builtin_amdgcn_raw_buffer_load_b128(rs, mloff, 0, AUX_SC1);
      }
    }
    STAMP(7);
    {
      const float m = __uint_as_float(am[0]), l = __uint_as_float(am[2]);
      float M = __builtin_fmaxf(m, dpp<ROR8>(m)); M = x32_max(x16_max(M));
      const float f = __builtin_amdgcn_exp2f(m - M);
      float v[5] = {f * __uint_as_float(a0[0]), f * __uint_as_float(a0[2]), f * __uint_as_float(a1[0]), f * __uint_as_float(a1[2]), f * l};
#pragma unroll
      for (int i = 0; i < 5; ++i) v[i] += dpp<ROR8>(v[i]);
#pragma unroll
      for (int i = 0; i < 5; ++i) v[i] = x16_sum(v[i]);
#pragma unroll
      for (int i = 0; i < 5; ++i) v[i] = x32_sum(v[i]);
      const float inv = __builtin_amdgcn_rcpf(v[4]);
      if (lane < 8) *(v2u*)((u16*)(lds + L_OB) + pw * 32 + 4 * lane) = v2u{pack2(v[0] * inv, v[1] * inv), pack2(v[2] * inv, v[3] * inv)};
    }
    if (arrive(CN(C_O), lane) != 3u) await<0>(CN(C_O), 4u);
    {
      const v4u oa = *(const v4u*)((const u16*)(lds + L_OB) + (c >> 2) * 32 + 8 * g);
      f32x4 d[4];
#pragma unroll
      for (int i = 0; i < 4; ++i) d[i] = mfma32(oa, wof[i], f32x4{0.f, 0.f, 0.f, 0.f});
#pragma unroll
      for (int i = 0; i < 4; ++i) KEEP(d[i]);
      const unsigned off = WS_P3 + ((kg * 2 + cg) * 4 + pw) * 2048 + lane * 16;
      const v4u s0 = {__float_as_uint(fmaf(stg[2], d[0][0], accm[0])), T, __float_as_uint(fmaf(stg[2], d[1][0], accm[1])), T};
      const v4u s1 = {__float_as_uint(fmaf(stg[2], d[2][0], accm[2])), T, __float_as_uint(fmaf(stg[2], d[3][0], accm[3])), T};
      __builtin_amdgcn_raw_buffer_store_b128(s0, rs, off, 0, AUX_SC1);
      __builtin_amdgcn_raw_buffer_store_b128(s1, rs, off + 1024, 0, AUX_SC1);
    }
    PIN();
    signal(CN(C_PUB3), lane);
    STAMP(8);
  }
  STAMP_DUMP();
}

extern "C" void kernel_launch(void* const* d_in, const int* in_sizes, int n_in,
                              void* d_out, int out_size, void* d_ws, size_t ws_size,
                              hipStream_t stream) {
  (void)in_sizes; (void)n_in; (void)out_size; (void)ws_size;
  fused_kernel<<<256, 512, 0, stream>>>(
      (const u16*)d_in[0], (const u16*)d_in[1], (const u16*)d_in[2], (const u16*)d_in[3], (const u16*)d_in[4],
      (const u16*)d_in[6], (const u16*)d_in[7], (const u16*)d_in[8], (const u16*)d_in[9], (const u16*)d_in[10],
      (u16*)d_out, (unsigned char*)d_ws);
}
```

```cpp
#define QGATE 2
#define ISSN 4u
#define LATE_FIRST 0
#include <hip/hip_runtime.h>
#include <hip/hip_bf16.h>
#include <stdint.h>

#define GAS __attribute__((address_space(1)))
#define LAS __attribute__((address_space(3)))
typedef unsigned short u16;
typedef unsigned v4u __attribute__((ext_vector_type(4)));
typedef unsigned v2u __attribute__((ext_vector_type(2)));
typedef float f32x4 __attribute__((ext_vector_type(4)));
typedef float f32x2 __attribute__((ext_vector_type(2)));
typedef __bf16 bf16x8 __attribute__((ext_vector_type(8)));
typedef __bf16 bf16x2 __attribute__((ext_vector_type(2)));
typedef short s16x4 __attribute__((ext_vector_type(4)));
typedef GAS unsigned gu32;

constexpr int DM = 512, SK = 4096, DH = 128, WINDOW = 1024, FF = 1792, NQ = 4096;
constexpr unsigned WS_QP = 4096;
constexpr unsigned WS_HP = WS_QP + (512u << 10);
constexpr unsigned WS_P2 = WS_HP + (256u << 10);
constexpr unsigned WS_ML = WS_P2 + (1u << 20);
constexpr unsigned WS_P3 = WS_ML + 16384;
constexpr unsigned WS_END = WS_P3 + (2u << 20);
constexpr unsigned SPIN_CAP = 1u << 17;
constexpr unsigned LSPIN_CAP = 1u << 22;
constexpr int AUX_SC1 = 16, AUX_NT = 2;
constexpr float LOG2E = 1.4426950408889634f;
constexpr float SCL2 = 0.08838834764831845f * 1.4426950408889634f;

#define AWIN 6
#define ADLY 0
#define DLY1 16
#define DLY2 8
#define DLY3 10
#define SETPRIO(n) __builtin_amdgcn_s_setprio(n)

__device__ __forceinline__ unsigned pack2(float lo, float hi) {
  return __builtin_bit_cast(unsigned, __builtin_convertvector(f32x2{lo, hi}, bf16x2));
}
__device__ __forceinline__ float bfr(float f) { return (float)(__bf16)f; }
__device__ __forceinline__ float lo2f(unsigned u) { return __builtin_bit_cast(float, u << 16); }
__device__ __forceinline__ float hi2f(unsigned u) { return __builtin_bit_cast(float, u & 0xffff0000u); }
__device__ __forceinline__ unsigned perm_lo(unsigned a, unsigned b) { return __builtin_amdgcn_perm(b, a, 0x05040100u); }
__device__ __forceinline__ unsigned perm_hi(unsigned a, unsigned b) { return __builtin_amdgcn_perm(b, a, 0x07060302u); }

template <int CTRL> __device__ __forceinline__ float dpp(float x) {
  return __builtin_bit_cast(float, __builtin_amdgcn_mov_dpp(__builtin_bit_cast(int, x), CTRL, 0xf, 0xf, true));
}
constexpr int XOR1 = 0xB1, XOR2 = 0x4E, ROR8 = 0x128, ROR4 = 0x124;
__device__ __forceinline__ float x16_max(float x) {
  auto s = __builtin_amdgcn_permlane16_swap(__float_as_uint(x), __float_as_uint(x), false, false);
  return __builtin_fmaxf(__uint_as_float(s[0]), __uint_as_float(s[1]));
}
__device__ __forceinline__ float x32_max(float x) {
  auto s = __builtin_amdgcn_permlane32_swap(__float_as_uint(x), __float_as_uint(x), false, false);
  return __builtin_fmaxf(__uint_as_float(s[0]), __uint_as_float(s[1]));
}
__device__ __forceinline__ float x16_sum(float x) {
  auto s = __builtin_amdgcn_permlane16_swap(__float_as_uint(x), __float_as_uint(x), false, false);
  return __uint_as_float(s[0]) + __uint_as_float(s[1]);
}
__device__ __forceinline__ float x32_sum(float x) {
  auto s = __builtin_amdgcn_permlane32_swap(__float_as_uint(x), __float_as_uint(x), false, false);
  return __uint_as_float(s[0]) + __uint_as_float(s[1]);
}
__device__ __forceinline__ float wave_sum(float x) {
  x += dpp<XOR1>(x); x += dpp<XOR2>(x); x += dpp<ROR4>(x); x += dpp<ROR8>(x);
  return x32_sum(x16_sum(x));
}
__device__ __forceinline__ f32x4 mfma32(v4u a, v4u b, f32x4 c) {
  return __builtin_amdgcn_mfma_f32_16x16x32_bf16(__builtin_bit_cast(bf16x8, a), __builtin_bit_cast(bf16x8, b), c, 0, 0, 0);
}
__device__ __forceinline__ f32x4 mfma16(v2u a, v2u b, f32x4 c) {
  return __builtin_amdgcn_mfma_f32_16x16x16bf16_1k(__builtin_bit_cast(s16x4, a), __builtin_bit_cast(s16x4, b), c, 0, 0, 0);
}
#define PIN() __builtin_amdgcn_sched_barrier(0)
#define BAR() do { PIN(); asm volatile("s_waitcnt lgkmcnt(0)" ::: "memory"); __builtin_amdgcn_s_barrier(); asm volatile("" ::: "memory"); PIN(); } while (0)
#define KEEP(x) asm volatile("" : "+v"(x))
#define STAMP(i)
#define STAMP_INIT()
#define STAMP_DUMP()

template <typename T> __device__ __forceinline__ T ldg(const u16* p) { return *(const GAS T*)p; }
template <typename T> __device__ __forceinline__ T ldnt(const u16* p) { return __builtin_nontemporal_load((const GAS T*)p); }

constexpr int L_CNT = 0;
constexpr int L_ST = 16;
constexpr int L_RQ = 32;
constexpr int L_RH = L_RQ + 1024;
constexpr int L_QS = L_RH + 1024;
constexpr int L_HB = L_QS + 2048;
constexpr int L_MO = L_HB + 32;
constexpr int L_ML = L_MO + 2048;
constexpr int L_OB = L_ML + 32;
constexpr int L_QB = L_OB + 64;
constexpr int L_FR = L_QB + 256;
constexpr int L_KS = L_FR + 32;
constexpr int L_END = L_KS + 8192;
enum { C_Q = 0, C_QG, C_QRDY, C_ISS, C_ATT, C_PUB2, C_H, C_HRDY, C_O, C_PUB3, C_FIN, C_ST, C_QPUB };

__device__ __forceinline__ unsigned arrive(unsigned addr, int lane) {
  unsigned old = 0;
  if (lane == 0)
    asm volatile("s_waitcnt lgkmcnt(0)\n\tds_add_rtn_u32 %0, %1, %2\n\ts_waitcnt lgkmcnt(0)" : "=v"(old) : "v"(addr), "v"(1u) : "memory");
  return __builtin_amdgcn_readfirstlane(old);
}
__device__ __forceinline__ void signal(unsigned addr, int lane) {
  if (lane == 0) asm volatile("s_waitcnt lgkmcnt(0)\n\tds_add_u32 %0, %1" :: "v"(addr), "v"(1u) : "memory");
}
template <int SLEEP> __device__ __forceinline__ void await(unsigned addr, unsigned n) {
#pragma clang loop unroll(disable)
  for (unsigned s = 0; s < LSPIN_CAP; ++s) {
    unsigned v;
    asm volatile("ds_read_b32 %0, %1\n\ts_waitcnt lgkmcnt(0)" : "=v"(v) : "v"(addr) : "memory");
    if ((unsigned)__builtin_amdgcn_readfirstlane(v) >= n) break;
    if (SLEEP) __builtin_amdgcn_s_sleep(SLEEP);
  }
}

__device__ __forceinline__ v4u tr8x4(const v2u (&W)[8], int i) {
  v4u f;
#pragma unroll
  for (int jj = 0; jj < 4; ++jj)
    f[jj] = (i & 1) ? perm_hi(W[2 * jj][i >> 1], W[2 * jj + 1][i >> 1]) : perm_lo(W[2 * jj][i >> 1], W[2 * jj + 1][i >> 1]);
  return f;
}
__device__ __forceinline__ unsigned tagbad(v4u v, unsigned T) { return (v[1] ^ T) | (v[3] ^ T); }

__global__ void __launch_bounds__(512, 2)
fused_kernel(const u16* __restrict__ x, const u16* __restrict__ kcache, const u16* __restrict__ vcache,
             const u16* __restrict__ wq, const u16* __restrict__ wo, const u16* __restrict__ w1,
             const u16* __restrict__ b1, const u16* __restrict__ w2, const u16* __restrict__ b2,
             const u16* __restrict__ gw, u16* __restrict__ out, unsigned char* ws) {
  __shared__ __attribute__((aligned(16))) float lds[L_END];
  asm volatile("" :: "s"(x), "s"(kcache), "s"(vcache), "s"(wq), "s"(wo), "s"(w1), "s"(b1), "s"(w2), "s"(b2), "s"(gw), "s"(out), "s"(ws));
  const int tid = threadIdx.x, lane = tid & 63, w = __builtin_amdgcn_readfirstlane(tid >> 6);
  const int g = lane >> 4, c = lane & 15;
  const int bid = blockIdx.x, hk = bid & 7, idx = bid >> 3;
  const int ab = idx >> 3, as = idx & 7;
  const int kgl = idx >> 1, cg = idx & 1, kg = hk * 16 + kgl;
  const __amdgpu_buffer_rsrc_t rs = __builtin_amdgcn_make_buffer_rsrc((void*)ws, 0, (int)WS_END, 0x00020000);
  const unsigned cn = (unsigned)(size_t)(LAS float*)(lds + L_CNT);
#define CN(i) (cn + 4u * (i))
  STAMP_INIT();
  STAMP(0);

  if (w >= 4) {
    const int aw = w - 4;
    BAR();
    unsigned tt;
    {
      const unsigned char* tp = ws;
      tp = lane == 0 ? (const unsigned char*)(out + (bid >> 6) * DM) : tp;
      tp = lane == 1 ? ws + WS_P3 : tp;
      tp = lane == 2 ? ws + WS_END - 64 : tp;
      tp = lane == 3 ? (const unsigned char*)b2 : tp;
      tp = lane == 4 ? (const unsigned char*)(wo + (size_t)(32 * kg) * DM) : tp;
      tp = lane == 5 ? (const unsigned char*)(w2 + (size_t)(224 * hk) * DM) : tp;
      tp = lane == 6 ? (const unsigned char*)b1 : tp;
      tp = lane == 7 ? ws + WS_P2 : tp;
      tp = lane == 8 ? (const unsigned char*)(vcache + ((size_t)(ab * 8 + hk) * SK + (SK - WINDOW) + 128 * as + 32 * aw) * DH) : tp;
      tp = lane == 9 ? (const unsigned char*)(wo + (size_t)(32 * kg + 31) * DM + 511) - 2 : tp;
      tp = lane == 10 ? ws + WS_HP : tp;
      tp = lane == 11 ? ws + WS_ML : tp;
      tt = *(const GAS unsigned*)tp;
    }
    PIN();
    if (ADLY) __builtin_amdgcn_s_sleep(ADLY);
    v4u kr[8], vf[8];
    {
      const size_t kvoff = ((size_t)(ab * 8 + hk) * SK + (SK - WINDOW) + 128 * as + 32 * aw) * DH;
      const u16* kp = kcache + kvoff + 8 * lane;
      const u16* vp = vcache + kvoff + 4 * g * DH + 8 * c;
#pragma unroll
      for (int i = 0; i < 8; ++i) kr[i] = ldnt<v4u>(kp + i * 512);
#pragma unroll
      for (int j = 0; j < 8; ++j) vf[j] = ldnt<v4u>(vp + (16 * (j >> 2) + (j & 3)) * DH);
    }
    PIN();
    SETPRIO(2);
    const unsigned T = (unsigned)__builtin_amdgcn_readlane(tt, 63) + 1u;
    STAMP(1);
    v4u kf[8];
    {
      unsigned char* ks_ = (unsigned char*)(lds + L_KS) + aw * 8192;
#pragma unroll
      for (int i = 0; i < 8; ++i) { const int row = 4 * i + g; *(v4u*)(ks_ + row * 256 + ((c ^ (row & 15)) << 4)) = kr[i]; }
      asm volatile("s_waitcnt lgkmcnt(0)" ::: "memory");
#pragma unroll
      for (int t = 0; t < 2; ++t)
#pragma unroll
        for (int ks = 0; ks < 4; ++ks) kf[t * 4 + ks] = *(const v4u*)(ks_ + (16 * t + c) * 256 + (((4 * ks + g) ^ c) << 4));
#pragma unroll
      for (int i = 0; i < 8; ++i) KEEP(kf[i]);
    }
    PIN();
    asm volatile("s_waitcnt vmcnt(%0)" :: "n"(QGATE) : "memory");
    signal(CN(C_ISS), lane);
    STAMP(2);
    await<0>(CN(C_QRDY), 1u);
    STAMP(3);
    v4u pf;
    {
      f32x4 s0 = {0.f, 0.f, 0.f, 0.f}, s1 = {0.f, 0.f, 0.f, 0.f};
      const u16* qb = (const u16*)(lds + L_QB) + (c >> 2) * 128 + 8 * g;
      v4u qf[4];
#pragma unroll
      for (int ks = 0; ks < 4; ++ks) qf[ks] = *(const v4u*)(qb + 32 * ks);
#pragma unroll
      for (int ks = 0; ks < 4; ++ks) {
        s0 = mfma32(kf[ks], qf[ks], s0);
        s1 = mfma32(kf[4 + ks], qf[ks], s1);
      }
      float m = __builtin_fmaxf(__builtin_fmaxf(__builtin_fmaxf(s0[0], s0[1]), __builtin_fmaxf(s0[2], s0[3])),
                                __builtin_fmaxf(__builtin_fmaxf(s1[0], s1[1]), __builtin_fmaxf(s1[2], s1[3])));
      m = x32_max(x16_max(m));
      float p[8];
#pragma unroll
      for (int j = 0; j < 4; ++j) { p[j] = __builtin_amdgcn_exp2f((s0[j] - m) * SCL2); p[4 + j] = __builtin_amdgcn_exp2f((s1[j] - m) * SCL2); }
      pf = v4u{pack2(p[0], p[1]), pack2(p[2], p[3]), pack2(p[4], p[5]), pack2(p[6], p[7])};
      float l = ((p[0] + p[1]) + (p[2] + p[3])) + ((p[4] + p[5]) + (p[6] + p[7]));
      l = x32_sum(x16_sum(l));
      if (g == 0 && (c & 3) == 0) *(f32x2*)(lds + L_ML + (aw * 4 + (c >> 2)) * 2) = f32x2{m * SCL2, l};
      KEEP(pf);
    }
    PIN();
    STAMP(4);
    {
      v4u vt[8];
#pragma unroll
      for (int i = 0; i < 8; ++i)
#pragma unroll
        for (int jj = 0; jj < 4; ++jj)
          vt[i][jj] = (i & 1) ? perm_hi(vf[2 * jj][i >> 1], vf[2 * jj + 1][i >> 1]) : perm_lo(vf[2 * jj][i >> 1], vf[2 * jj + 1][i >> 1]);
      f32x4 o[8];
#pragma unroll
      for (int i = 0; i < 8; ++i) o[i] = mfma32(pf, vt[i], f32x4{0.f, 0.f, 0.f, 0.f});
#pragma unroll
      for (int i = 0; i < 8; ++i) KEEP(o[i]);
      float* mo = lds + L_MO + (aw * 4 + g) * 128 + 8 * c;
      *(f32x4*)(mo) = f32x4{o[0][0], o[1][0], o[2][0], o[3][0]};
      *(f32x4*)(mo + 4) = f32x4{o[4][0], o[5][0], o[6][0], o[7][0]};
    }
    if (arrive(CN(C_ATT), lane) != 3u) await<0>(CN(C_ATT), 4u);
    STAMP(5);
    {
      f32x2 ml[4], ov[4];
#pragma unroll
      for (int u = 0; u < 4; ++u) { ml[u] = *(const f32x2*)(lds + L_ML + (u * 4 + aw) * 2); ov[u] = *(const f32x2*)(lds + L_MO + (u * 4 + aw) * 128 + 2 * lane); }
      const float M = __builtin_fmaxf(__builtin_fmaxf(ml[0][0], ml[1][0]), __builtin_fmaxf(ml[2][0], ml[3][0]));
      float L = 0.f, o0 = 0.f, o1 = 0.f;
#pragma unroll
      for (int u = 0; u < 4; ++u) {
        const float f = __builtin_amdgcn_exp2f(ml[u][0] - M);
        L = fmaf(f, ml[u][1], L); o0 = fmaf(f, ov[u][0], o0); o1 = fmaf(f, ov[u][1], o1);
      }
      const int hg = 4 * hk + aw;
      const v4u pv = {__float_as_uint(o0), T, __float_as_uint(o1), T};
      __builtin_amdgcn_raw_buffer_store_b128(pv, rs, WS_P2 + (((hg * 4 + (lane >> 4)) * 4 + ab) * 8 + as) * 256 + (lane & 15) * 16, 0, AUX_SC1);
      if (lane == 0) {
        const v4u mv = {__float_as_uint(M), T, __float_as_uint(L), T};
        __builtin_amdgcn_raw_buffer_store_b128(mv, rs, WS_ML + ((hg * 4 + ab) * 8 + as) * 16, 0, AUX_SC1);
      }
    }
    PIN();
    signal(CN(C_PUB2), lane);
    STAMP(6);
    const int ob = bid >> 6, ocw = (bid >> 3) & 7, om = (bid >> 1) & 3, oh = bid & 1;
    const int col = 64 * ocw + 4 * (4 * om + (lane & 3)) + 2 * oh;
    const unsigned b2v = ldg<unsigned>(b2 + col);
    const unsigned off = WS_P3 + ((aw * 32 + (lane >> 2)) * 8 + ocw) * 2048 + oh * 1024 + (ob * 16 + 4 * om + (lane & 3)) * 16;
    await<2>(CN(C_PUB3), 4u);
    __builtin_amdgcn_s_sleep(DLY3);
    STAMP(8);
    v4u pv0 = __builtin_amdgcn_raw_buffer_load_b128(rs, off, 0, AUX_SC1);
    v4u pv1 = __builtin_amdgcn_raw_buffer_load_b128(rs, off + 16 * 8 * 2048, 0, AUX_SC1);
#pragma clang loop unroll(disable)
    for (unsigned spins = 0; spins < SPIN_CAP; ++spins) {
      const unsigned bad0 = tagbad(pv0, T), bad1 = tagbad(pv1, T);
      if (__all((bad0 | bad1) == 0)) break;
      asm volatile("" ::: "memory");
      if (bad0) pv0 = __builtin_amdgcn_raw_buffer_load_b128(rs, off, 0, AUX_SC1);
      if (bad1) pv1 = __builtin_amdgcn_raw_buffer_load_b128(rs, off + 16 * 8 * 2048, 0, AUX_SC1);
    }
    STAMP(9);
    float sa = __uint_as_float(pv0[0]) + __uint_as_float(pv1[0]), sb = __uint_as_float(pv0[2]) + __uint_as_float(pv1[2]);
    sa += dpp<ROR4>(sa); sb += dpp<ROR4>(sb); sa += dpp<ROR8>(sa); sb += dpp<ROR8>(sb);
    sa = x16_sum(sa); sb = x16_sum(sb); sa = x32_sum(sa); sb = x32_sum(sb);
    if (lane < 4) *(f32x2*)(lds + L_FR + aw * 8 + 2 * lane) = f32x2{sa, sb};
    if (arrive(CN(C_FIN), lane) == 3u) {
      if (lane < 4) {
        const float omg = lds[L_ST + 4 * ob + 3];
        f32x2 t = *(const f32x2*)(lds + L_FR + 2 * lane);
#pragma unroll
        for (int u = 1; u < 4; ++u) t += *(const f32x2*)(lds + L_FR + u * 8 + 2 * lane);
        *(GAS unsigned*)(out + ob * DM + col) = pack2(fmaf(omg, lo2f(b2v), t[0]), fmaf(omg, hi2f(b2v), t[1]));
      }
      if (bid == 0 && lane == 0) __hip_atomic_store((gu32*)ws, T, __ATOMIC_RELAXED, __HIP_MEMORY_SCOPE_AGENT);
    }
  } else {
    const int pw = w;
    SETPRIO(3);
    const int kq = idx & 3, cq = idx >> 2;
    const bool has_ht = idx < 14;
    const int th = 14 * hk + idx;
    const int qrow = 128 * kq + 32 * pw + 8 * g, hrow = 128 * (th & 3) + 32 * pw + 8 * g;
    v2u wv[8], w1v[8];
    {
      const u16* p = wq + (size_t)qrow * NQ + hk * 512 + 64 * cq + 4 * c;
#pragma unroll
      for (int j = 0; j < 8; ++j) wv[j] = ldnt<v2u>(p + j * NQ);
    }
    const v4u xa = ldg<v4u>(x + (c >> 2) * DM + qrow);
    const unsigned ev = __builtin_amdgcn_raw_buffer_load_b32(rs, 0, 0, 0);
    PIN();
    const v4u xs = ldg<v4u>(x + pw * DM + 8 * lane);
    const v4u gwv = ldg<v4u>(gw + 8 * lane);
    const v4u xh = ldg<v4u>(x + (c >> 2) * DM + hrow);
    {
      const __amdgpu_buffer_rsrc_t rw1 = __builtin_amdgcn_make_buffer_rsrc((void*)w1, 0, has_ht ? DM * FF * 2 : 0, 0x00020000);
      const int o = (hrow * FF + 64 * (th >> 2) + 4 * c) * 2;
#pragma unroll
      for (int j = 0; j < 8; ++j) w1v[j] = __builtin_amdgcn_raw_buffer_load_b64(rw1, o + j * FF * 2, 0, AUX_NT);
    }
    if (tid < 16) ((LAS unsigned*)(lds + L_CNT))[tid] = 0u;
    BAR();
    PIN();
    unsigned T;
    {
      f32x4 d[4];
#pragma unroll
      for (int i = 0; i < 4; ++i) d[i] = mfma32(xa, tr8x4(wv, i), f32x4{0.f, 0.f, 0.f, 0.f});
      *(f32x4*)(lds + L_RQ + (pw * 64 + lane) * 4) = f32x4{d[0][0], d[1][0], d[2][0], d[3][0]};
      T = (unsigned)__builtin_amdgcn_readfirstlane(ev) + 1u;
      if (arrive(CN(C_Q), lane) == 3u) {
        f32x4 a = *(const f32x4*)(lds + L_RQ + lane * 4);
#pragma unroll
        for (int p = 1; p < 4; ++p) a += *(const f32x4*)(lds + L_RQ + (p * 64 + lane) * 4);
        const unsigned off = WS_QP + ((hk * 8 + cq) * 4 + kq) * 2048 + lane * 16;
        __builtin_amdgcn_raw_buffer_store_b128(v4u{__float_as_uint(a[0]), T, __float_as_uint(a[1]), T}, rs, off, 0, AUX_SC1);
        __builtin_amdgcn_raw_buffer_store_b128(v4u{__float_as_uint(a[2]), T, __float_as_uint(a[3]), T}, rs, off + 1024, 0, AUX_SC1);
      }
    }
    PIN();
    STAMP(1);
    {
      float xv[8], gv[8];
#pragma unroll
      for (int k = 0; k < 4; ++k) { xv[2 * k] = lo2f(xs[k]); xv[2 * k + 1] = hi2f(xs[k]); gv[2 * k] = lo2f(gwv[k]); gv[2 * k + 1] = hi2f(gwv[k]); }
      float s = 0.f, gs = 0.f;
#pragma unroll
      for (int k = 0; k < 8; ++k) { s += xv[k]; gs += bfr(xv[k] * gv[k]); }
      s = wave_sum(s); gs = wave_sum(gs);
      const float mean = s * (1.0f / DM);
      float vs = 0.f;
#pragma unroll
      for (int k = 0; k < 8; ++k) { const float d = xv[k] - mean; vs = fmaf(d, d, vs); }
      vs = wave_sum(vs);
      if (lane == 0) {
        const float var_b = bfr(vs * (1.0f / DM));
        const float den = bfr(sqrtf(bfr(var_b + 1e-5f)));
        const float gt = bfr(1.0f / (1.0f + __expf(-bfr(gs))));
        *(f32x4*)(lds + L_ST + 4 * pw) = f32x4{bfr(mean), den, gt, bfr(1.0f - gt)};
      }
      if (arrive(CN(C_ST), lane) != 3u) await<0>(CN(C_ST), 4u);
    }
    if (has_ht) {
      const f32x4 st = *(const f32x4*)(lds + L_ST + 4 * (c >> 2));
      v4u ya;
#pragma unroll
      for (int k = 0; k < 4; ++k) ya[k] = pack2(bfr(lo2f(xh[k]) - st[0]) / st[1], bfr(hi2f(xh[k]) - st[0]) / st[1]);
      f32x4 d[4];
#pragma unroll
      for (int i = 0; i < 4; ++i) d[i] = mfma32(ya, tr8x4(w1v, i), f32x4{0.f, 0.f, 0.f, 0.f});
      *(f32x4*)(lds + L_RH + (pw * 64 + lane) * 4) = f32x4{d[0][0], d[1][0], d[2][0], d[3][0]};
      if (arrive(CN(C_H), lane) == 3u) {
        f32x4 a = *(const f32x4*)(lds + L_RH + lane * 4);
#pragma unroll
        for (int p = 1; p < 4; ++p) a += *(const f32x4*)(lds + L_RH + (p * 64 + lane) * 4);
        const unsigned off = WS_HP + th * 2048 + lane * 16;
        __builtin_amdgcn_raw_buffer_store_b128(v4u{__float_as_uint(a[0]), T, __float_as_uint(a[1]), T}, rs, off, 0, AUX_SC1);
        __builtin_amdgcn_raw_buffer_store_b128(v4u{__float_as_uint(a[2]), T, __float_as_uint(a[3]), T}, rs, off + 1024, 0, AUX_SC1);
      }
    }
    f32x4 stg = *(const f32x4*)(lds + L_ST + 4 * g);
    KEEP(stg);
    PIN();
    STAMP(2);
    await<2>(CN(C_ISS), ISSN);
    const bool has_mlp = kgl < 14;
    const int fs = 14 * hk + kgl, f0 = 16 * fs;
    v2u wov[8], w2v[4];
    {
      const unsigned qoff = WS_QP + ((hk * 8 + (lane >> 5)) * 4 + pw) * 2048 + ((lane >> 4) & 1) * 1024 + (16 * ab + c) * 16;
      v4u qv[4];
#pragma unroll
      for (int i = 0; i < 4; ++i) qv[i] = __builtin_amdgcn_raw_buffer_load_b128(rs, qoff + i * 16384, 0, AUX_SC1);
      PIN();
      if (LATE_FIRST) {
        const __amdgpu_buffer_rsrc_t rw2 = __builtin_amdgcn_make_buffer_rsrc((void*)w2, 0, has_mlp ? DM * FF * 2 : 0, 0x00020000);
        const u16* p = wo + (size_t)(32 * kg + 8 * g) * DM + 256 * cg + 64 * pw + 4 * c;
        const int o2 = ((f0 + 4 * g) * DM + 256 * cg + 64 * pw + 4 * c) * 2;
#pragma unroll
        for (int j = 0; j < 8; ++j) wov[j] = ldnt<v2u>(p + j * DM);
#pragma unroll
        for (int j = 0; j < 4; ++j) w2v[j] = __builtin_amdgcn_raw_buffer_load_b64(rw2, o2 + j * DM * 2, 0, AUX_NT);
      }
      PIN();
      unsigned bad0 = tagbad(qv[0], T) | tagbad(qv[1], T) | tagbad(qv[2], T) | tagbad(qv[3], T);
      if (!__all(bad0 == 0)) {
#pragma clang loop unroll(disable)
        for (unsigned spins = 0; spins < SPIN_CAP; ++spins) {
          unsigned bad[4];
#pragma unroll
          for (int i = 0; i < 4; ++i) bad[i] = tagbad(qv[i], T);
          if (__all((bad[0] | bad[1] | bad[2] | bad[3]) == 0)) break;
          asm volatile("" ::: "memory");
#pragma unroll
          for (int i = 0; i < 4; ++i) if (bad[i]) qv[i] = __builtin_amdgcn_raw_buffer_load_b128(rs, qoff + i * 16384, 0, AUX_SC1);
        }
      }
      float* qs = lds + L_QS + (pw * 64 + lane) * 8;
      *(v4u*)(qs) = v4u{qv[0][0], qv[0][2], qv[1][0], qv[1][2]};
      *(v4u*)(qs + 4) = v4u{qv[2][0], qv[2][2], qv[3][0], qv[3][2]};
      if (arrive(CN(C_QG), lane) == 3u) {
        f32x4 a0 = *(const f32x4*)(lds + L_QS + lane * 8), a1 = *(const f32x4*)(lds + L_QS + lane * 8 + 4);
#pragma unroll
        for (int p = 1; p < 4; ++p) { a0 += *(const f32x4*)(lds + L_QS + (p * 64 + lane) * 8); a1 += *(const f32x4*)(lds + L_QS + (p * 64 + lane) * 8 + 4); }
        unsigned* qb = (unsigned*)(lds + L_QB) + 32 * (lane >> 5) + 2 * c + ((lane >> 4) & 1);
        qb[0] = pack2(a0[0], a0[1]); qb[64] = pack2(a0[2], a0[3]); qb[128] = pack2(a1[0], a1[1]); qb[192] = pack2(a1[2], a1[3]);
        signal(CN(C_QRDY), lane);
      }
    }
    STAMP(3);
    if (!LATE_FIRST) {
      const __amdgpu_buffer_rsrc_t rw2 = __builtin_amdgcn_make_buffer_rsrc((void*)w2, 0, has_mlp ? DM * FF * 2 : 0, 0x00020000);
      const u16* p = wo + (size_t)(32 * kg + 8 * g) * DM + 256 * cg + 64 * pw + 4 * c;
      const int o2 = ((f0 + 4 * g) * DM + 256 * cg + 64 * pw + 4 * c) * 2;
#pragma unroll
      for (int j = 0; j < 8; ++j) wov[j] = ldnt<v2u>(p + j * DM);
#pragma unroll
      for (int j = 0; j < 4; ++j) w2v[j] = __builtin_amdgcn_raw_buffer_load_b64(rw2, o2 + j * DM * 2, 0, AUX_NT);
    }
    PIN();
    SETPRIO(3);
    STAMP(4);
    if (pw == 3) {
      float hv = 0.f;
      if (has_mlp) {
        const float bv = lo2f((unsigned)ldg<u16>(b1 + f0 + 4 * (c & 3) + g));
        const unsigned hoff = WS_HP + ((fs >> 2) * 4 + g) * 2048 + (16 * (c >> 2) + 4 * (fs & 3) + (c & 3)) * 16;
        v4u h0 = __builtin_amdgcn_raw_buffer_load_b128(rs, hoff, 0, AUX_SC1);
        v4u h1 = __builtin_amdgcn_raw_buffer_load_b128(rs, hoff + 1024, 0, AUX_SC1);
#pragma clang loop unroll(disable)
        for (unsigned spins = 0; spins < SPIN_CAP; ++spins) {
          const unsigned bad = tagbad(h0, T) | tagbad(h1, T);
          if (__all(bad == 0)) break;
          asm volatile("" ::: "memory");
          if (bad) {
            h0 = __builtin_amdgcn_raw_buffer_load_b128(rs, hoff, 0, AUX_SC1);
            h1 = __builtin_amdgcn_raw_buffer_load_b128(rs, hoff + 1024, 0, AUX_SC1);
          }
        }
        float a[4] = {__uint_as_float(h0[0]), __uint_as_float(h0[2]), __uint_as_float(h1[0]), __uint_as_float(h1[2])};
#pragma unroll
        for (int i = 0; i < 4; ++i) a[i] = x32_sum(x16_sum(a[i]));
        const float av = g == 0 ? a[0] : g == 1 ? a[1] : g == 2 ? a[2] : a[3];
        const float u = bfr(bfr(av) + bv);
        const float inner = 0.7978845608028654f * (u + 0.044715f * u * u * u);
        hv = 0.5f * u * (1.0f + tanhf(inner));
      }
      ((u16*)(lds + L_HB))[(c >> 2) * 16 + 4 * (c & 3) + g] = (u16)pack2(hv, 0.f);
      signal(CN(C_HRDY), lane);
    }
    v4u wof[4];
    float accm[4];
    {
      v2u w2f[4];
#pragma unroll
      for (int i = 0; i < 4; ++i) {
        wof[i] = tr8x4(wov, i);
#pragma unroll
        for (int jj = 0; jj < 2; ++jj)
          w2f[i][jj] = (i & 1) ? perm_hi(w2v[2 * jj][i >> 1], w2v[2 * jj + 1][i >> 1]) : perm_lo(w2v[2 * jj][i >> 1], w2v[2 * jj + 1][i >> 1]);
        KEEP(wof[i]);
      }
      await<1>(CN(C_HRDY), 1u);
      const v2u ha = *(const v2u*)((const u16*)(lds + L_HB) + (c >> 2) * 16 + 4 * g);
#pragma unroll
      for (int i = 0; i < 4; ++i) {
        const f32x4 e = mfma16(ha, w2f[i], f32x4{0.f, 0.f, 0.f, 0.f});
        accm[i] = stg[3] * e[0];
        KEEP(accm[i]);
      }
    }
    PIN();
    STAMP(5);
    const int h2 = 4 * hk + (kgl >> 2);
    const unsigned p2off = WS_P2 + (((h2 * 4 + (kgl & 3)) * 4 + pw) * 8 + (lane >> 3)) * 256 + (lane & 7) * 32;
    const unsigned mloff = WS_ML + ((h2 * 4 + pw) * 8 + (lane >> 3)) * 16;
    await<1>(CN(C_PUB2), 4u);
    __builtin_amdgcn_s_sleep(DLY2);
    STAMP(6);
    v4u a0 = __builtin_amdgcn_raw_buffer_load_b128(rs, p2off, 0, AUX_SC1);
    v4u a1 = __builtin_amdgcn_raw_buffer_load_b128(rs, p2off + 16, 0, AUX_SC1);
    v4u am = __builtin_amdgcn_raw_buffer_load_b128(rs, mloff, 0, AUX_SC1);
#pragma clang loop unroll(disable)
    for (unsigned spins = 0; spins < SPIN_CAP; ++spins) {
      const unsigned bad = tagbad(a0, T) | tagbad(a1, T) | tagbad(am, T);
      if (__all(bad == 0)) break;
      asm volatile("" ::: "memory");
      if (bad) {
        a0 = __builtin_amdgcn_raw_buffer_load_b128(rs, p2off, 0, AUX_SC1);
        a1 = __builtin_amdgcn_raw_buffer_load_b128(rs, p2off + 16, 0, AUX_SC1);
        am = __builtin_amdgcn_raw_buffer_load_b128(rs, mloff, 0, AUX_SC1);
      }
    }
    STAMP(7);
    {
      const float m = __uint_as_float(am[0]), l = __uint_as_float(am[2]);
      float M = __builtin_fmaxf(m, dpp<ROR8>(m)); M = x32_max(x16_max(M));
      const float f = __builtin_amdgcn_exp2f(m - M);
      float v[5] = {f * __uint_as_float(a0[0]), f * __uint_as_float(a0[2]), f * __uint_as_float(a1[0]), f * __uint_as_float(a1[2]), f * l};
#pragma unroll
      for (int i = 0; i < 5; ++i) v[i] += dpp<ROR8>(v[i]);
#pragma unroll
      for (int i = 0; i < 5; ++i) v[i] = x16_sum(v[i]);
#pragma unroll
      for (int i = 0; i < 5; ++i) v[i] = x32_sum(v[i]);
      const float inv = __builtin_amdgcn_rcpf(v[4]);
      if (lane < 8) *(v2u*)((u16*)(lds + L_OB) + pw * 32 + 4 * lane) = v2u{pack2(v[0] * inv, v[1] * inv), pack2(v[2] * inv, v[3] * inv)};
    }
    if (arrive(CN(C_O), lane) != 3u) await<0>(CN(C_O), 4u);
    {
      const v4u oa = *(const v4u*)((const u16*)(lds + L_OB) + (c >> 2) * 32 + 8 * g);
      f32x4 d[4];
#pragma unroll
      for (int i = 0; i < 4; ++i) d[i] = mfma32(oa, wof[i], f32x4{0.f, 0.f, 0.f, 0.f});
#pragma unroll
      for (int i = 0; i < 4; ++i) KEEP(d[i]);
      const unsigned off = WS_P3 + ((kg * 2 + cg) * 4 + pw) * 2048 + lane * 16;
      const v4u s0 = {__float_as_uint(fmaf(stg[2], d[0][0], accm[0])), T, __float_as_uint(fmaf(stg[2], d[1][0], accm[1])), T};
      const v4u s1 = {__float_as_uint(fmaf(stg[2], d[2][0], accm[2])), T, __float_as_uint(fmaf(stg[2], d[3][0], accm[3])), T};
      __builtin_amdgcn_raw_buffer_store_b128(s0, rs, off, 0, AUX_SC1);
      __builtin_amdgcn_raw_buffer_store_b128(s1, rs, off + 1024, 0, AUX_SC1);
    }
    PIN();
    signal(CN(C_PUB3), lane);
    STAMP(8);
  }
  STAMP_DUMP();
}

extern "C" void kernel_launch(void* const* d_in, const int* in_sizes, int n_in,
                              void* d_out, int out_size, void* d_ws, size_t ws_size,
                              hipStream_t stream) {
  (void)in_sizes; (void)n_in; (void)out_size; (void)ws_size;
  fused_kernel<<<256, 512, 0, stream>>>(
      (const u16*)d_in[0], (const u16*)d_in[1], (const u16*)d_in[2], (const u16*)d_in[3], (const u16*)d_in[4],
      (const u16*)d_in[6], (const u16*)d_in[7], (const u16*)d_in[8], (const u16*)d_in[9], (const u16*)d_in[10],
      (u16*)d_out, (unsigned char*)d_ws);
}
```

```cpp
#define QGATE 6
#define LATE_FIRST 0
#define CODEPF 1
#include <hip/hip_runtime.h>
#include <hip/hip_bf16.h>
#include <stdint.h>

#define GAS __attribute__((address_space(1)))
#define LAS __attribute__((address_space(3)))
typedef unsigned short u16;
typedef unsigned v4u __attribute__((ext_vector_type(4)));
typedef unsigned v2u __attribute__((ext_vector_type(2)));
typedef float f32x4 __attribute__((ext_vector_type(4)));
typedef float f32x2 __attribute__((ext_vector_type(2)));
typedef __bf16 bf16x8 __attribute__((ext_vector_type(8)));
typedef __bf16 bf16x2 __attribute__((ext_vector_type(2)));
typedef short s16x4 __attribute__((ext_vector_type(4)));
typedef GAS unsigned gu32;

constexpr int DM = 512, SK = 4096, DH = 128, WINDOW = 1024, FF = 1792, NQ = 4096;
constexpr unsigned WS_QP = 4096;
constexpr unsigned WS_HP = WS_QP + (512u << 10);
constexpr unsigned WS_P2 = WS_HP + (256u << 10);
constexpr unsigned WS_ML = WS_P2 + (1u << 20);
constexpr unsigned WS_P3 = WS_ML + 16384;
constexpr unsigned WS_END = WS_P3 + (2u << 20);
constexpr unsigned SPIN_CAP = 1u << 17;
constexpr unsigned LSPIN_CAP = 1u << 22;
constexpr int AUX_SC1 = 16, AUX_NT = 2;
constexpr float LOG2E = 1.4426950408889634f;
constexpr float SCL2 = 0.08838834764831845f * 1.4426950408889634f;

#define AWIN 6
#define ADLY 0
#define ISSN 4u
#define DLY1 16
#define DLY2 8
#define DLY3 10
#define SETPRIO(n) __builtin_amdgcn_s_setprio(n)

__device__ __forceinline__ unsigned pack2(float lo, float hi) {
  return __builtin_bit_cast(unsigned, __builtin_convertvector(f32x2{lo, hi}, bf16x2));
}
__device__ __forceinline__ float bfr(float f) { return (float)(__bf16)f; }
__device__ __forceinline__ float lo2f(unsigned u) { return __builtin_bit_cast(float, u << 16); }
__device__ __forceinline__ float hi2f(unsigned u) { return __builtin_bit_cast(float, u & 0xffff0000u); }
__device__ __forceinline__ unsigned perm_lo(unsigned a, unsigned b) { return __builtin_amdgcn_perm(b, a, 0x05040100u); }
__device__ __forceinline__ unsigned perm_hi(unsigned a, unsigned b) { return __builtin_amdgcn_perm(b, a, 0x07060302u); }

template <int CTRL> __device__ __forceinline__ float dpp(float x) {
  return __builtin_bit_cast(float, __builtin_amdgcn_mov_dpp(__builtin_bit_cast(int, x), CTRL, 0xf, 0xf, true));
}
constexpr int XOR1 = 0xB1, XOR2 = 0x4E, ROR8 = 0x128, ROR4 = 0x124;
__device__ __forceinline__ float x16_max(float x) {
  auto s = __builtin_amdgcn_permlane16_swap(__float_as_uint(x), __float_as_uint(x), false, false);
  return __builtin_fmaxf(__uint_as_float(s[0]), __uint_as_float(s[1]));
}
__device__ __forceinline__ float x32_max(float x) {
  auto s = __builtin_amdgcn_permlane32_swap(__float_as_uint(x), __float_as_uint(x), false, false);
  return __builtin_fmaxf(__uint_as_float(s[0]), __uint_as_float(s[1]));
}
__device__ __forceinline__ float x16_sum(float x) {
  auto s = __builtin_amdgcn_permlane16_swap(__float_as_uint(x), __float_as_uint(x), false, false);
  return __uint_as_float(s[0]) + __uint_as_float(s[1]);
}
__device__ __forceinline__ float x32_sum(float x) {
  auto s = __builtin_amdgcn_permlane32_swap(__float_as_uint(x), __float_as_uint(x), false, false);
  return __uint_as_float(s[0]) + __uint_as_float(s[1]);
}
__device__ __forceinline__ float wave_sum(float x) {
  x += dpp<XOR1>(x); x += dpp<XOR2>(x); x += dpp<ROR4>(x); x += dpp<ROR8>(x);
  return x32_sum(x16_sum(x));
}
__device__ __forceinline__ f32x4 mfma32(v4u a, v4u b, f32x4 c) {
  return __builtin_amdgcn_mfma_f32_16x16x32_bf16(__builtin_bit_cast(bf16x8, a), __builtin_bit_cast(bf16x8, b), c, 0, 0, 0);
}
__device__ __forceinline__ f32x4 mfma16(v2u a, v2u b, f32x4 c) {
  return __builtin_amdgcn_mfma_f32_16x16x16bf16_1k(__builtin_bit_cast(s16x4, a), __builtin_bit_cast(s16x4, b), c, 0, 0, 0);
}
#define PIN() __builtin_amdgcn_sched_barrier(0)
#define BAR() do { PIN(); asm volatile("s_waitcnt lgkmcnt(0)" ::: "memory"); __builtin_amdgcn_s_barrier(); asm volatile("" ::: "memory"); PIN(); } while (0)
#define KEEP(x) asm volatile("" : "+v"(x))
#define STAMP(i)
#define STAMP_INIT()
#define STAMP_DUMP()

template <typename T> __device__ __forceinline__ T ldg(const u16* p) { return *(const GAS T*)p; }
template <typename T> __device__ __forceinline__ T ldnt(const u16* p) { return __builtin_nontemporal_load((const GAS T*)p); }

constexpr int L_CNT = 0;
constexpr int L_ST = 16;
constexpr int L_RQ = 32;
constexpr int L_RH = L_RQ + 1024;
constexpr int L_QS = L_RH + 1024;
constexpr int L_HB = L_QS + 2048;
constexpr int L_MO = L_HB + 32;
constexpr int L_ML = L_MO + 2048;
constexpr int L_OB = L_ML + 32;
constexpr int L_QB = L_OB + 64;
constexpr int L_FR = L_QB + 256;
constexpr int L_KS = L_FR + 32;
constexpr int L_END = L_KS + 8192;
enum { C_Q = 0, C_QG, C_QRDY, C_ISS, C_ATT, C_PUB2, C_H, C_HRDY, C_O, C_PUB3, C_FIN, C_ST, C_QPUB };

__device__ __forceinline__ unsigned arrive(unsigned addr, int lane) {
  unsigned old = 0;
  if (lane == 0)
    asm volatile("s_waitcnt lgkmcnt(0)\n\tds_add_rtn_u32 %0, %1, %2\n\ts_waitcnt lgkmcnt(0)" : "=v"(old) : "v"(addr), "v"(1u) : "memory");
  return __builtin_amdgcn_readfirstlane(old);
}
__device__ __forceinline__ void signal(unsigned addr, int lane) {
  if (lane == 0) asm volatile("s_waitcnt lgkmcnt(0)\n\tds_add_u32 %0, %1" :: "v"(addr), "v"(1u) : "memory");
}
template <int SLEEP> __device__ __forceinline__ void await(unsigned addr, unsigned n) {
#pragma clang loop unroll(disable)
  for (unsigned s = 0; s < LSPIN_CAP; ++s) {
    unsigned v;
    asm volatile("ds_read_b32 %0, %1\n\ts_waitcnt lgkmcnt(0)" : "=v"(v) : "v"(addr) : "memory");
    if ((unsigned)__builtin_amdgcn_readfirstlane(v) >= n) break;
    if (SLEEP) __builtin_amdgcn_s_sleep(SLEEP);
  }
}

__device__ __forceinline__ v4u tr8x4(const v2u (&W)[8], int i) {
  v4u f;
#pragma unroll
  for (int jj = 0; jj < 4; ++jj)
    f[jj] = (i & 1) ? perm_hi(W[2 * jj][i >> 1], W[2 * jj + 1][i >> 1]) : perm_lo(W[2 * jj][i >> 1], W[2 * jj + 1][i >> 1]);
  return f;
}
__device__ __forceinline__ unsigned tagbad(v4u v, unsigned T) { return (v[1] ^ T) | (v[3] ^ T); }

__global__ void __launch_bounds__(512, 2)
fused_kernel(const u16* __restrict__ x, const u16* __restrict__ kcache, const u16* __restrict__ vcache,
             const u16* __restrict__ wq, const u16* __restrict__ wo, const u16* __restrict__ w1,
             const u16* __restrict__ b1, const u16* __restrict__ w2, const u16* __restrict__ b2,
             const u16* __restrict__ gw, u16* __restrict__ out, unsigned char* ws) {
  __shared__ __attribute__((aligned(16))) float lds[L_END];
  unsigned long long pc0;
  asm volatile("s_getpc_b64 %0" : "=s"(pc0));
  pc0 &= ~255ull;
  asm volatile("" :: "s"(x), "s"(kcache), "s"(vcache), "s"(wq), "s"(wo), "s"(w1), "s"(b1), "s"(w2), "s"(b2), "s"(gw), "s"(out), "s"(ws));
  const int tid = threadIdx.x, lane = tid & 63, w = __builtin_amdgcn_readfirstlane(tid >> 6);
  const int g = lane >> 4, c = lane & 15;
  const int bid = blockIdx.x, hk = bid & 7, idx = bid >> 3;
  const int ab = idx >> 3, as = idx & 7;
  const int kgl = idx >> 1, cg = idx & 1, kg = hk * 16 + kgl;
  const __amdgpu_buffer_rsrc_t rs = __builtin_amdgcn_make_buffer_rsrc((void*)ws, 0, (int)WS_END, 0x00020000);
  const unsigned cn = (unsigned)(size_t)(LAS float*)(lds + L_CNT);
#define CN(i) (cn + 4u * (i))
  STAMP_INIT();
  STAMP(0);

  if (w >= 4) {
    const int aw = w - 4;
    unsigned cp0 = 0, cp1 = 0;
    if (CODEPF && aw == 0) {
      cp0 = *(const GAS unsigned*)(pc0 + lane * 128);
      cp1 = *(const GAS unsigned*)(pc0 + 8192 + (lane < 17 ? lane : 0) * 128);
    }
    PIN();
    BAR();
    unsigned tt;
    {
      const unsigned char* tp = ws;
      tp = lane == 0 ? (const unsigned char*)(out + (bid >> 6) * DM) : tp;
      tp = lane == 1 ? ws + WS_P3 : tp;
      tp = lane == 2 ? ws + WS_END - 64 : tp;
      tp = lane == 3 ? (const unsigned char*)b2 : tp;
      tp = lane == 4 ? (const unsigned char*)(wo + (size_t)(32 * kg) * DM) : tp;
      tp = lane == 5 ? (const unsigned char*)(w2 + (size_t)(224 * hk) * DM) : tp;
      tp = lane == 6 ? (const unsigned char*)b1 : tp;
      tp = lane == 7 ? ws + WS_P2 : tp;
      tp = lane == 8 ? (const unsigned char*)(vcache + ((size_t)(ab * 8 + hk) * SK + (SK - WINDOW) + 128 * as + 32 * aw) * DH) : tp;
      tp = lane == 9 ? (const unsigned char*)(wo + (size_t)(32 * kg + 31) * DM + 511) - 2 : tp;
      tp = lane == 10 ? ws + WS_HP : tp;
      tp = lane == 11 ? ws + WS_ML : tp;
      tt = *(const GAS unsigned*)tp;
    }
    PIN();
    if (ADLY) __builtin_amdgcn_s_sleep(ADLY);
    v4u kr[8], vf[8];
    {
      const size_t kvoff = ((size_t)(ab * 8 + hk) * SK + (SK - WINDOW) + 128 * as + 32 * aw) * DH;
      const u16* kp = kcache + kvoff + 8 * lane;
      const u16* vp = vcache + kvoff + 4 * g * DH + 8 * c;
#pragma unroll
      for (int i = 0; i < 8; ++i) kr[i] = ldnt<v4u>(kp + i * 512);
#pragma unroll
      for (int j = 0; j < 8; ++j) vf[j] = ldnt<v4u>(vp + (16 * (j >> 2) + (j & 3)) * DH);
    }
    PIN();
    SETPRIO(2);
    asm volatile("" :: "v"(cp0), "v"(cp1));
    const unsigned T = (unsigned)__builtin_amdgcn_readlane(tt, 63) + 1u;
    STAMP(1);
    v4u kf[8];
    {
      unsigned char* ks_ = (unsigned char*)(lds + L_KS) + aw * 8192;
#pragma unroll
      for (int i = 0; i < 8; ++i) { const int row = 4 * i + g; *(v4u*)(ks_ + row * 256 + ((c ^ (row & 15)) << 4)) = kr[i]; }
      asm volatile("s_waitcnt lgkmcnt(0)" ::: "memory");
#pragma unroll
      for (int t = 0; t < 2; ++t)
#pragma unroll
        for (int ks = 0; ks < 4; ++ks) kf[t * 4 + ks] = *(const v4u*)(ks_ + (16 * t + c) * 256 + (((4 * ks + g) ^ c) << 4));
#pragma unroll
      for (int i = 0; i < 8; ++i) KEEP(kf[i]);
    }
    PIN();
    asm volatile("s_waitcnt vmcnt(%0)" :: "n"(QGATE) : "memory");
    signal(CN(C_ISS), lane);
    STAMP(2);
    await<0>(CN(C_QRDY), 1u);
    STAMP(3);
    v4u pf;
    {
      f32x4 s0 = {0.f, 0.f, 0.f, 0.f}, s1 = {0.f, 0.f, 0.f, 0.f};
      const u16* qb = (const u16*)(lds + L_QB) + (c >> 2) * 128 + 8 * g;
      v4u qf[4];
#pragma unroll
      for (int ks = 0; ks < 4; ++ks) qf[ks] = *(const v4u*)(qb + 32 * ks);
#pragma unroll
      for (int ks = 0; ks < 4; ++ks) {
        s0 = mfma32(kf[ks], qf[ks], s0);
        s1 = mfma32(kf[4 + ks], qf[ks], s1);
      }
      float m = __builtin_fmaxf(__builtin_fmaxf(__builtin_fmaxf(s0[0], s0[1]), __builtin_fmaxf(s0[2], s0[3])),
                                __builtin_fmaxf(__builtin_fmaxf(s1[0], s1[1]), __builtin_fmaxf(s1[2], s1[3])));
      m = x32_max(x16_max(m));
      float p[8];
#pragma unroll
      for (int j = 0; j < 4; ++j) { p[j] = __builtin_amdgcn_exp2f((s0[j] - m) * SCL2); p[4 + j] = __builtin_amdgcn_exp2f((s1[j] - m) * SCL2); }
      pf = v4u{pack2(p[0], p[1]), pack2(p[2], p[3]), pack2(p[4], p[5]), pack2(p[6], p[7])};
      float l = ((p[0] + p[1]) + (p[2] + p[3])) + ((p[4] + p[5]) + (p[6] + p[7]));
      l = x32_sum(x16_sum(l));
      if (g == 0 && (c & 3) == 0) *(f32x2*)(lds + L_ML + (aw * 4 + (c >> 2)) * 2) = f32x2{m * SCL2, l};
      KEEP(pf);
    }
    PIN();
    STAMP(4);
    {
      v4u vt[8];
#pragma unroll
      for (int i = 0; i < 8; ++i)
#pragma unroll
        for (int jj = 0; jj < 4; ++jj)
          vt[i][jj] = (i & 1) ? perm_hi(vf[2 * jj][i >> 1], vf[2 * jj + 1][i >> 1]) : perm_lo(vf[2 * jj][i >> 1], vf[2 * jj + 1][i >> 1]);
      f32x4 o[8];
#pragma unroll
      for (int i = 0; i < 8; ++i) o[i] = mfma32(pf, vt[i], f32x4{0.f, 0.f, 0.f, 0.f});
#pragma unroll
      for (int i = 0; i < 8; ++i) KEEP(o[i]);
      float* mo = lds + L_MO + (aw * 4 + g) * 128 + 8 * c;
      *(f32x4*)(mo) = f32x4{o[0][0], o[1][0], o[2][0], o[3][0]};
      *(f32x4*)(mo + 4) = f32x4{o[4][0], o[5][0], o[6][0], o[7][0]};
    }
    if (arrive(CN(C_ATT), lane) != 3u) await<0>(CN(C_ATT), 4u);
    STAMP(5);
    {
      f32x2 ml[4], ov[4];
#pragma unroll
      for (int u = 0; u < 4; ++u) { ml[u] = *(const f32x2*)(lds + L_ML + (u * 4 + aw) * 2); ov[u] = *(const f32x2*)(lds + L_MO + (u * 4 + aw) * 128 + 2 * lane); }
      const float M = __builtin_fmaxf(__builtin_fmaxf(ml[0][0], ml[1][0]), __builtin_fmaxf(ml[2][0], ml[3][0]));
      float L = 0.f, o0 = 0.f, o1 = 0.f;
#pragma unroll
      for (int u = 0; u < 4; ++u) {
        const float f = __builtin_amdgcn_exp2f(ml[u][0] - M);
        L = fmaf(f, ml[u][1], L); o0 = fmaf(f, ov[u][0], o0); o1 = fmaf(f, ov[u][1], o1);
      }
      const int hg = 4 * hk + aw;
      const v4u pv = {__float_as_uint(o0), T, __float_as_uint(o1), T};
      __builtin_amdgcn_raw_buffer_store_b128(pv, rs, WS_P2 + (((hg * 4 + (lane >> 4)) * 4 + ab) * 8 + as) * 256 + (lane & 15) * 16, 0, AUX_SC1);
      if (lane == 0) {
        const v4u mv = {__float_as_uint(M), T, __float_as_uint(L), T};
        __builtin_amdgcn_raw_buffer_store_b128(mv, rs, WS_ML + ((hg * 4 + ab) * 8 + as) * 16, 0, AUX_SC1);
      }
    }
    PIN();
    signal(CN(C_PUB2), lane);
    STAMP(6);
    const int ob = bid >> 6, ocw = (bid >> 3) & 7, om = (bid >> 1) & 3, oh = bid & 1;
    const int col = 64 * ocw + 4 * (4 * om + (lane & 3)) + 2 * oh;
    const unsigned b2v = ldg<unsigned>(b2 + col);
    const unsigned off = WS_P3 + ((aw * 32 + (lane >> 2)) * 8 + ocw) * 2048 + oh * 1024 + (ob * 16 + 4 * om + (lane & 3)) * 16;
    await<2>(CN(C_PUB3), 4u);
    __builtin_amdgcn_s_sleep(DLY3);
    STAMP(8);
    v4u pv0 = __builtin_amdgcn_raw_buffer_load_b128(rs, off, 0, AUX_SC1);
    v4u pv1 = __builtin_amdgcn_raw_buffer_load_b128(rs, off + 16 * 8 * 2048, 0, AUX_SC1);
#pragma clang loop unroll(disable)
    for (unsigned spins = 0; spins < SPIN_CAP; ++spins) {
      const unsigned bad0 = tagbad(pv0, T), bad1 = tagbad(pv1, T);
      if (__all((bad0 | bad1) == 0)) break;
      asm volatile("" ::: "memory");
      if (bad0) pv0 = __builtin_amdgcn_raw_buffer_load_b128(rs, off, 0, AUX_SC1);
      if (bad1) pv1 = __builtin_amdgcn_raw_buffer_load_b128(rs, off + 16 * 8 * 2048, 0, AUX_SC1);
    }
    STAMP(9);
    float sa = __uint_as_float(pv0[0]) + __uint_as_float(pv1[0]), sb = __uint_as_float(pv0[2]) + __uint_as_float(pv1[2]);
    sa += dpp<ROR4>(sa); sb += dpp<ROR4>(sb); sa += dpp<ROR8>(sa); sb += dpp<ROR8>(sb);
    sa = x16_sum(sa); sb = x16_sum(sb); sa = x32_sum(sa); sb = x32_sum(sb);
    if (lane < 4) *(f32x2*)(lds + L_FR + aw * 8 + 2 * lane) = f32x2{sa, sb};
    if (arrive(CN(C_FIN), lane) == 3u) {
      if (lane < 4) {
        const float omg = lds[L_ST + 4 * ob + 3];
        f32x2 t = *(const f32x2*)(lds + L_FR + 2 * lane);
#pragma unroll
        for (int u = 1; u < 4; ++u) t += *(const f32x2*)(lds + L_FR + u * 8 + 2 * lane);
        *(GAS unsigned*)(out + ob * DM + col) = pack2(fmaf(omg, lo2f(b2v), t[0]), fmaf(omg, hi2f(b2v), t[1]));
      }
      if (bid == 0 && lane == 0) __hip_atomic_store((gu32*)ws, T, __ATOMIC_RELAXED, __HIP_MEMORY_SCOPE_AGENT);
    }
  } else {
    const int pw = w;
    SETPRIO(3);
    const int kq = idx & 3, cq = idx >> 2;
    const bool has_ht = idx < 14;
    const int th = 14 * hk + idx;
    const int qrow = 128 * kq + 32 * pw + 8 * g, hrow = 128 * (th & 3) + 32 * pw + 8 * g;
    v2u wv[8], w1v[8];
    {
      const u16* p = wq + (size_t)qrow * NQ + hk * 512 + 64 * cq + 4 * c;
#pragma unroll
      for (int j = 0; j < 8; ++j) wv[j] = ldnt<v2u>(p + j * NQ);
    }
    const v4u xa = ldg<v4u>(x + (c >> 2) * DM + qrow);
    const unsigned ev = __builtin_amdgcn_raw_buffer_load_b32(rs, 0, 0, 0);
    PIN();
    const v4u xs = ldg<v4u>(x + pw * DM + 8 * lane);
    const v4u gwv = ldg<v4u>(gw + 8 * lane);
    const v4u xh = ldg<v4u>(x + (c >> 2) * DM + hrow);
    {
      const __amdgpu_buffer_rsrc_t rw1 = __builtin_amdgcn_make_buffer_rsrc((void*)w1, 0, has_ht ? DM * FF * 2 : 0, 0x00020000);
      const int o = (hrow * FF + 64 * (th >> 2) + 4 * c) * 2;
#pragma unroll
      for (int j = 0; j < 8; ++j) w1v[j] = __builtin_amdgcn_raw_buffer_load_b64(rw1, o + j * FF * 2, 0, AUX_NT);
    }
    if (tid < 16) ((LAS unsigned*)(lds + L_CNT))[tid] = 0u;
    BAR();
    PIN();
    unsigned T;
    {
      f32x4 d[4];
#pragma unroll
      for (int i = 0; i < 4; ++i) d[i] = mfma32(xa, tr8x4(wv, i), f32x4{0.f, 0.f, 0.f, 0.f});
      *(f32x4*)(lds + L_RQ + (pw * 64 + lane) * 4) = f32x4{d[0][0], d[1][0], d[2][0], d[3][0]};
      T = (unsigned)__builtin_amdgcn_readfirstlane(ev) + 1u;
      if (arrive(CN(C_Q), lane) == 3u) {
        f32x4 a = *(const f32x4*)(lds + L_RQ + lane * 4);
#pragma unroll
        for (int p = 1; p < 4; ++p) a += *(const f32x4*)(lds + L_RQ + (p * 64 + lane) * 4);
        const unsigned off = WS_QP + ((hk * 8 + cq) * 4 + kq) * 2048 + lane * 16;
        __builtin_amdgcn_raw_buffer_store_b128(v4u{__float_as_uint(a[0]), T, __float_as_uint(a[1]), T}, rs, off, 0, AUX_SC1);
        __builtin_amdgcn_raw_buffer_store_b128(v4u{__float_as_uint(a[2]), T, __float_as_uint(a[3]), T}, rs, off + 1024, 0, AUX_SC1);
      }
    }
    PIN();
    STAMP(1);
    {
      float xv[8], gv[8];
#pragma unroll
      for (int k = 0; k < 4; ++k) { xv[2 * k] = lo2f(xs[k]); xv[2 * k + 1] = hi2f(xs[k]); gv[2 * k] = lo2f(gwv[k]); gv[2 * k + 1] = hi2f(gwv[k]); }
      float s = 0.f, gs = 0.f;
#pragma unroll
      for (int k = 0; k < 8; ++k) { s += xv[k]; gs += bfr(xv[k] * gv[k]); }
      s = wave_sum(s); gs = wave_sum(gs);
      const float mean = s * (1.0f / DM);
      float vs = 0.f;
#pragma unroll
      for (int k = 0; k < 8; ++k) { const float d = xv[k] - mean; vs = fmaf(d, d, vs); }
      vs = wave_sum(vs);
      if (lane == 0) {
        const float var_b = bfr(vs * (1.0f / DM));
        const float den = bfr(sqrtf(bfr(var_b + 1e-5f)));
        const float gt = bfr(1.0f / (1.0f + __expf(-bfr(gs))));
        *(f32x4*)(lds + L_ST + 4 * pw) = f32x4{bfr(mean), den, gt, bfr(1.0f - gt)};
      }
      if (arrive(CN(C_ST), lane) != 3u) await<0>(CN(C_ST), 4u);
    }
    if (has_ht) {
      const f32x4 st = *(const f32x4*)(lds + L_ST + 4 * (c >> 2));
      v4u ya;
#pragma unroll
      for (int k = 0; k < 4; ++k) ya[k] = pack2(bfr(lo2f(xh[k]) - st[0]) / st[1], bfr(hi2f(xh[k]) - st[0]) / st[1]);
      f32x4 d[4];
#pragma unroll
      for (int i = 0; i < 4; ++i) d[i] = mfma32(ya, tr8x4(w1v, i), f32x4{0.f, 0.f, 0.f, 0.f});
      *(f32x4*)(lds + L_RH + (pw * 64 + lane) * 4) = f32x4{d[0][0], d[1][0], d[2][0], d[3][0]};
      if (arrive(CN(C_H), lane) == 3u) {
        f32x4 a = *(const f32x4*)(lds + L_RH + lane * 4);
#pragma unroll
        for (int p = 1; p < 4; ++p) a += *(const f32x4*)(lds + L_RH + (p * 64 + lane) * 4);
        const unsigned off = WS_HP + th * 2048 + lane * 16;
        __builtin_amdgcn_raw_buffer_store_b128(v4u{__float_as_uint(a[0]), T, __float_as_uint(a[1]), T}, rs, off, 0, AUX_SC1);
        __builtin_amdgcn_raw_buffer_store_b128(v4u{__float_as_uint(a[2]), T, __float_as_uint(a[3]), T}, rs, off + 1024, 0, AUX_SC1);
      }
    }
    f32x4 stg = *(const f32x4*)(lds + L_ST + 4 * g);
    KEEP(stg);
    PIN();
    STAMP(2);
    await<2>(CN(C_ISS), ISSN);
    const bool has_mlp = kgl < 14;
    const int fs = 14 * hk + kgl, f0 = 16 * fs;
    v2u wov[8], w2v[4];
    {
      const unsigned qoff = WS_QP + ((hk * 8 + (lane >> 5)) * 4 + pw) * 2048 + ((lane >> 4) & 1) * 1024 + (16 * ab + c) * 16;
      v4u qv[4];
#pragma unroll
      for (int i = 0; i < 4; ++i) qv[i] = __builtin_amdgcn_raw_buffer_load_b128(rs, qoff + i * 16384, 0, AUX_SC1);
      PIN();
      if (LATE_FIRST) {
        const __amdgpu_buffer_rsrc_t rw2 = __builtin_amdgcn_make_buffer_rsrc((void*)w2, 0, has_mlp ? DM * FF * 2 : 0, 0x00020000);
        const u16* p = wo + (size_t)(32 * kg + 8 * g) * DM + 256 * cg + 64 * pw + 4 * c;
        const int o2 = ((f0 + 4 * g) * DM + 256 * cg + 64 * pw + 4 * c) * 2;
#pragma unroll
        for (int j = 0; j < 8; ++j) wov[j] = ldnt<v2u>(p + j * DM);
#pragma unroll
        for (int j = 0; j < 4; ++j) w2v[j] = __builtin_amdgcn_raw_buffer_load_b64(rw2, o2 + j * DM * 2, 0, AUX_NT);
      }
      PIN();
      unsigned bad0 = tagbad(qv[0], T) | tagbad(qv[1], T) | tagbad(qv[2], T) | tagbad(qv[3], T);
      if (!__all(bad0 == 0)) {
#pragma clang loop unroll(disable)
        for (unsigned spins = 0; spins < SPIN_CAP; ++spins) {
          unsigned bad[4];
#pragma unroll
          for (int i = 0; i < 4; ++i) bad[i] = tagbad(qv[i], T);
          if (__all((bad[0] | bad[1] | bad[2] | bad[3]) == 0)) break;
          asm volatile("" ::: "memory");
#pragma unroll
          for (int i = 0; i < 4; ++i) if (bad[i]) qv[i] = __builtin_amdgcn_raw_buffer_load_b128(rs, qoff + i * 16384, 0, AUX_SC1);
        }
      }
      float* qs = lds + L_QS + (pw * 64 + lane) * 8;
      *(v4u*)(qs) = v4u{qv[0][0], qv[0][2], qv[1][0], qv[1][2]};
      *(v4u*)(qs + 4) = v4u{qv[2][0], qv[2][2], qv[3][0], qv[3][2]};
      if (arrive(CN(C_QG), lane) == 3u) {
        f32x4 a0 = *(const f32x4*)(lds + L_QS + lane * 8), a1 = *(const f32x4*)(lds + L_QS + lane * 8 + 4);
#pragma unroll
        for (int p = 1; p < 4; ++p) { a0 += *(const f32x4*)(lds + L_QS + (p * 64 + lane) * 8); a1 += *(const f32x4*)(lds + L_QS + (p * 64 + lane) * 8 + 4); }
        unsigned* qb = (unsigned*)(lds + L_QB) + 32 * (lane >> 5) + 2 * c + ((lane >> 4) & 1);
        qb[0] = pack2(a0[0], a0[1]); qb[64] = pack2(a0[2], a0[3]); qb[128] = pack2(a1[0], a1[1]); qb[192] = pack2(a1[2], a1[3]);
        signal(CN(C_QRDY), lane);
      }
    }
    STAMP(3);
    if (!LATE_FIRST) {
      const __amdgpu_buffer_rsrc_t rw2 = __builtin_amdgcn_make_buffer_rsrc((void*)w2, 0, has_mlp ? DM * FF * 2 : 0, 0x00020000);
      const u16* p = wo + (size_t)(32 * kg + 8 * g) * DM + 256 * cg + 64 * pw + 4 * c;
      const int o2 = ((f0 + 4 * g) * DM + 256 * cg + 64 * pw + 4 * c) * 2;
#pragma unroll
      for (int j = 0; j < 8; ++j) wov[j] = ldnt<v2u>(p + j * DM);
#pragma unroll
      for (int j = 0; j < 4; ++j) w2v[j] = __builtin_amdgcn_raw_buffer_load_b64(rw2, o2 + j * DM * 2, 0, AUX_NT);
    }
    PIN();
    SETPRIO(3);
    STAMP(4);
    if (pw == 3) {
      float hv = 0.f;
      if (has_mlp) {
        const float bv = lo2f((unsigned)ldg<u16>(b1 + f0 + 4 * (c & 3) + g));
        const unsigned hoff = WS_HP + ((fs >> 2) * 4 + g) * 2048 + (16 * (c >> 2) + 4 * (fs & 3) + (c & 3)) * 16;
        v4u h0 = __builtin_amdgcn_raw_buffer_load_b128(rs, hoff, 0, AUX_SC1);
        v4u h1 = __builtin_amdgcn_raw_buffer_load_b128(rs, hoff + 1024, 0, AUX_SC1);
#pragma clang loop unroll(disable)
        for (unsigned spins = 0; spins < SPIN_CAP; ++spins) {
          const unsigned bad = tagbad(h0, T) | tagbad(h1, T);
          if (__all(bad == 0)) break;
          asm volatile("" ::: "memory");
          if (bad) {
            h0 = __builtin_amdgcn_raw_buffer_load_b128(rs, hoff, 0, AUX_SC1);
            h1 = __builtin_amdgcn_raw_buffer_load_b128(rs, hoff + 1024, 0, AUX_SC1);
          }
        }
        float a[4] = {__uint_as_float(h0[0]), __uint_as_float(h0[2]), __uint_as_float(h1[0]), __uint_as_float(h1[2])};
#pragma unroll
        for (int i = 0; i < 4; ++i) a[i] = x32_sum(x16_sum(a[i]));
        const float av = g == 0 ? a[0] : g == 1 ? a[1] : g == 2 ? a[2] : a[3];
        const float u = bfr(bfr(av) + bv);
        const float inner = 0.7978845608028654f * (u + 0.044715f * u * u * u);
        hv = 0.5f * u * (1.0f + tanhf(inner));
      }
      ((u16*)(lds + L_HB))[(c >> 2) * 16 + 4 * (c & 3) + g] = (u16)pack2(hv, 0.f);
      signal(CN(C_HRDY), lane);
    }
    v4u wof[4];
    float accm[4];
    {
      v2u w2f[4];
#pragma unroll
      for (int i = 0; i < 4; ++i) {
        wof[i] = tr8x4(wov, i);
#pragma unroll
        for (int jj = 0; jj < 2; ++jj)
          w2f[i][jj] = (i & 1) ? perm_hi(w2v[2 * jj][i >> 1], w2v[2 * jj + 1][i >> 1]) : perm_lo(w2v[2 * jj][i >> 1], w2v[2 * jj + 1][i >> 1]);
        KEEP(wof[i]);
      }
      await<1>(CN(C_HRDY), 1u);
      const v2u ha = *(const v2u*)((const u16*)(lds + L_HB) + (c >> 2) * 16 + 4 * g);
#pragma unroll
      for (int i = 0; i < 4; ++i) {
        const f32x4 e = mfma16(ha, w2f[i], f32x4{0.f, 0.f, 0.f, 0.f});
        accm[i] = stg[3] * e[0];
        KEEP(accm[i]);
      }
    }
    PIN();
    STAMP(5);
    const int h2 = 4 * hk + (kgl >> 2);
    const unsigned p2off = WS_P2 + (((h2 * 4 + (kgl & 3)) * 4 + pw) * 8 + (lane >> 3)) * 256 + (lane & 7) * 32;
    const unsigned mloff = WS_ML + ((h2 * 4 + pw) * 8 + (lane >> 3)) * 16;
    await<1>(CN(C_PUB2), 4u);
    __builtin_amdgcn_s_sleep(DLY2);
    STAMP(6);
    v4u a0 = __builtin_amdgcn_raw_buffer_load_b128(rs, p2off, 0, AUX_SC1);
    v4u a1 = __builtin_amdgcn_raw_buffer_load_b128(rs, p2off + 16, 0, AUX_SC1);
    v4u am = __builtin_amdgcn_raw_buffer_load_b128(rs, mloff, 0, AUX_SC1);
#pragma clang loop unroll(disable)
    for (unsigned spins = 0; spins < SPIN_CAP; ++spins) {
      const unsigned bad = tagbad(a0, T) | tagbad(a1, T) | tagbad(am, T);
      if (__all(bad == 0)) break;
      asm volatile("" ::: "memory");
      if (bad) {
        a0 = __builtin_amdgcn_raw_buffer_load_b128(rs, p2off, 0, AUX_SC1);
        a1 = __builtin_amdgcn_raw_buffer_load_b128(rs, p2off + 16, 0, AUX_SC1);
        am = __builtin_amdgcn_raw_buffer_load_b128(rs, mloff, 0, AUX_SC1);
      }
    }
    STAMP(7);
    {
      const float m = __uint_as_float(am[0]), l = __uint_as_float(am[2]);
      float M = __builtin_fmaxf(m, dpp<ROR8>(m)); M = x32_max(x16_max(M));
      const float f = __builtin_amdgcn_exp2f(m - M);
      float v[5] = {f * __uint_as_float(a0[0]), f * __uint_as_float(a0[2]), f * __uint_as_float(a1[0]), f * __uint_as_float(a1[2]), f * l};
#pragma unroll
      for (int i = 0; i < 5; ++i) v[i] += dpp<ROR8>(v[i]);
#pragma unroll
      for (int i = 0; i < 5; ++i) v[i] = x16_sum(v[i]);
#pragma unroll
      for (int i = 0; i < 5; ++i) v[i] = x32_sum(v[i]);
      const float inv = __builtin_amdgcn_rcpf(v[4]);
      if (lane < 8) *(v2u*)((u16*)(lds + L_OB) + pw * 32 + 4 * lane) = v2u{pack2(v[0] * inv, v[1] * inv), pack2(v[2] * inv, v[3] * inv)};
    }
    if (arrive(CN(C_O), lane) != 3u) await<0>(CN(C_O), 4u);
    {
      const v4u oa = *(const v4u*)((const u16*)(lds + L_OB) + (c >> 2) * 32 + 8 * g);
      f32x4 d[4];
#pragma unroll
      for (int i = 0; i < 4; ++i) d[i] = mfma32(oa, wof[i], f32x4{0.f, 0.f, 0.f, 0.f});
#pragma unroll
      for (int i = 0; i < 4; ++i) KEEP(d[i]);
      const unsigned off = WS_P3 + ((kg * 2 + cg) * 4 + pw) * 2048 + lane * 16;
      const v4u s0 = {__float_as_uint(fmaf(stg[2], d[0][0], accm[0])), T, __float_as_uint(fmaf(stg[2], d[1][0], accm[1])), T};
      const v4u s1 = {__float_as_uint(fmaf(stg[2], d[2][0], accm[2])), T, __float_as_uint(fmaf(stg[2], d[3][0], accm[3])), T};
      __builtin_amdgcn_raw_buffer_store_b128(s0, rs, off, 0, AUX_SC1);
      __builtin_amdgcn_raw_buffer_store_b128(s1, rs, off + 1024, 0, AUX_SC1);
    }
    PIN();
    signal(CN(C_PUB3), lane);
    STAMP(8);
  }
  STAMP_DUMP();
}

extern "C" void kernel_launch(void* const* d_in, const int* in_sizes, int n_in,
                              void* d_out, int out_size, void* d_ws, size_t ws_size,
                              hipStream_t stream) {
  (void)in_sizes; (void)n_in; (void)out_size; (void)ws_size;
  fused_kernel<<<256, 512, 0, stream>>>(
      (const u16*)d_in[0], (const u16*)d_in[1], (const u16*)d_in[2], (const u16*)d_in[3], (const u16*)d_in[4],
      (const u16*)d_in[6], (const u16*)d_in[7], (const u16*)d_in[8], (const u16*)d_in[9], (const u16*)d_in[10],
      (u16*)d_out, (unsigned char*)d_ws);
}
```

```cpp
#define QGATE 6
#define LATE_FIRST 0
#include <hip/hip_runtime.h>
#include <hip/hip_bf16.h>
#include <stdint.h>

#define GAS __attribute__((address_space(1)))
#define LAS __attribute__((address_space(3)))
typedef unsigned short u16;
typedef unsigned v4u __attribute__((ext_vector_type(4)));
typedef unsigned v2u __attribute__((ext_vector_type(2)));
typedef float f32x4 __attribute__((ext_vector_type(4)));
typedef float f32x2 __attribute__((ext_vector_type(2)));
typedef __bf16 bf16x8 __attribute__((ext_vector_type(8)));
typedef __bf16 bf16x2 __attribute__((ext_vector_type(2)));
typedef short s16x4 __attribute__((ext_vector_type(4)));
typedef GAS unsigned gu32;

constexpr int DM = 512, SK = 4096, DH = 128, WINDOW = 1024, FF = 1792, NQ = 4096;
constexpr unsigned WS_QP = 4096;
constexpr unsigned WS_HP = WS_QP + (512u << 10);
constexpr unsigned WS_P2 = WS_HP + (256u << 10);
constexpr unsigned WS_ML = WS_P2 + (1u << 20);
constexpr unsigned WS_P3 = WS_ML + 16384;
constexpr unsigned WS_END = WS_P3 + (2u << 20);
constexpr unsigned SPIN_CAP = 1u << 17;
constexpr unsigned LSPIN_CAP = 1u << 22;
constexpr int AUX_SC1 = 16, AUX_NT = 2;
constexpr float LOG2E = 1.4426950408889634f;
constexpr float SCL2 = 0.08838834764831845f * 1.4426950408889634f;

#define AWIN 6
#define ADLY 0
#define ISSN 4u
#define DLY1 16
#define DLY2 8
#define DLY3 10
#define SETPRIO(n) __builtin_amdgcn_s_setprio(n)

__device__ __forceinline__ unsigned pack2(float lo, float hi) {
  return __builtin_bit_cast(unsigned, __builtin_convertvector(f32x2{lo, hi}, bf16x2));
}
__device__ __forceinline__ float bfr(float f) { return (float)(__bf16)f; }
__device__ __forceinline__ float lo2f(unsigned u) { return __builtin_bit_cast(float, u << 16); }
__device__ __forceinline__ float hi2f(unsigned u) { return __builtin_bit_cast(float, u & 0xffff0000u); }
__device__ __forceinline__ unsigned perm_lo(unsigned a, unsigned b) { return __builtin_amdgcn_perm(b, a, 0x05040100u); }
__device__ __forceinline__ unsigned perm_hi(unsigned a, unsigned b) { return __builtin_amdgcn_perm(b, a, 0x07060302u); }

template <int CTRL> __device__ __forceinline__ float dpp(float x) {
  return __builtin_bit_cast(float, __builtin_amdgcn_mov_dpp(__builtin_bit_cast(int, x), CTRL, 0xf, 0xf, true));
}
constexpr int XOR1 = 0xB1, XOR2 = 0x4E, ROR8 = 0x128, ROR4 = 0x124;
__device__ __forceinline__ float x16_max(float x) {
  auto s = __builtin_amdgcn_permlane16_swap(__float_as_uint(x), __float_as_uint(x), false, false);
  return __builtin_fmaxf(__uint_as_float(s[0]), __uint_as_float(s[1]));
}
__device__ __forceinline__ float x32_max(float x) {
  auto s = __builtin_amdgcn_permlane32_swap(__float_as_uint(x), __float_as_uint(x), false, false);
  return __builtin_fmaxf(__uint_as_float(s[0]), __uint_as_float(s[1]));
}
__device__ __forceinline__ float x16_sum(float x) {
  auto s = __builtin_amdgcn_permlane16_swap(__float_as_uint(x), __float_as_uint(x), false, false);
  return __uint_as_float(s[0]) + __uint_as_float(s[1]);
}
__device__ __forceinline__ float x32_sum(float x) {
  auto s = __builtin_amdgcn_permlane32_swap(__float_as_uint(x), __float_as_uint(x), false, false);
  return __uint_as_float(s[0]) + __uint_as_float(s[1]);
}
__device__ __forceinline__ float wave_sum(float x) {
  x += dpp<XOR1>(x); x += dpp<XOR2>(x); x += dpp<ROR4>(x); x += dpp<ROR8>(x);
  return x32_sum(x16_sum(x));
}
__device__ __forceinline__ f32x4 mfma32(v4u a, v4u b, f32x4 c) {
  return __builtin_amdgcn_mfma_f32_16x16x32_bf16(__builtin_bit_cast(bf16x8, a), __builtin_bit_cast(bf16x8, b), c, 0, 0, 0);
}
__device__ __forceinline__ f32x4 mfma16(v2u a, v2u b, f32x4 c) {
  return __builtin_amdgcn_mfma_f32_16x16x16bf16_1k(__builtin_bit_cast(s16x4, a), __builtin_bit_cast(s16x4, b), c, 0, 0, 0);
}
#define PIN() __builtin_amdgcn_sched_barrier(0)
#define BAR() do { PIN(); asm volatile("s_waitcnt lgkmcnt(0)" ::: "memory"); __builtin_amdgcn_s_barrier(); asm volatile("" ::: "memory"); PIN(); } while (0)
#define KEEP(x) asm volatile("" : "+v"(x))
#define STAMP(i) do { PIN(); unsigned long long t_ = __builtin_amdgcn_s_memrealtime(); PIN(); if (lane == 0) stl_[w * 16 + (i)] = t_; PIN(); } while (0)
#define STAMP_INIT() __shared__ unsigned long long stl_[128]
#define STAMP_DUMP()

template <typename T> __device__ __forceinline__ T ldg(const u16* p) { return *(const GAS T*)p; }
template <typename T> __device__ __forceinline__ T ldnt(const u16* p) { return __builtin_nontemporal_load((const GAS T*)p); }

constexpr int L_CNT = 0;
constexpr int L_ST = 16;
constexpr int L_RQ = 32;
constexpr int L_RH = L_RQ + 1024;
constexpr int L_QS = L_RH + 1024;
constexpr int L_HB = L_QS + 2048;
constexpr int L_MO = L_HB + 32;
constexpr int L_ML = L_MO + 2048;
constexpr int L_OB = L_ML + 32;
constexpr int L_QB = L_OB + 64;
constexpr int L_FR = L_QB + 256;
constexpr int L_KS = L_FR + 32;
constexpr int L_END = L_KS + 8192;
enum { C_Q = 0, C_QG, C_QRDY, C_ISS, C_ATT, C_PUB2, C_H, C_HRDY, C_O, C_PUB3, C_FIN, C_ST, C_QPUB };

__device__ __forceinline__ unsigned arrive(unsigned addr, int lane) {
  unsigned old = 0;
  if (lane == 0)
    asm volatile("s_waitcnt lgkmcnt(0)\n\tds_add_rtn_u32 %0, %1, %2\n\ts_waitcnt lgkmcnt(0)" : "=v"(old) : "v"(addr), "v"(1u) : "memory");
  return __builtin_amdgcn_readfirstlane(old);
}
__device__ __forceinline__ void signal(unsigned addr, int lane) {
  if (lane == 0) asm volatile("s_waitcnt lgkmcnt(0)\n\tds_add_u32 %0, %1" :: "v"(addr), "v"(1u) : "memory");
}
template <int SLEEP> __device__ __forceinline__ void await(unsigned addr, unsigned n) {
#pragma clang loop unroll(disable)
  for (unsigned s = 0; s < LSPIN_CAP; ++s) {
    unsigned v;
    asm volatile("ds_read_b32 %0, %1\n\ts_waitcnt lgkmcnt(0)" : "=v"(v) : "v"(addr) : "memory");
    if ((unsigned)__builtin_amdgcn_readfirstlane(v) >= n) break;
    if (SLEEP) __builtin_amdgcn_s_sleep(SLEEP);
  }
}

__device__ __forceinline__ v4u tr8x4(const v2u (&W)[8], int i) {
  v4u f;
#pragma unroll
  for (int jj = 0; jj < 4; ++jj)
    f[jj] = (i & 1) ? perm_hi(W[2 * jj][i >> 1], W[2 * jj + 1][i >> 1]) : perm_lo(W[2 * jj][i >> 1], W[2 * jj + 1][i >> 1]);
  return f;
}
__device__ __forceinline__ unsigned tagbad(v4u v, unsigned T) { return (v[1] ^ T) | (v[3] ^ T); }

__global__ void __launch_bounds__(512, 2)
fused_kernel(const u16* __restrict__ x, const u16* __restrict__ kcache, const u16* __restrict__ vcache,
             const u16* __restrict__ wq, const u16* __restrict__ wo, const u16* __restrict__ w1,
             const u16* __restrict__ b1, const u16* __restrict__ w2, const u16* __restrict__ b2,
             const u16* __restrict__ gw, u16* __restrict__ out, unsigned char* ws) {
  __shared__ __attribute__((aligned(16))) float lds[L_END];
  asm volatile("" :: "s"(x), "s"(kcache), "s"(vcache), "s"(wq), "s"(wo), "s"(w1), "s"(b1), "s"(w2), "s"(b2), "s"(gw), "s"(out), "s"(ws));
  const int tid = threadIdx.x, lane = tid & 63, w = __builtin_amdgcn_readfirstlane(tid >> 6);
  const int g = lane >> 4, c = lane & 15;
  const int bid = blockIdx.x, hk = bid & 7, idx = bid >> 3;
  const int ab = idx >> 3, as = idx & 7;
  const int kgl = idx >> 1, cg = idx & 1, kg = hk * 16 + kgl;
  const __amdgpu_buffer_rsrc_t rs = __builtin_amdgcn_make_buffer_rsrc((void*)ws, 0, (int)WS_END + 8192, 0x00020000);
  const unsigned cn = (unsigned)(size_t)(LAS float*)(lds + L_CNT);
#define CN(i) (cn + 4u * (i))
  STAMP_INIT();
  STAMP(0);

  if (w >= 4) {
    const int aw = w - 4;
    BAR();
    unsigned tt;
    {
      const unsigned char* tp = ws;
      tp = lane == 0 ? (const unsigned char*)(out + (bid >> 6) * DM) : tp;
      tp = lane == 1 ? ws + WS_P3 : tp;
      tp = lane == 2 ? ws + WS_END - 64 : tp;
      tp = lane == 3 ? (const unsigned char*)b2 : tp;
      tp = lane == 4 ? (const unsigned char*)(wo + (size_t)(32 * kg) * DM) : tp;
      tp = lane == 5 ? (const unsigned char*)(w2 + (size_t)(224 * hk) * DM) : tp;
      tp = lane == 6 ? (const unsigned char*)b1 : tp;
      tp = lane == 7 ? ws + WS_P2 : tp;
      tp = lane == 8 ? (const unsigned char*)(vcache + ((size_t)(ab * 8 + hk) * SK + (SK - WINDOW) + 128 * as + 32 * aw) * DH) : tp;
      tp = lane == 9 ? (const unsigned char*)(wo + (size_t)(32 * kg + 31) * DM + 511) - 2 : tp;
      tp = lane == 10 ? ws + WS_HP : tp;
      tp = lane == 11 ? ws + WS_ML : tp;
      tt = *(const GAS unsigned*)tp;
    }
    PIN();
    if (ADLY) __builtin_amdgcn_s_sleep(ADLY);
    v4u kr[8], vf[8];
    {
      const size_t kvoff = ((size_t)(ab * 8 + hk) * SK + (SK - WINDOW) + 128 * as + 32 * aw) * DH;
      const u16* kp = kcache + kvoff + 8 * lane;
      const u16* vp = vcache + kvoff + 4 * g * DH + 8 * c;
#pragma unroll
      for (int i = 0; i < 8; ++i) kr[i] = ldnt<v4u>(kp + i * 512);
#pragma unroll
      for (int j = 0; j < 8; ++j) vf[j] = ldnt<v4u>(vp + (16 * (j >> 2) + (j & 3)) * DH);
    }
    PIN();
    SETPRIO(2);
    const unsigned T = (unsigned)__builtin_amdgcn_readlane(tt, 63) + 1u;
    STAMP(1);
    v4u kf[8];
    {
      unsigned char* ks_ = (unsigned char*)(lds + L_KS) + aw * 8192;
#pragma unroll
      for (int i = 0; i < 8; ++i) { const int row = 4 * i + g; *(v4u*)(ks_ + row * 256 + ((c ^ (row & 15)) << 4)) = kr[i]; }
      asm volatile("s_waitcnt lgkmcnt(0)" ::: "memory");
#pragma unroll
      for (int t = 0; t < 2; ++t)
#pragma unroll
        for (int ks = 0; ks < 4; ++ks) kf[t * 4 + ks] = *(const v4u*)(ks_ + (16 * t + c) * 256 + (((4 * ks + g) ^ c) << 4));
#pragma unroll
      for (int i = 0; i < 8; ++i) KEEP(kf[i]);
    }
    PIN();
    asm volatile("s_waitcnt vmcnt(%0)" :: "n"(QGATE) : "memory");
    signal(CN(C_ISS), lane);
    STAMP(2);
    await<0>(CN(C_QRDY), 1u);
    STAMP(3);
    v4u pf;
    {
      f32x4 s0 = {0.f, 0.f, 0.f, 0.f}, s1 = {0.f, 0.f, 0.f, 0.f};
      const u16* qb = (const u16*)(lds + L_QB) + (c >> 2) * 128 + 8 * g;
      v4u qf[4];
#pragma unroll
      for (int ks = 0; ks < 4; ++ks) qf[ks] = *(const v4u*)(qb + 32 * ks);
#pragma unroll
      for (int ks = 0; ks < 4; ++ks) {
        s0 = mfma32(kf[ks], qf[ks], s0);
        s1 = mfma32(kf[4 + ks], qf[ks], s1);
      }
      float m = __builtin_fmaxf(__builtin_fmaxf(__builtin_fmaxf(s0[0], s0[1]), __builtin_fmaxf(s0[2], s0[3])),
                                __builtin_fmaxf(__builtin_fmaxf(s1[0], s1[1]), __builtin_fmaxf(s1[2], s1[3])));
      m = x32_max(x16_max(m));
      float p[8];
#pragma unroll
      for (int j = 0; j < 4; ++j) { p[j] = __builtin_amdgcn_exp2f((s0[j] - m) * SCL2); p[4 + j] = __builtin_amdgcn_exp2f((s1[j] - m) * SCL2); }
      pf = v4u{pack2(p[0], p[1]), pack2(p[2], p[3]), pack2(p[4], p[5]), pack2(p[6], p[7])};
      float l = ((p[0] + p[1]) + (p[2] + p[3])) + ((p[4] + p[5]) + (p[6] + p[7]));
      l = x32_sum(x16_sum(l));
      if (g == 0 && (c & 3) == 0) *(f32x2*)(lds + L_ML + (aw * 4 + (c >> 2)) * 2) = f32x2{m * SCL2, l};
      KEEP(pf);
    }
    PIN();
    STAMP(4);
    {
      v4u vt[8];
#pragma unroll
      for (int i = 0; i < 8; ++i)
#pragma unroll
        for (int jj = 0; jj < 4; ++jj)
          vt[i][jj] = (i & 1) ? perm_hi(vf[2 * jj][i >> 1], vf[2 * jj + 1][i >> 1]) : perm_lo(vf[2 * jj][i >> 1], vf[2 * jj + 1][i >> 1]);
      f32x4 o[8];
#pragma unroll
      for (int i = 0; i < 8; ++i) o[i] = mfma32(pf, vt[i], f32x4{0.f, 0.f, 0.f, 0.f});
#pragma unroll
      for (int i = 0; i < 8; ++i) KEEP(o[i]);
      float* mo = lds + L_MO + (aw * 4 + g) * 128 + 8 * c;
      *(f32x4*)(mo) = f32x4{o[0][0], o[1][0], o[2][0], o[3][0]};
      *(f32x4*)(mo + 4) = f32x4{o[4][0], o[5][0], o[6][0], o[7][0]};
    }
    if (arrive(CN(C_ATT), lane) != 3u) await<0>(CN(C_ATT), 4u);
    STAMP(5);
    {
      f32x2 ml[4], ov[4];
#pragma unroll
      for (int u = 0; u < 4; ++u) { ml[u] = *(const f32x2*)(lds + L_ML + (u * 4 + aw) * 2); ov[u] = *(const f32x2*)(lds + L_MO + (u * 4 + aw) * 128 + 2 * lane); }
      const float M = __builtin_fmaxf(__builtin_fmaxf(ml[0][0], ml[1][0]), __builtin_fmaxf(ml[2][0], ml[3][0]));
      float L = 0.f, o0 = 0.f, o1 = 0.f;
#pragma unroll
      for (int u = 0; u < 4; ++u) {
        const float f = __builtin_amdgcn_exp2f(ml[u][0] - M);
        L = fmaf(f, ml[u][1], L); o0 = fmaf(f, ov[u][0], o0); o1 = fmaf(f, ov[u][1], o1);
      }
      const int hg = 4 * hk + aw;
      const v4u pv = {__float_as_uint(o0), T, __float_as_uint(o1), T};
      __builtin_amdgcn_raw_buffer_store_b128(pv, rs, WS_P2 + (((hg * 4 + (lane >> 4)) * 4 + ab) * 8 + as) * 256 + (lane & 15) * 16, 0, AUX_SC1);
      if (lane == 0) {
        const v4u mv = {__float_as_uint(M), T, __float_as_uint(L), T};
        __builtin_amdgcn_raw_buffer_store_b128(mv, rs, WS_ML + ((hg * 4 + ab) * 8 + as) * 16, 0, AUX_SC1);
      }
    }
    PIN();
    signal(CN(C_PUB2), lane);
    STAMP(6);
    const int ob = bid >> 6, ocw = (bid >> 3) & 7, om = (bid >> 1) & 3, oh = bid & 1;
    const int col = 64 * ocw + 4 * (4 * om + (lane & 3)) + 2 * oh;
    const unsigned b2v = ldg<unsigned>(b2 + col);
    const unsigned off = WS_P3 + ((aw * 32 + (lane >> 2)) * 8 + ocw) * 2048 + oh * 1024 + (ob * 16 + 4 * om + (lane & 3)) * 16;
    await<2>(CN(C_PUB3), 4u);
    __builtin_amdgcn_s_sleep(DLY3);
    STAMP(8);
    v4u pv0 = __builtin_amdgcn_raw_buffer_load_b128(rs, off, 0, AUX_SC1);
    v4u pv1 = __builtin_amdgcn_raw_buffer_load_b128(rs, off + 16 * 8 * 2048, 0, AUX_SC1);
#pragma clang loop unroll(disable)
    for (unsigned spins = 0; spins < SPIN_CAP; ++spins) {
      const unsigned bad0 = tagbad(pv0, T), bad1 = tagbad(pv1, T);
      if (__all((bad0 | bad1) == 0)) break;
      asm volatile("" ::: "memory");
      if (bad0) pv0 = __builtin_amdgcn_raw_buffer_load_b128(rs, off, 0, AUX_SC1);
      if (bad1) pv1 = __builtin_amdgcn_raw_buffer_load_b128(rs, off + 16 * 8 * 2048, 0, AUX_SC1);
    }
    STAMP(9);
    float sa = __uint_as_float(pv0[0]) + __uint_as_float(pv1[0]), sb = __uint_as_float(pv0[2]) + __uint_as_float(pv1[2]);
    sa += dpp<ROR4>(sa); sb += dpp<ROR4>(sb); sa += dpp<ROR8>(sa); sb += dpp<ROR8>(sb);
    sa = x16_sum(sa); sb = x16_sum(sb); sa = x32_sum(sa); sb = x32_sum(sb);
    if (lane < 4) *(f32x2*)(lds + L_FR + aw * 8 + 2 * lane) = f32x2{sa, sb};
    if (arrive(CN(C_FIN), lane) == 3u) {
      if (lane < 4) {
        const float omg = lds[L_ST + 4 * ob + 3];
        f32x2 t = *(const f32x2*)(lds + L_FR + 2 * lane);
#pragma unroll
        for (int u = 1; u < 4; ++u) t += *(const f32x2*)(lds + L_FR + u * 8 + 2 * lane);
        *(GAS unsigned*)(out + ob * DM + col) = pack2(fmaf(omg, lo2f(b2v), t[0]), fmaf(omg, hi2f(b2v), t[1]));
      }
      if (bid == 0) {
        int mn = 0x7fffffff, mx = 0x7fffffff; unsigned ref = 0;
        v4u rv[4];
        for (int it = 0; it < 2000; ++it) {
          unsigned bad = 0;
          for (int k = 0; k < 4; ++k) { rv[k] = __builtin_amdgcn_raw_buffer_load_b128(rs, WS_END + (lane * 4 + k) * 16, 0, AUX_SC1); bad |= tagbad(rv[k], T); }
          if (__all(bad == 0)) break;
          asm volatile("" ::: "memory");
        }
        ref = __builtin_amdgcn_readfirstlane(rv[0][0]);
        for (int k = 0; k < 4; ++k) { int a = (int)(rv[k][0] - ref), b = (int)(rv[k][2] - ref); mn = a < mn ? a : mn; mx = b < mx ? b : mx; }
        for (int o = 32; o; o >>= 1) { int a = __shfl_xor(mn, o), b = __shfl_xor(mx, o); mn = a < mn ? a : mn; mx = b < mx ? b : mx; }
        int d_ = mx - mn; if (d_ > 3000) d_ = 3000; if (d_ < 0) d_ = 0;
        const unsigned long long n_ = __builtin_amdgcn_s_memrealtime(); while ((long long)(__builtin_amdgcn_s_memrealtime() - n_) < d_) __builtin_amdgcn_s_sleep(1);
      }
      if (bid == 0 && lane == 0) __hip_atomic_store((gu32*)ws, T, __ATOMIC_RELAXED, __HIP_MEMORY_SCOPE_AGENT);
    }
  } else {
    const int pw = w;
    SETPRIO(3);
    const int kq = idx & 3, cq = idx >> 2;
    const bool has_ht = idx < 14;
    const int th = 14 * hk + idx;
    const int qrow = 128 * kq + 32 * pw + 8 * g, hrow = 128 * (th & 3) + 32 * pw + 8 * g;
    v2u wv[8], w1v[8];
    {
      const u16* p = wq + (size_t)qrow * NQ + hk * 512 + 64 * cq + 4 * c;
#pragma unroll
      for (int j = 0; j < 8; ++j) wv[j] = ldnt<v2u>(p + j * NQ);
    }
    const v4u xa = ldg<v4u>(x + (c >> 2) * DM + qrow);
    const unsigned ev = __builtin_amdgcn_raw_buffer_load_b32(rs, 0, 0, 0);
    PIN();
    const v4u xs = ldg<v4u>(x + pw * DM + 8 * lane);
    const v4u gwv = ldg<v4u>(gw + 8 * lane);
    const v4u xh = ldg<v4u>(x + (c >> 2) * DM + hrow);
    {
      const __amdgpu_buffer_rsrc_t rw1 = __builtin_amdgcn_make_buffer_rsrc((void*)w1, 0, has_ht ? DM * FF * 2 : 0, 0x00020000);
      const int o = (hrow * FF + 64 * (th >> 2) + 4 * c) * 2;
#pragma unroll
      for (int j = 0; j < 8; ++j) w1v[j] = __builtin_amdgcn_raw_buffer_load_b64(rw1, o + j * FF * 2, 0, AUX_NT);
    }
    if (tid < 16) ((LAS unsigned*)(lds + L_CNT))[tid] = 0u;
    BAR();
    PIN();
    unsigned T;
    {
      f32x4 d[4];
#pragma unroll
      for (int i = 0; i < 4; ++i) d[i] = mfma32(xa, tr8x4(wv, i), f32x4{0.f, 0.f, 0.f, 0.f});
      *(f32x4*)(lds + L_RQ + (pw * 64 + lane) * 4) = f32x4{d[0][0], d[1][0], d[2][0], d[3][0]};
      T = (unsigned)__builtin_amdgcn_readfirstlane(ev) + 1u;
      if (arrive(CN(C_Q), lane) == 3u) {
        f32x4 a = *(const f32x4*)(lds + L_RQ + lane * 4);
#pragma unroll
        for (int p = 1; p < 4; ++p) a += *(const f32x4*)(lds + L_RQ + (p * 64 + lane) * 4);
        const unsigned off = WS_QP + ((hk * 8 + cq) * 4 + kq) * 2048 + lane * 16;
        __builtin_amdgcn_raw_buffer_store_b128(v4u{__float_as_uint(a[0]), T, __float_as_uint(a[1]), T}, rs, off, 0, AUX_SC1);
        __builtin_amdgcn_raw_buffer_store_b128(v4u{__float_as_uint(a[2]), T, __float_as_uint(a[3]), T}, rs, off + 1024, 0, AUX_SC1);
      }
    }
    PIN();
    STAMP(1);
    {
      float xv[8], gv[8];
#pragma unroll
      for (int k = 0; k < 4; ++k) { xv[2 * k] = lo2f(xs[k]); xv[2 * k + 1] = hi2f(xs[k]); gv[2 * k] = lo2f(gwv[k]); gv[2 * k + 1] = hi2f(gwv[k]); }
      float s = 0.f, gs = 0.f;
#pragma unroll
      for (int k = 0; k < 8; ++k) { s += xv[k]; gs += bfr(xv[k] * gv[k]); }
      s = wave_sum(s); gs = wave_sum(gs);
      const float mean = s * (1.0f / DM);
      float vs = 0.f;
#pragma unroll
      for (int k = 0; k < 8; ++k) { const float d = xv[k] - mean; vs = fmaf(d, d, vs); }
      vs = wave_sum(vs);
      if (lane == 0) {
        const float var_b = bfr(vs * (1.0f / DM));
        const float den = bfr(sqrtf(bfr(var_b + 1e-5f)));
        const float gt = bfr(1.0f / (1.0f + __expf(-bfr(gs))));
        *(f32x4*)(lds + L_ST + 4 * pw) = f32x4{bfr(mean), den, gt, bfr(1.0f - gt)};
      }
      if (arrive(CN(C_ST), lane) != 3u) await<0>(CN(C_ST), 4u);
    }
    if (has_ht) {
      const f32x4 st = *(const f32x4*)(lds + L_ST + 4 * (c >> 2));
      v4u ya;
#pragma unroll
      for (int k = 0; k < 4; ++k) ya[k] = pack2(bfr(lo2f(xh[k]) - st[0]) / st[1], bfr(hi2f(xh[k]) - st[0]) / st[1]);
      f32x4 d[4];
#pragma unroll
      for (int i = 0; i < 4; ++i) d[i] = mfma32(ya, tr8x4(w1v, i), f32x4{0.f, 0.f, 0.f, 0.f});
      *(f32x4*)(lds + L_RH + (pw * 64 + lane) * 4) = f32x4{d[0][0], d[1][0], d[2][0], d[3][0]};
      if (arrive(CN(C_H), lane) == 3u) {
        f32x4 a = *(const f32x4*)(lds + L_RH + lane * 4);
#pragma unroll
        for (int p = 1; p < 4; ++p) a += *(const f32x4*)(lds + L_RH + (p * 64 + lane) * 4);
        const unsigned off = WS_HP + th * 2048 + lane * 16;
        __builtin_amdgcn_raw_buffer_store_b128(v4u{__float_as_uint(a[0]), T, __float_as_uint(a[1]), T}, rs, off, 0, AUX_SC1);
        __builtin_amdgcn_raw_buffer_store_b128(v4u{__float_as_uint(a[2]), T, __float_as_uint(a[3]), T}, rs, off + 1024, 0, AUX_SC1);
      }
    }
    f32x4 stg = *(const f32x4*)(lds + L_ST + 4 * g);
    KEEP(stg);
    PIN();
    STAMP(2);
    await<2>(CN(C_ISS), ISSN);
    const bool has_mlp = kgl < 14;
    const int fs = 14 * hk + kgl, f0 = 16 * fs;
    v2u wov[8], w2v[4];
    {
      const unsigned qoff = WS_QP + ((hk * 8 + (lane >> 5)) * 4 + pw) * 2048 + ((lane >> 4) & 1) * 1024 + (16 * ab + c) * 16;
      v4u qv[4];
#pragma unroll
      for (int i = 0; i < 4; ++i) qv[i] = __builtin_amdgcn_raw_buffer_load_b128(rs, qoff + i * 16384, 0, AUX_SC1);
      PIN();
      if (LATE_FIRST) {
        const __amdgpu_buffer_rsrc_t rw2 = __builtin_amdgcn_make_buffer_rsrc((void*)w2, 0, has_mlp ? DM * FF * 2 : 0, 0x00020000);
        const u16* p = wo + (size_t)(32 * kg + 8 * g) * DM + 256 * cg + 64 * pw + 4 * c;
        const int o2 = ((f0 + 4 * g) * DM + 256 * cg + 64 * pw + 4 * c) * 2;
#pragma unroll
        for (int j = 0; j < 8; ++j) wov[j] = ldnt<v2u>(p + j * DM);
#pragma unroll
        for (int j = 0; j < 4; ++j) w2v[j] = __builtin_amdgcn_raw_buffer_load_b64(rw2, o2 + j * DM * 2, 0, AUX_NT);
      }
      PIN();
      unsigned bad0 = tagbad(qv[0], T) | tagbad(qv[1], T) | tagbad(qv[2], T) | tagbad(qv[3], T);
      if (!__all(bad0 == 0)) {
#pragma clang loop unroll(disable)
        for (unsigned spins = 0; spins < SPIN_CAP; ++spins) {
          unsigned bad[4];
#pragma unroll
          for (int i = 0; i < 4; ++i) bad[i] = tagbad(qv[i], T);
          if (__all((bad[0] | bad[1] | bad[2] | bad[3]) == 0)) break;
          asm volatile("" ::: "memory");
#pragma unroll
          for (int i = 0; i < 4; ++i) if (bad[i]) qv[i] = __builtin_amdgcn_raw_buffer_load_b128(rs, qoff + i * 16384, 0, AUX_SC1);
        }
      }
      float* qs = lds + L_QS + (pw * 64 + lane) * 8;
      *(v4u*)(qs) = v4u{qv[0][0], qv[0][2], qv[1][0], qv[1][2]};
      *(v4u*)(qs + 4) = v4u{qv[2][0], qv[2][2], qv[3][0], qv[3][2]};
      if (arrive(CN(C_QG), lane) == 3u) {
        f32x4 a0 = *(const f32x4*)(lds + L_QS + lane * 8), a1 = *(const f32x4*)(lds + L_QS + lane * 8 + 4);
#pragma unroll
        for (int p = 1; p < 4; ++p) { a0 += *(const f32x4*)(lds + L_QS + (p * 64 + lane) * 8); a1 += *(const f32x4*)(lds + L_QS + (p * 64 + lane) * 8 + 4); }
        unsigned* qb = (unsigned*)(lds + L_QB) + 32 * (lane >> 5) + 2 * c + ((lane >> 4) & 1);
        qb[0] = pack2(a0[0], a0[1]); qb[64] = pack2(a0[2], a0[3]); qb[128] = pack2(a1[0], a1[1]); qb[192] = pack2(a1[2], a1[3]);
        signal(CN(C_QRDY), lane);
      }
    }
    STAMP(3);
    if (!LATE_FIRST) {
      const __amdgpu_buffer_rsrc_t rw2 = __builtin_amdgcn_make_buffer_rsrc((void*)w2, 0, has_mlp ? DM * FF * 2 : 0, 0x00020000);
      const u16* p = wo + (size_t)(32 * kg + 8 * g) * DM + 256 * cg + 64 * pw + 4 * c;
      const int o2 = ((f0 + 4 * g) * DM + 256 * cg + 64 * pw + 4 * c) * 2;
#pragma unroll
      for (int j = 0; j < 8; ++j) wov[j] = ldnt<v2u>(p + j * DM);
#pragma unroll
      for (int j = 0; j < 4; ++j) w2v[j] = __builtin_amdgcn_raw_buffer_load_b64(rw2, o2 + j * DM * 2, 0, AUX_NT);
    }
    PIN();
    SETPRIO(3);
    STAMP(4);
    if (pw == 3) {
      float hv = 0.f;
      if (has_mlp) {
        const float bv = lo2f((unsigned)ldg<u16>(b1 + f0 + 4 * (c & 3) + g));
        const unsigned hoff = WS_HP + ((fs >> 2) * 4 + g) * 2048 + (16 * (c >> 2) + 4 * (fs & 3) + (c & 3)) * 16;
        v4u h0 = __builtin_amdgcn_raw_buffer_load_b128(rs, hoff, 0, AUX_SC1);
        v4u h1 = __builtin_amdgcn_raw_buffer_load_b128(rs, hoff + 1024, 0, AUX_SC1);
#pragma clang loop unroll(disable)
        for (unsigned spins = 0; spins < SPIN_CAP; ++spins) {
          const unsigned bad = tagbad(h0, T) | tagbad(h1, T);
          if (__all(bad == 0)) break;
          asm volatile("" ::: "memory");
          if (bad) {
            h0 = __builtin_amdgcn_raw_buffer_load_b128(rs, hoff, 0, AUX_SC1);
            h1 = __builtin_amdgcn_raw_buffer_load_b128(rs, hoff + 1024, 0, AUX_SC1);
          }
        }
        float a[4] = {__uint_as_float(h0[0]), __uint_as_float(h0[2]), __uint_as_float(h1[0]), __uint_as_float(h1[2])};
#pragma unroll
        for (int i = 0; i < 4; ++i) a[i] = x32_sum(x16_sum(a[i]));
        const float av = g == 0 ? a[0] : g == 1 ? a[1] : g == 2 ? a[2] : a[3];
        const float u = bfr(bfr(av) + bv);
        const float inner = 0.7978845608028654f * (u + 0.044715f * u * u * u);
        hv = 0.5f * u * (1.0f + tanhf(inner));
      }
      ((u16*)(lds + L_HB))[(c >> 2) * 16 + 4 * (c & 3) + g] = (u16)pack2(hv, 0.f);
      signal(CN(C_HRDY), lane);
    }
    v4u wof[4];
    float accm[4];
    {
      v2u w2f[4];
#pragma unroll
      for (int i = 0; i < 4; ++i) {
        wof[i] = tr8x4(wov, i);
#pragma unroll
        for (int jj = 0; jj < 2; ++jj)
          w2f[i][jj] = (i & 1) ? perm_hi(w2v[2 * jj][i >> 1], w2v[2 * jj + 1][i >> 1]) : perm_lo(w2v[2 * jj][i >> 1], w2v[2 * jj + 1][i >> 1]);
        KEEP(wof[i]);
      }
      await<1>(CN(C_HRDY), 1u);
      const v2u ha = *(const v2u*)((const u16*)(lds + L_HB) + (c >> 2) * 16 + 4 * g);
#pragma unroll
      for (int i = 0; i < 4; ++i) {
        const f32x4 e = mfma16(ha, w2f[i], f32x4{0.f, 0.f, 0.f, 0.f});
        accm[i] = stg[3] * e[0];
        KEEP(accm[i]);
      }
    }
    PIN();
    STAMP(5);
    const int h2 = 4 * hk + (kgl >> 2);
    const unsigned p2off = WS_P2 + (((h2 * 4 + (kgl & 3)) * 4 + pw) * 8 + (lane >> 3)) * 256 + (lane & 7) * 32;
    const unsigned mloff = WS_ML + ((h2 * 4 + pw) * 8 + (lane >> 3)) * 16;
    await<1>(CN(C_PUB2), 4u);
    __builtin_amdgcn_s_sleep(DLY2);
    STAMP(6);
    v4u a0 = __builtin_amdgcn_raw_buffer_load_b128(rs, p2off, 0, AUX_SC1);
    v4u a1 = __builtin_amdgcn_raw_buffer_load_b128(rs, p2off + 16, 0, AUX_SC1);
    v4u am = __builtin_amdgcn_raw_buffer_load_b128(rs, mloff, 0, AUX_SC1);
#pragma clang loop unroll(disable)
    for (unsigned spins = 0; spins < SPIN_CAP; ++spins) {
      const unsigned bad = tagbad(a0, T) | tagbad(a1, T) | tagbad(am, T);
      if (__all(bad == 0)) break;
      asm volatile("" ::: "memory");
      if (bad) {
        a0 = __builtin_amdgcn_raw_buffer_load_b128(rs, p2off, 0, AUX_SC1);
        a1 = __builtin_amdgcn_raw_buffer_load_b128(rs, p2off + 16, 0, AUX_SC1);
        am = __builtin_amdgcn_raw_buffer_load_b128(rs, mloff, 0, AUX_SC1);
      }
    }
    STAMP(7);
    {
      const float m = __uint_as_float(am[0]), l = __uint_as_float(am[2]);
      float M = __builtin_fmaxf(m, dpp<ROR8>(m)); M = x32_max(x16_max(M));
      const float f = __builtin_amdgcn_exp2f(m - M);
      float v[5] = {f * __uint_as_float(a0[0]), f * __uint_as_float(a0[2]), f * __uint_as_float(a1[0]), f * __uint_as_float(a1[2]), f * l};
#pragma unroll
      for (int i = 0; i < 5; ++i) v[i] += dpp<ROR8>(v[i]);
#pragma unroll
      for (int i = 0; i < 5; ++i) v[i] = x16_sum(v[i]);
#pragma unroll
      for (int i = 0; i < 5; ++i) v[i] = x32_sum(v[i]);
      const float inv = __builtin_amdgcn_rcpf(v[4]);
      if (lane < 8) *(v2u*)((u16*)(lds + L_OB) + pw * 32 + 4 * lane) = v2u{pack2(v[0] * inv, v[1] * inv), pack2(v[2] * inv, v[3] * inv)};
    }
    if (arrive(CN(C_O), lane) != 3u) await<0>(CN(C_O), 4u);
    {
      const v4u oa = *(const v4u*)((const u16*)(lds + L_OB) + (c >> 2) * 32 + 8 * g);
      f32x4 d[4];
#pragma unroll
      for (int i = 0; i < 4; ++i) d[i] = mfma32(oa, wof[i], f32x4{0.f, 0.f, 0.f, 0.f});
#pragma unroll
      for (int i = 0; i < 4; ++i) KEEP(d[i]);
      const unsigned off = WS_P3 + ((kg * 2 + cg) * 4 + pw) * 2048 + lane * 16;
      const v4u s0 = {__float_as_uint(fmaf(stg[2], d[0][0], accm[0])), T, __float_as_uint(fmaf(stg[2], d[1][0], accm[1])), T};
      const v4u s1 = {__float_as_uint(fmaf(stg[2], d[2][0], accm[2])), T, __float_as_uint(fmaf(stg[2], d[3][0], accm[3])), T};
      __builtin_amdgcn_raw_buffer_store_b128(s0, rs, off, 0, AUX_SC1);
      __builtin_amdgcn_raw_buffer_store_b128(s1, rs, off + 1024, 0, AUX_SC1);
    }
    PIN();
    signal(CN(C_PUB3), lane);
    STAMP(8);
    if (pw == 0) {
      await<1>(CN(C_PUB3), 4u);
      __builtin_amdgcn_s_sleep(4);
      asm volatile("s_waitcnt lgkmcnt(0)" ::: "memory");
      if (lane == 0) {
        volatile unsigned long long* sv = stl_;
        unsigned long long m = 0; for (int k = 0; k < 4; ++k) { unsigned long long v = sv[(0 + k) * 16 + 0]; m = v > m ? v : m; }
        const unsigned a = (unsigned)sv[0], b = (unsigned)m;
        __builtin_amdgcn_raw_buffer_store_b128(v4u{a, T, b, T}, rs, WS_END + bid * 16, 0, AUX_SC1);
      }
    }
  }
  STAMP_DUMP();
}

extern "C" void kernel_launch(void* const* d_in, const int* in_sizes, int n_in,
                              void* d_out, int out_size, void* d_ws, size_t ws_size,
                              hipStream_t stream) {
  (void)in_sizes; (void)n_in; (void)out_size; (void)ws_size;
  fused_kernel<<<256, 512, 0, stream>>>(
      (const u16*)d_in[0], (const u16*)d_in[1], (const u16*)d_in[2], (const u16*)d_in[3], (const u16*)d_in[4],
      (const u16*)d_in[6], (const u16*)d_in[7], (const u16*)d_in[8], (const u16*)d_in[9], (const u16*)d_in[10],
      (u16*)d_out, (unsigned char*)d_ws);
}
```

```cpp
#define LATE_FIRST 0
#define QGATE 8
#define DLY2 12
#define DLY3 14
#include <hip/hip_runtime.h>
#include <hip/hip_bf16.h>
#include <stdint.h>

#define GAS __attribute__((address_space(1)))
#define LAS __attribute__((address_space(3)))
typedef unsigned short u16;
typedef unsigned v4u __attribute__((ext_vector_type(4)));
typedef unsigned v2u __attribute__((ext_vector_type(2)));
typedef float f32x4 __attribute__((ext_vector_type(4)));
typedef float f32x2 __attribute__((ext_vector_type(2)));
typedef __bf16 bf16x8 __attribute__((ext_vector_type(8)));
typedef __bf16 bf16x2 __attribute__((ext_vector_type(2)));
typedef short s16x4 __attribute__((ext_vector_type(4)));
typedef GAS unsigned gu32;

constexpr int DM = 512, SK = 4096, DH = 128, WINDOW = 1024, FF = 1792, NQ = 4096;
constexpr unsigned WS_QP = 4096;
constexpr unsigned WS_HP = WS_QP + (512u << 10);
constexpr unsigned WS_P2 = WS_HP + (256u << 10);
constexpr unsigned WS_ML = WS_P2 + (1u << 20);
constexpr unsigned WS_P3 = WS_ML + 16384;
constexpr unsigned WS_END = WS_P3 + (2u << 20);
constexpr unsigned SPIN_CAP = 1u << 17;
constexpr unsigned LSPIN_CAP = 1u << 22;
constexpr int AUX_SC1 = 16, AUX_NT = 2;
constexpr float LOG2E = 1.4426950408889634f;
constexpr float SCL2 = 0.08838834764831845f * 1.4426950408889634f;

#define AWIN 6
#define ADLY 0
#define ISSN 4u
#define DLY1 16
#define SETPRIO(n) __builtin_amdgcn_s_setprio(n)

__device__ __forceinline__ unsigned pack2(float lo, float hi) {
  return __builtin_bit_cast(unsigned, __builtin_convertvector(f32x2{lo, hi}, bf16x2));
}
__device__ __forceinline__ float bfr(float f) { return (float)(__bf16)f; }
__device__ __forceinline__ float lo2f(unsigned u) { return __builtin_bit_cast(float, u << 16); }
__device__ __forceinline__ float hi2f(unsigned u) { return __builtin_bit_cast(float, u & 0xffff0000u); }
__device__ __forceinline__ unsigned perm_lo(unsigned a, unsigned b) { return __builtin_amdgcn_perm(b, a, 0x05040100u); }
__device__ __forceinline__ unsigned perm_hi(unsigned a, unsigned b) { return __builtin_amdgcn_perm(b, a, 0x07060302u); }

template <int CTRL> __device__ __forceinline__ float dpp(float x) {
  return __builtin_bit_cast(float, __builtin_amdgcn_mov_dpp(__builtin_bit_cast(int, x), CTRL, 0xf, 0xf, true));
}
constexpr int XOR1 = 0xB1, XOR2 = 0x4E, ROR8 = 0x128, ROR4 = 0x124;
__device__ __forceinline__ float x16_max(float x) {
  auto s = __builtin_amdgcn_permlane16_swap(__float_as_uint(x), __float_as_uint(x), false, false);
  return __builtin_fmaxf(__uint_as_float(s[0]), __uint_as_float(s[1]));
}
__device__ __forceinline__ float x32_max(float x) {
  auto s = __builtin_amdgcn_permlane32_swap(__float_as_uint(x), __float_as_uint(x), false, false);
  return __builtin_fmaxf(__uint_as_float(s[0]), __uint_as_float(s[1]));
}
__device__ __forceinline__ float x16_sum(float x) {
  auto s = __builtin_amdgcn_permlane16_swap(__float_as_uint(x), __float_as_uint(x), false, false);
  return __uint_as_float(s[0]) + __uint_as_float(s[1]);
}
__device__ __forceinline__ float x32_sum(float x) {
  auto s = __builtin_amdgcn_permlane32_swap(__float_as_uint(x), __float_as_uint(x), false, false);
  return __uint_as_float(s[0]) + __uint_as_float(s[1]);
}
__device__ __forceinline__ float wave_sum(float x) {
  x += dpp<XOR1>(x); x += dpp<XOR2>(x); x += dpp<ROR4>(x); x += dpp<ROR8>(x);
  return x32_sum(x16_sum(x));
}
__device__ __forceinline__ f32x4 mfma32(v4u a, v4u b, f32x4 c) {
  return __builtin_amdgcn_mfma_f32_16x16x32_bf16(__builtin_bit_cast(bf16x8, a), __builtin_bit_cast(bf16x8, b), c, 0, 0, 0);
}
__device__ __forceinline__ f32x4 mfma16(v2u a, v2u b, f32x4 c) {
  return __builtin_amdgcn_mfma_f32_16x16x16bf16_1k(__builtin_bit_cast(s16x4, a), __builtin_bit_cast(s16x4, b), c, 0, 0, 0);
}
#define PIN() __builtin_amdgcn_sched_barrier(0)
#define BAR() do { PIN(); asm volatile("s_waitcnt lgkmcnt(0)" ::: "memory"); __builtin_amdgcn_s_barrier(); asm volatile("" ::: "memory"); PIN(); } while (0)
#define KEEP(x) asm volatile("" : "+v"(x))
#define STAMP(i)
#define STAMP_INIT()
#define STAMP_DUMP()

template <typename T> __device__ __forceinline__ T ldg(const u16* p) { return *(const GAS T*)p; }
template <typename T> __device__ __forceinline__ T ldnt(const u16* p) { return __builtin_nontemporal_load((const GAS T*)p); }

constexpr int L_CNT = 0;
constexpr int L_ST = 16;
constexpr int L_RQ = 32;
constexpr int L_RH = L_RQ + 1024;
constexpr int L_QS = L_RH + 1024;
constexpr int L_HB = L_QS + 2048;
constexpr int L_MO = L_HB + 32;
constexpr int L_ML = L_MO + 2048;
constexpr int L_OB = L_ML + 32;
constexpr int L_QB = L_OB + 64;
constexpr int L_FR = L_QB + 256;
constexpr int L_KS = L_FR + 32;
constexpr int L_END = L_KS + 8192;
enum { C_Q = 0, C_QG, C_QRDY, C_ISS, C_ATT, C_PUB2, C_H, C_HRDY, C_O, C_PUB3, C_FIN, C_ST, C_QPUB };

__device__ __forceinline__ unsigned arrive(unsigned addr, int lane) {
  unsigned old = 0;
  if (lane == 0)
    asm volatile("s_waitcnt lgkmcnt(0)\n\tds_add_rtn_u32 %0, %1, %2\n\ts_waitcnt lgkmcnt(0)" : "=v"(old) : "v"(addr), "v"(1u) : "memory");
  return __builtin_amdgcn_readfirstlane(old);
}
__device__ __forceinline__ void signal(unsigned addr, int lane) {
  if (lane == 0) asm volatile("s_waitcnt lgkmcnt(0)\n\tds_add_u32 %0, %1" :: "v"(addr), "v"(1u) : "memory");
}
template <int SLEEP> __device__ __forceinline__ void await(unsigned addr, unsigned n) {
#pragma clang loop unroll(disable)
  for (unsigned s = 0; s < LSPIN_CAP; ++s) {
    unsigned v;
    asm volatile("ds_read_b32 %0, %1\n\ts_waitcnt lgkmcnt(0)" : "=v"(v) : "v"(addr) : "memory");
    if ((unsigned)__builtin_amdgcn_readfirstlane(v) >= n) break;
    if (SLEEP) __builtin_amdgcn_s_sleep(SLEEP);
  }
}

__device__ __forceinline__ v4u tr8x4(const v2u (&W)[8], int i) {
  v4u f;
#pragma unroll
  for (int jj = 0; jj < 4; ++jj)
    f[jj] = (i & 1) ? perm_hi(W[2 * jj][i >> 1], W[2 * jj + 1][i >> 1]) : perm_lo(W[2 * jj][i >> 1], W[2 * jj + 1][i >> 1]);
  return f;
}
__device__ __forceinline__ unsigned tagbad(v4u v, unsigned T) { return (v[1] ^ T) | (v[3] ^ T); }

__global__ void __launch_bounds__(512, 2)
fused_kernel(const u16* __restrict__ x, const u16* __restrict__ kcache, const u16* __restrict__ vcache,
             const u16* __restrict__ wq, const u16* __restrict__ wo, const u16* __restrict__ w1,
             const u16* __restrict__ b1, const u16* __restrict__ w2, const u16* __restrict__ b2,
             const u16* __restrict__ gw, u16* __restrict__ out, unsigned char* ws) {
  __shared__ __attribute__((aligned(16))) float lds[L_END];
  asm volatile("" :: "s"(x), "s"(kcache), "s"(vcache), "s"(wq), "s"(wo), "s"(w1), "s"(b1), "s"(w2), "s"(b2), "s"(gw), "s"(out), "s"(ws));
  const int tid = threadIdx.x, lane = tid & 63, w = __builtin_amdgcn_readfirstlane(tid >> 6);
  const int g = lane >> 4, c = lane & 15;
  const int bid = blockIdx.x, hk = bid & 7, idx = bid >> 3;
  const int ab = idx >> 3, as = idx & 7;
  const int kgl = idx >> 1, cg = idx & 1, kg = hk * 16 + kgl;
  const __amdgpu_buffer_rsrc_t rs = __builtin_amdgcn_make_buffer_rsrc((void*)ws, 0, (int)WS_END, 0x00020000);
  const unsigned cn = (unsigned)(size_t)(LAS float*)(lds + L_CNT);
#define CN(i) (cn + 4u * (i))
  STAMP_INIT();
  STAMP(0);

  if (w >= 4) {
    const int aw = w - 4;
    BAR();
    unsigned tt;
    {
      const unsigned char* tp = ws;
      tp = lane == 0 ? (const unsigned char*)(out + (bid >> 6) * DM) : tp;
      tp = lane == 1 ? ws + WS_P3 : tp;
      tp = lane == 2 ? ws + WS_END - 64 : tp;
      tp = lane == 3 ? (const unsigned char*)b2 : tp;
      tp = lane == 4 ? (const unsigned char*)(wo + (size_t)(32 * kg) * DM) : tp;
      tp = lane == 5 ? (const unsigned char*)(w2 + (size_t)(224 * hk) * DM) : tp;
      tp = lane == 6 ? (const unsigned char*)b1 : tp;
      tp = lane == 7 ? ws + WS_P2 : tp;
      tp = lane == 8 ? (const unsigned char*)(vcache + ((size_t)(ab * 8 + hk) * SK + (SK - WINDOW) + 128 * as + 32 * aw) * DH) : tp;
      tp = lane == 9 ? (const unsigned char*)(wo + (size_t)(32 * kg + 31) * DM + 511) - 2 : tp;
      tp = lane == 10 ? ws + WS_HP : tp;
      tp = lane == 11 ? ws + WS_ML : tp;
      tt = *(const GAS unsigned*)tp;
    }
    PIN();
    if (ADLY) __builtin_amdgcn_s_sleep(ADLY);
    v4u kr[8], vf[8];
    {
      const size_t kvoff = ((size_t)(ab * 8 + hk) * SK + (SK - WINDOW) + 128 * as + 32 * aw) * DH;
      const u16* kp = kcache + kvoff + 8 * lane;
      const u16* vp = vcache + kvoff + 4 * g * DH + 8 * c;
#pragma unroll
      for (int i = 0; i < 8; ++i) kr[i] = ldnt<v4u>(kp + i * 512);
#pragma unroll
      for (int j = 0; j < 8; ++j) vf[j] = ldnt<v4u>(vp + (16 * (j >> 2) + (j & 3)) * DH);
    }
    PIN();
    SETPRIO(2);
    const unsigned T = (unsigned)__builtin_amdgcn_readlane(tt, 63) + 1u;
    STAMP(1);
    v4u kf[8];
    {
      unsigned char* ks_ = (unsigned char*)(lds + L_KS) + aw * 8192;
#pragma unroll
      for (int i = 0; i < 8; ++i) { const int row = 4 * i + g; *(v4u*)(ks_ + row * 256 + ((c ^ (row & 15)) << 4)) = kr[i]; }
      asm volatile("s_waitcnt lgkmcnt(0)" ::: "memory");
#pragma unroll
      for (int t = 0; t < 2; ++t)
#pragma unroll
        for (int ks = 0; ks < 4; ++ks) kf[t * 4 + ks] = *(const v4u*)(ks_ + (16 * t + c) * 256 + (((4 * ks + g) ^ c) << 4));
#pragma unroll
      for (int i = 0; i < 8; ++i) KEEP(kf[i]);
    }
    PIN();
    asm volatile("s_waitcnt vmcnt(%0)" :: "n"(QGATE) : "memory");
    signal(CN(C_ISS), lane);
    STAMP(2);
    await<0>(CN(C_QRDY), 1u);
    STAMP(3);
    v4u pf;
    {
      f32x4 s0 = {0.f, 0.f, 0.f, 0.f}, s1 = {0.f, 0.f, 0.f, 0.f};
      const u16* qb = (const u16*)(lds + L_QB) + (c >> 2) * 128 + 8 * g;
      v4u qf[4];
#pragma unroll
      for (int ks = 0; ks < 4; ++ks) qf[ks] = *(const v4u*)(qb + 32 * ks);
#pragma unroll
      for (int ks = 0; ks < 4; ++ks) {
        s0 = mfma32(kf[ks], qf[ks], s0);
        s1 = mfma32(kf[4 + ks], qf[ks], s1);
      }
      float m = __builtin_fmaxf(__builtin_fmaxf(__builtin_fmaxf(s0[0], s0[1]), __builtin_fmaxf(s0[2], s0[3])),
                                __builtin_fmaxf(__builtin_fmaxf(s1[0], s1[1]), __builtin_fmaxf(s1[2], s1[3])));
      m = x32_max(x16_max(m));
      float p[8];
#pragma unroll
      for (int j = 0; j < 4; ++j) { p[j] = __builtin_amdgcn_exp2f((s0[j] - m) * SCL2); p[4 + j] = __builtin_amdgcn_exp2f((s1[j] - m) * SCL2); }
      pf = v4u{pack2(p[0], p[1]), pack2(p[2], p[3]), pack2(p[4], p[5]), pack2(p[6], p[7])};
      float l = ((p[0] + p[1]) + (p[2] + p[3])) + ((p[4] + p[5]) + (p[6] + p[7]));
      l = x32_sum(x16_sum(l));
      if (g == 0 && (c & 3) == 0) *(f32x2*)(lds + L_ML + (aw * 4 + (c >> 2)) * 2) = f32x2{m * SCL2, l};
      KEEP(pf);
    }
    PIN();
    STAMP(4);
    {
      v4u vt[8];
#pragma unroll
      for (int i = 0; i < 8; ++i)
#pragma unroll
        for (int jj = 0; jj < 4; ++jj)
          vt[i][jj] = (i & 1) ? perm_hi(vf[2 * jj][i >> 1], vf[2 * jj + 1][i >> 1]) : perm_lo(vf[2 * jj][i >> 1], vf[2 * jj + 1][i >> 1]);
      f32x4 o[8];
#pragma unroll
      for (int i = 0; i < 8; ++i) o[i] = mfma32(pf, vt[i], f32x4{0.f, 0.f, 0.f, 0.f});
#pragma unroll
      for (int i = 0; i < 8; ++i) KEEP(o[i]);
      float* mo = lds + L_MO + (aw * 4 + g) * 128 + 8 * c;
      *(f32x4*)(mo) = f32x4{o[0][0], o[1][0], o[2][0], o[3][0]};
      *(f32x4*)(mo + 4) = f32x4{o[4][0], o[5][0], o[6][0], o[7][0]};
    }
    if (arrive(CN(C_ATT), lane) != 3u) await<0>(CN(C_ATT), 4u);
    STAMP(5);
    {
      f32x2 ml[4], ov[4];
#pragma unroll
      for (int u = 0; u < 4; ++u) { ml[u] = *(const f32x2*)(lds + L_ML + (u * 4 + aw) * 2); ov[u] = *(const f32x2*)(lds + L_MO + (u * 4 + aw) * 128 + 2 * lane); }
      const float M = __builtin_fmaxf(__builtin_fmaxf(ml[0][0], ml[1][0]), __builtin_fmaxf(ml[2][0], ml[3][0]));
      float L = 0.f, o0 = 0.f, o1 = 0.f;
#pragma unroll
      for (int u = 0; u < 4; ++u) {
        const float f = __builtin_amdgcn_exp2f(ml[u][0] - M);
        L = fmaf(f, ml[u][1], L); o0 = fmaf(f, ov[u][0], o0); o1 = fmaf(f, ov[u][1], o1);
      }
      const int hg = 4 * hk + aw;
      const v4u pv = {__float_as_uint(o0), T, __float_as_uint(o1), T};
      __builtin_amdgcn_raw_buffer_store_b128(pv, rs, WS_P2 + (((hg * 4 + (lane >> 4)) * 4 + ab) * 8 + as) * 256 + (lane & 15) * 16, 0, AUX_SC1);
      if (lane == 0) {
        const v4u mv = {__float_as_uint(M), T, __float_as_uint(L), T};
        __builtin_amdgcn_raw_buffer_store_b128(mv, rs, WS_ML + ((hg * 4 + ab) * 8 + as) * 16, 0, AUX_SC1);
      }
    }
    PIN();
    signal(CN(C_PUB2), lane);
    STAMP(6);
    const int ob = bid >> 6, ocw = (bid >> 3) & 7, om = (bid >> 1) & 3, oh = bid & 1;
    const int col = 64 * ocw + 4 * (4 * om + (lane & 3)) + 2 * oh;
    const unsigned b2v = ldg<unsigned>(b2 + col);
    const unsigned off = WS_P3 + ((aw * 32 + (lane >> 2)) * 8 + ocw) * 2048 + oh * 1024 + (ob * 16 + 4 * om + (lane & 3)) * 16;
    await<2>(CN(C_PUB3), 4u);
    __builtin_amdgcn_s_sleep(DLY3);
    STAMP(8);
    v4u pv0 = __builtin_amdgcn_raw_buffer_load_b128(rs, off, 0, AUX_SC1);
    v4u pv1 = __builtin_amdgcn_raw_buffer_load_b128(rs, off + 16 * 8 * 2048, 0, AUX_SC1);
#pragma clang loop unroll(disable)
    for (unsigned spins = 0; spins < SPIN_CAP; ++spins) {
      const unsigned bad0 = tagbad(pv0, T), bad1 = tagbad(pv1, T);
      if (__all((bad0 | bad1) == 0)) break;
      asm volatile("" ::: "memory");
      if (bad0) pv0 = __builtin_amdgcn_raw_buffer_load_b128(rs, off, 0, AUX_SC1);
      if (bad1) pv1 = __builtin_amdgcn_raw_buffer_load_b128(rs, off + 16 * 8 * 2048, 0, AUX_SC1);
    }
    STAMP(9);
    float sa = __uint_as_float(pv0[0]) + __uint_as_float(pv1[0]), sb = __uint_as_float(pv0[2]) + __uint_as_float(pv1[2]);
    sa += dpp<ROR4>(sa); sb += dpp<ROR4>(sb); sa += dpp<ROR8>(sa); sb += dpp<ROR8>(sb);
    sa = x16_sum(sa); sb = x16_sum(sb); sa = x32_sum(sa); sb = x32_sum(sb);
    if (lane < 4) *(f32x2*)(lds + L_FR + aw * 8 + 2 * lane) = f32x2{sa, sb};
    if (arrive(CN(C_FIN), lane) == 3u) {
      if (lane < 4) {
        const float omg = lds[L_ST + 4 * ob + 3];
        f32x2 t = *(const f32x2*)(lds + L_FR + 2 * lane);
#pragma unroll
        for (int u = 1; u < 4; ++u) t += *(const f32x2*)(lds + L_FR + u * 8 + 2 * lane);
        *(GAS unsigned*)(out + ob * DM + col) = pack2(fmaf(omg, lo2f(b2v), t[0]), fmaf(omg, hi2f(b2v), t[1]));
      }
      if (bid == 0 && lane == 0) __hip_atomic_store((gu32*)ws, T, __ATOMIC_RELAXED, __HIP_MEMORY_SCOPE_AGENT);
    }
  } else {
    const int pw = w;
    SETPRIO(3);
    const int kq = idx & 3, cq = idx >> 2;
    const bool has_ht = idx < 14;
    const int th = 14 * hk + idx;
    const int qrow = 128 * kq + 32 * pw + 8 * g, hrow = 128 * (th & 3) + 32 * pw + 8 * g;
    v2u wv[8], w1v[8];
    {
      const u16* p = wq + (size_t)qrow * NQ + hk * 512 + 64 * cq + 4 * c;
#pragma unroll
      for (int j = 0; j < 8; ++j) wv[j] = ldnt<v2u>(p + j * NQ);
    }
    const v4u xa = ldg<v4u>(x + (c >> 2) * DM + qrow);
    const unsigned ev = __builtin_amdgcn_raw_buffer_load_b32(rs, 0, 0, 0);
    PIN();
    const v4u xs = ldg<v4u>(x + pw * DM + 8 * lane);
    const v4u gwv = ldg<v4u>(gw + 8 * lane);
    const v4u xh = ldg<v4u>(x + (c >> 2) * DM + hrow);
    {
      const __amdgpu_buffer_rsrc_t rw1 = __builtin_amdgcn_make_buffer_rsrc((void*)w1, 0, has_ht ? DM * FF * 2 : 0, 0x00020000);
      const int o = (hrow * FF + 64 * (th >> 2) + 4 * c) * 2;
#pragma unroll
      for (int j = 0; j < 8; ++j) w1v[j] = __builtin_amdgcn_raw_buffer_load_b64(rw1, o + j * FF * 2, 0, AUX_NT);
    }
    if (tid < 16) ((LAS unsigned*)(lds + L_CNT))[tid] = 0u;
    BAR();
    PIN();
    unsigned T;
    {
      f32x4 d[4];
#pragma unroll
      for (int i = 0; i < 4; ++i) d[i] = mfma32(xa, tr8x4(wv, i), f32x4{0.f, 0.f, 0.f, 0.f});
      *(f32x4*)(lds + L_RQ + (pw * 64 + lane) * 4) = f32x4{d[0][0], d[1][0], d[2][0], d[3][0]};
      T = (unsigned)__builtin_amdgcn_readfirstlane(ev) + 1u;
      if (arrive(CN(C_Q), lane) == 3u) {
        f32x4 a = *(const f32x4*)(lds + L_RQ + lane * 4);
#pragma unroll
        for (int p = 1; p < 4; ++p) a += *(const f32x4*)(lds + L_RQ + (p * 64 + lane) * 4);
        const unsigned off = WS_QP + ((hk * 8 + cq) * 4 + kq) * 2048 + lane * 16;
        __builtin_amdgcn_raw_buffer_store_b128(v4u{__float_as_uint(a[0]), T, __float_as_uint(a[1]), T}, rs, off, 0, AUX_SC1);
        __builtin_amdgcn_raw_buffer_store_b128(v4u{__float_as_uint(a[2]), T, __float_as_uint(a[3]), T}, rs, off + 1024, 0, AUX_SC1);
      }
    }
    PIN();
    STAMP(1);
    {
      float xv[8], gv[8];
#pragma unroll
      for (int k = 0; k < 4; ++k) { xv[2 * k] = lo2f(xs[k]); xv[2 * k + 1] = hi2f(xs[k]); gv[2 * k] = lo2f(gwv[k]); gv[2 * k + 1] = hi2f(gwv[k]); }
      float s = 0.f, gs = 0.f;
#pragma unroll
      for (int k = 0; k < 8; ++k) { s += xv[k]; gs += bfr(xv[k] * gv[k]); }
      s = wave_sum(s); gs = wave_sum(gs);
      const float mean = s * (1.0f / DM);
      float vs = 0.f;
#pragma unroll
      for (int k = 0; k < 8; ++k) { const float d = xv[k] - mean; vs = fmaf(d, d, vs); }
      vs = wave_sum(vs);
      if (lane == 0) {
        const float var_b = bfr(vs * (1.0f / DM));
        const float den = bfr(sqrtf(bfr(var_b + 1e-5f)));
        const float gt = bfr(1.0f / (1.0f + __expf(-bfr(gs))));
        *(f32x4*)(lds + L_ST + 4 * pw) = f32x4{bfr(mean), den, gt, bfr(1.0f - gt)};
      }
      if (arrive(CN(C_ST), lane) != 3u) await<0>(CN(C_ST), 4u);
    }
    if (has_ht) {
      const f32x4 st = *(const f32x4*)(lds + L_ST + 4 * (c >> 2));
      v4u ya;
#pragma unroll
      for (int k = 0; k < 4; ++k) ya[k] = pack2(bfr(lo2f(xh[k]) - st[0]) / st[1], bfr(hi2f(xh[k]) - st[0]) / st[1]);
      f32x4 d[4];
#pragma unroll
      for (int i = 0; i < 4; ++i) d[i] = mfma32(ya, tr8x4(w1v, i), f32x4{0.f, 0.f, 0.f, 0.f});
      *(f32x4*)(lds + L_RH + (pw * 64 + lane) * 4) = f32x4{d[0][0], d[1][0], d[2][0], d[3][0]};
      if (arrive(CN(C_H), lane) == 3u) {
        f32x4 a = *(const f32x4*)(lds + L_RH + lane * 4);
#pragma unroll
        for (int p = 1; p < 4; ++p) a += *(const f32x4*)(lds + L_RH + (p * 64 + lane) * 4);
        const unsigned off = WS_HP + th * 2048 + lane * 16;
        __builtin_amdgcn_raw_buffer_store_b128(v4u{__float_as_uint(a[0]), T, __float_as_uint(a[1]), T}, rs, off, 0, AUX_SC1);
        __builtin_amdgcn_raw_buffer_store_b128(v4u{__float_as_uint(a[2]), T, __float_as_uint(a[3]), T}, rs, off + 1024, 0, AUX_SC1);
      }
    }
    f32x4 stg = *(const f32x4*)(lds + L_ST + 4 * g);
    KEEP(stg);
    PIN();
    STAMP(2);
    await<2>(CN(C_ISS), ISSN);
    const bool has_mlp = kgl < 14;
    const int fs = 14 * hk + kgl, f0 = 16 * fs;
    v2u wov[8], w2v[4];
    {
      const unsigned qoff = WS_QP + ((hk * 8 + (lane >> 5)) * 4 + pw) * 2048 + ((lane >> 4) & 1) * 1024 + (16 * ab + c) * 16;
      v4u qv[4];
#pragma unroll
      for (int i = 0; i < 4; ++i) qv[i] = __builtin_amdgcn_raw_buffer_load_b128(rs, qoff + i * 16384, 0, AUX_SC1);
      PIN();
      if (LATE_FIRST) {
        const __amdgpu_buffer_rsrc_t rw2 = __builtin_amdgcn_make_buffer_rsrc((void*)w2, 0, has_mlp ? DM * FF * 2 : 0, 0x00020000);
        const u16* p = wo + (size_t)(32 * kg + 8 * g) * DM + 256 * cg + 64 * pw + 4 * c;
        const int o2 = ((f0 + 4 * g) * DM + 256 * cg + 64 * pw + 4 * c) * 2;
#pragma unroll
        for (int j = 0; j < 8; ++j) wov[j] = ldnt<v2u>(p + j * DM);
#pragma unroll
        for (int j = 0; j < 4; ++j) w2v[j] = __builtin_amdgcn_raw_buffer_load_b64(rw2, o2 + j * DM * 2, 0, AUX_NT);
      }
      PIN();
      unsigned bad0 = tagbad(qv[0], T) | tagbad(qv[1], T) | tagbad(qv[2], T) | tagbad(qv[3], T);
      if (!__all(bad0 == 0)) {
#pragma clang loop unroll(disable)
        for (unsigned spins = 0; spins < SPIN_CAP; ++spins) {
          unsigned bad[4];
#pragma unroll
          for (int i = 0; i < 4; ++i) bad[i] = tagbad(qv[i], T);
          if (__all((bad[0] | bad[1] | bad[2] | bad[3]) == 0)) break;
          asm volatile("" ::: "memory");
#pragma unroll
          for (int i = 0; i < 4; ++i) if (bad[i]) qv[i] = __builtin_amdgcn_raw_buffer_load_b128(rs, qoff + i * 16384, 0, AUX_SC1);
        }
      }
      float* qs = lds + L_QS + (pw * 64 + lane) * 8;
      *(v4u*)(qs) = v4u{qv[0][0], qv[0][2], qv[1][0], qv[1][2]};
      *(v4u*)(qs + 4) = v4u{qv[2][0], qv[2][2], qv[3][0], qv[3][2]};
      if (arrive(CN(C_QG), lane) == 3u) {
        f32x4 a0 = *(const f32x4*)(lds + L_QS + lane * 8), a1 = *(const f32x4*)(lds + L_QS + lane * 8 + 4);
#pragma unroll
        for (int p = 1; p < 4; ++p) { a0 += *(const f32x4*)(lds + L_QS + (p * 64 + lane) * 8); a1 += *(const f32x4*)(lds + L_QS + (p * 64 + lane) * 8 + 4); }
        unsigned* qb = (unsigned*)(lds + L_QB) + 32 * (lane >> 5) + 2 * c + ((lane >> 4) & 1);
        qb[0] = pack2(a0[0], a0[1]); qb[64] = pack2(a0[2], a0[3]); qb[128] = pack2(a1[0], a1[1]); qb[192] = pack2(a1[2], a1[3]);
        signal(CN(C_QRDY), lane);
      }
    }
    STAMP(3);
    if (!LATE_FIRST) {
      const __amdgpu_buffer_rsrc_t rw2 = __builtin_amdgcn_make_buffer_rsrc((void*)w2, 0, has_mlp ? DM * FF * 2 : 0, 0x00020000);
      const u16* p = wo + (size_t)(32 * kg + 8 * g) * DM + 256 * cg + 64 * pw + 4 * c;
      const int o2 = ((f0 + 4 * g) * DM + 256 * cg + 64 * pw + 4 * c) * 2;
#pragma unroll
      for (int j = 0; j < 8; ++j) wov[j] = ldnt<v2u>(p + j * DM);
#pragma unroll
      for (int j = 0; j < 4; ++j) w2v[j] = __builtin_amdgcn_raw_buffer_load_b64(rw2, o2 + j * DM * 2, 0, AUX_NT);
    }
    PIN();
    SETPRIO(3);
    STAMP(4);
    if (pw == 3) {
      float hv = 0.f;
      if (has_mlp) {
        const float bv = lo2f((unsigned)ldg<u16>(b1 + f0 + 4 * (c & 3) + g));
        const unsigned hoff = WS_HP + ((fs >> 2) * 4 + g) * 2048 + (16 * (c >> 2) + 4 * (fs & 3) + (c & 3)) * 16;
        v4u h0 = __builtin_amdgcn_raw_buffer_load_b128(rs, hoff, 0, AUX_SC1);
        v4u h1 = __builtin_amdgcn_raw_buffer_load_b128(rs, hoff + 1024, 0, AUX_SC1);
#pragma clang loop unroll(disable)
        for (unsigned spins = 0; spins < SPIN_CAP; ++spins) {
          const unsigned bad = tagbad(h0, T) | tagbad(h1, T);
          if (__all(bad == 0)) break;
          asm volatile("" ::: "memory");
          if (bad) {
            h0 = __builtin_amdgcn_raw_buffer_load_b128(rs, hoff, 0, AUX_SC1);
            h1 = __builtin_amdgcn_raw_buffer_load_b128(rs, hoff + 1024, 0, AUX_SC1);
          }
        }
        float a[4] = {__uint_as_float(h0[0]), __uint_as_float(h0[2]), __uint_as_float(h1[0]), __uint_as_float(h1[2])};
#pragma unroll
        for (int i = 0; i < 4; ++i) a[i] = x32_sum(x16_sum(a[i]));
        const float av = g == 0 ? a[0] : g == 1 ? a[1] : g == 2 ? a[2] : a[3];
        const float u = bfr(bfr(av) + bv);
        const float inner = 0.7978845608028654f * (u + 0.044715f * u * u * u);
        hv = 0.5f * u * (1.0f + tanhf(inner));
      }
      ((u16*)(lds + L_HB))[(c >> 2) * 16 + 4 * (c & 3) + g] = (u16)pack2(hv, 0.f);
      signal(CN(C_HRDY), lane);
    }
    v4u wof[4];
    float accm[4];
    {
      v2u w2f[4];
#pragma unroll
      for (int i = 0; i < 4; ++i) {
        wof[i] = tr8x4(wov, i);
#pragma unroll
        for (int jj = 0; jj < 2; ++jj)
          w2f[i][jj] = (i & 1) ? perm_hi(w2v[2 * jj][i >> 1], w2v[2 * jj + 1][i >> 1]) : perm_lo(w2v[2 * jj][i >> 1], w2v[2 * jj + 1][i >> 1]);
        KEEP(wof[i]);
      }
      await<1>(CN(C_HRDY), 1u);
      const v2u ha = *(const v2u*)((const u16*)(lds + L_HB) + (c >> 2) * 16 + 4 * g);
#pragma unroll
      for (int i = 0; i < 4; ++i) {
        const f32x4 e = mfma16(ha, w2f[i], f32x4{0.f, 0.f, 0.f, 0.f});
        accm[i] = stg[3] * e[0];
        KEEP(accm[i]);
      }
    }
    PIN();
    STAMP(5);
    const int h2 = 4 * hk + (kgl >> 2);
    const unsigned p2off = WS_P2 + (((h2 * 4 + (kgl & 3)) * 4 + pw) * 8 + (lane >> 3)) * 256 + (lane & 7) * 32;
    const unsigned mloff = WS_ML + ((h2 * 4 + pw) * 8 + (lane >> 3)) * 16;
    await<1>(CN(C_PUB2), 4u);
    __builtin_amdgcn_s_sleep(DLY2);
    STAMP(6);
    v4u a0 = __builtin_amdgcn_raw_buffer_load_b128(rs, p2off, 0, AUX_SC1);
    v4u a1 = __builtin_amdgcn_raw_buffer_load_b128(rs, p2off + 16, 0, AUX_SC1);
    v4u am = __builtin_amdgcn_raw_buffer_load_b128(rs, mloff, 0, AUX_SC1);
#pragma clang loop unroll(disable)
    for (unsigned spins = 0; spins < SPIN_CAP; ++spins) {
      const unsigned bad = tagbad(a0, T) | tagbad(a1, T) | tagbad(am, T);
      if (__all(bad == 0)) break;
      asm volatile("" ::: "memory");
      if (bad) {
        a0 = __builtin_amdgcn_raw_buffer_load_b128(rs, p2off, 0, AUX_SC1);
        a1 = __builtin_amdgcn_raw_buffer_load_b128(rs, p2off + 16, 0, AUX_SC1);
        am = __builtin_amdgcn_raw_buffer_load_b128(rs, mloff, 0, AUX_SC1);
      }
    }
    STAMP(7);
    {
      const float m = __uint_as_float(am[0]), l = __uint_as_float(am[2]);
      float M = __builtin_fmaxf(m, dpp<ROR8>(m)); M = x32_max(x16_max(M));
      const float f = __builtin_amdgcn_exp2f(m - M);
      float v[5] = {f * __uint_as_float(a0[0]), f * __uint_as_float(a0[2]), f * __uint_as_float(a1[0]), f * __uint_as_float(a1[2]), f * l};
#pragma unroll
      for (int i = 0; i < 5; ++i) v[i] += dpp<ROR8>(v[i]);
#pragma unroll
      for (int i = 0; i < 5; ++i) v[i] = x16_sum(v[i]);
#pragma unroll
      for (int i = 0; i < 5; ++i) v[i] = x32_sum(v[i]);
      const float inv = __builtin_amdgcn_rcpf(v[4]);
      if (lane < 8) *(v2u*)((u16*)(lds + L_OB) + pw * 32 + 4 * lane) = v2u{pack2(v[0] * inv, v[1] * inv), pack2(v[2] * inv, v[3] * inv)};
    }
    if (arrive(CN(C_O), lane) != 3u) await<0>(CN(C_O), 4u);
    {
      const v4u oa = *(const v4u*)((const u16*)(lds + L_OB) + (c >> 2) * 32 + 8 * g);
      f32x4 d[4];
#pragma unroll
      for (int i = 0; i < 4; ++i) d[i] = mfma32(oa, wof[i], f32x4{0.f, 0.f, 0.f, 0.f});
#pragma unroll
      for (int i = 0; i < 4; ++i) KEEP(d[i]);
      const unsigned off = WS_P3 + ((kg * 2 + cg) * 4 + pw) * 2048 + lane * 16;
      const v4u s0 = {__float_as_uint(fmaf(stg[2], d[0][0], accm[0])), T, __float_as_uint(fmaf(stg[2], d[1][0], accm[1])), T};
      const v4u s1 = {__float_as_uint(fmaf(stg[2], d[2][0], accm[2])), T, __float_as_uint(fmaf(stg[2], d[3][0], accm[3])), T};
      __builtin_amdgcn_raw_buffer_store_b128(s0, rs, off, 0, AUX_SC1);
      __builtin_amdgcn_raw_buffer_store_b128(s1, rs, off + 1024, 0, AUX_SC1);
    }
    PIN();
    signal(CN(C_PUB3), lane);
    STAMP(8);
  }
  STAMP_DUMP();
}

extern "C" void kernel_launch(void* const* d_in, const int* in_sizes, int n_in,
                              void* d_out, int out_size, void* d_ws, size_t ws_size,
                              hipStream_t stream) {
  (void)in_sizes; (void)n_in; (void)out_size; (void)ws_size;
  fused_kernel<<<256, 512, 0, stream>>>(
      (const u16*)d_in[0], (const u16*)d_in[1], (const u16*)d_in[2], (const u16*)d_in[3], (const u16*)d_in[4],
      (const u16*)d_in[6], (const u16*)d_in[7], (const u16*)d_in[8], (const u16*)d_in[9], (const u16*)d_in[10],
      (u16*)d_out, (unsigned char*)d_ws);
}
```

```cpp
#include <hip/hip_runtime.h>
#include <hip/hip_bf16.h>
#include <stdint.h>

#define GAS __attribute__((address_space(1)))
#define LAS __attribute__((address_space(3)))
typedef unsigned short u16;
typedef unsigned v4u __attribute__((ext_vector_type(4)));
typedef unsigned v2u __attribute__((ext_vector_type(2)));
typedef float f32x4 __attribute__((ext_vector_type(4)));
typedef float f32x2 __attribute__((ext_vector_type(2)));
typedef __bf16 bf16x8 __attribute__((ext_vector_type(8)));
typedef __bf16 bf16x2 __attribute__((ext_vector_type(2)));
typedef short s16x4 __attribute__((ext_vector_type(4)));
typedef GAS unsigned gu32;

constexpr int DM = 512, SK = 4096, DH = 128, WINDOW = 1024, FF = 1792, NQ = 4096;
constexpr unsigned WS_QP = 4096;
constexpr unsigned WS_HP = WS_QP + (512u << 10);
constexpr unsigned WS_P2 = WS_HP + (256u << 10);
constexpr unsigned WS_ML = WS_P2 + (1u << 20);
constexpr unsigned WS_P3 = WS_ML + 16384;
constexpr unsigned WS_END = WS_P3 + (2u << 20);
constexpr unsigned SPIN_CAP = 1u << 17;
constexpr unsigned LSPIN_CAP = 1u << 22;
constexpr int AUX_SC1 = 16, AUX_NT = 2;
constexpr float LOG2E = 1.4426950408889634f;
constexpr float SCL2 = 0.08838834764831845f * 1.4426950408889634f;

#define AWIN 6
#define ADLY 0
#define QGATE 8
#define ISSN 4u
#define LATE_FIRST 0
#define DLY1 16
#define DLY2 12
#define DLY3 14
#define SETPRIO(n) __builtin_amdgcn_s_setprio(n)

__device__ __forceinline__ unsigned pack2(float lo, float hi) {
  return __builtin_bit_cast(unsigned, __builtin_convertvector(f32x2{lo, hi}, bf16x2));
}
__device__ __forceinline__ float bfr(float f) { return (float)(__bf16)f; }
__device__ __forceinline__ float lo2f(unsigned u) { return __builtin_bit_cast(float, u << 16); }
__device__ __forceinline__ float hi2f(unsigned u) { return __builtin_bit_cast(float, u & 0xffff0000u); }
__device__ __forceinline__ unsigned perm_lo(unsigned a, unsigned b) { return __builtin_amdgcn_perm(b, a, 0x05040100u); }
__device__ __forceinline__ unsigned perm_hi(unsigned a, unsigned b) { return __builtin_amdgcn_perm(b, a, 0x07060302u); }

template <int CTRL> __device__ __forceinline__ float dpp(float x) {
  return __builtin_bit_cast(float, __builtin_amdgcn_mov_dpp(__builtin_bit_cast(int, x), CTRL, 0xf, 0xf, true));
}
constexpr int XOR1 = 0xB1, XOR2 = 0x4E, ROR8 = 0x128, ROR4 = 0x124;
__device__ __forceinline__ float x16_max(float x) {
  auto s = __builtin_amdgcn_permlane16_swap(__float_as_uint(x), __float_as_uint(x), false, false);
  return __builtin_fmaxf(__uint_as_float(s[0]), __uint_as_float(s[1]));
}
__device__ __forceinline__ float x32_max(float x) {
  auto s = __builtin_amdgcn_permlane32_swap(__float_as_uint(x), __float_as_uint(x), false, false);
  return __builtin_fmaxf(__uint_as_float(s[0]), __uint_as_float(s[1]));
}
__device__ __forceinline__ float x16_sum(float x) {
  auto s = __builtin_amdgcn_permlane16_swap(__float_as_uint(x), __float_as_uint(x), false, false);
  return __uint_as_float(s[0]) + __uint_as_float(s[1]);
}
__device__ __forceinline__ float x32_sum(float x) {
  auto s = __builtin_amdgcn_permlane32_swap(__float_as_uint(x), __float_as_uint(x), false, false);
  return __uint_as_float(s[0]) + __uint_as_float(s[1]);
}
__device__ __forceinline__ float wave_sum(float x) {
  x += dpp<XOR1>(x); x += dpp<XOR2>(x); x += dpp<ROR4>(x); x += dpp<ROR8>(x);
  return x32_sum(x16_sum(x));
}
__device__ __forceinline__ f32x4 mfma32(v4u a, v4u b, f32x4 c) {
  return __builtin_amdgcn_mfma_f32_16x16x32_bf16(__builtin_bit_cast(bf16x8, a), __builtin_bit_cast(bf16x8, b), c, 0, 0, 0);
}
__device__ __forceinline__ f32x4 mfma16(v2u a, v2u b, f32x4 c) {
  return __builtin_amdgcn_mfma_f32_16x16x16bf16_1k(__builtin_bit_cast(s16x4, a), __builtin_bit_cast(s16x4, b), c, 0, 0, 0);
}
#define PIN() __builtin_amdgcn_sched_barrier(0)
#define BAR() do { PIN(); asm volatile("s_waitcnt lgkmcnt(0)" ::: "memory"); __builtin_amdgcn_s_barrier(); asm volatile("" ::: "memory"); PIN(); } while (0)
#define KEEP(x) asm volatile("" : "+v"(x))
#define STAMP(i)
#define STAMP_INIT()
#define STAMP_DUMP()

template <typename T> __device__ __forceinline__ T ldg(const u16* p) { return *(const GAS T*)p; }
template <typename T> __device__ __forceinline__ T ldnt(const u16* p) { return __builtin_nontemporal_load((const GAS T*)p); }

constexpr int L_CNT = 0;
constexpr int L_ST = 16;
constexpr int L_RQ = 32;
constexpr int L_RH = L_RQ + 1024;
constexpr int L_QS = L_RH + 1024;
constexpr int L_HB = L_QS + 2048;
constexpr int L_MO = L_HB + 32;
constexpr int L_ML = L_MO + 2048;
constexpr int L_OB = L_ML + 32;
constexpr int L_QB = L_OB + 64;
constexpr int L_FR = L_QB + 256;
constexpr int L_KS = L_FR + 32;
constexpr int L_END = L_KS + 8192;
enum { C_Q = 0, C_QG, C_QRDY, C_ISS, C_ATT, C_PUB2, C_H, C_HRDY, C_O, C_PUB3, C_FIN, C_ST, C_QPUB };

__device__ __forceinline__ unsigned arrive(unsigned addr, int lane) {
  unsigned old = 0;
  if (lane == 0)
    asm volatile("s_waitcnt lgkmcnt(0)\n\tds_add_rtn_u32 %0, %1, %2\n\ts_waitcnt lgkmcnt(0)" : "=v"(old) : "v"(addr), "v"(1u) : "memory");
  return __builtin_amdgcn_readfirstlane(old);
}
__device__ __forceinline__ void signal(unsigned addr, int lane) {
  if (lane == 0) asm volatile("s_waitcnt lgkmcnt(0)\n\tds_add_u32 %0, %1" :: "v"(addr), "v"(1u) : "memory");
}
template <int SLEEP> __device__ __forceinline__ void await(unsigned addr, unsigned n) {
#pragma clang loop unroll(disable)
  for (unsigned s = 0; s < LSPIN_CAP; ++s) {
    unsigned v;
    asm volatile("ds_read_b32 %0, %1\n\ts_waitcnt lgkmcnt(0)" : "=v"(v) : "v"(addr) : "memory");
    if ((unsigned)__builtin_amdgcn_readfirstlane(v) >= n) break;
    if (SLEEP) __builtin_amdgcn_s_sleep(SLEEP);
  }
}

__device__ __forceinline__ v4u tr8x4(const v2u (&W)[8], int i) {
  v4u f;
#pragma unroll
  for (int jj = 0; jj < 4; ++jj)
    f[jj] = (i & 1) ? perm_hi(W[2 * jj][i >> 1], W[2 * jj + 1][i >> 1]) : perm_lo(W[2 * jj][i >> 1], W[2 * jj + 1][i >> 1]);
  return f;
}
__device__ __forceinline__ unsigned tagbad(v4u v, unsigned T) { return (v[1] ^ T) | (v[3] ^ T); }

__global__ void __launch_bounds__(512, 2)
fused_kernel(const u16* __restrict__ x, const u16* __restrict__ kcache, const u16* __restrict__ vcache,
             const u16* __restrict__ wq, const u16* __restrict__ wo, const u16* __restrict__ w1,
             const u16* __restrict__ b1, const u16* __restrict__ w2, const u16* __restrict__ b2,
             const u16* __restrict__ gw, u16* __restrict__ out, unsigned char* ws) {
  __shared__ __attribute__((aligned(16))) float lds[L_END];
  asm volatile("" :: "s"(x), "s"(kcache), "s"(vcache), "s"(wq), "s"(wo), "s"(w1), "s"(b1), "s"(w2), "s"(b2), "s"(gw), "s"(out), "s"(ws));
  const int tid = threadIdx.x, lane = tid & 63, w = __builtin_amdgcn_readfirstlane(tid >> 6);
  const int g = lane >> 4, c = lane & 15;
  const int bid = blockIdx.x, hk = bid & 7, idx = bid >> 3;
  const int ab = idx >> 3, as = idx & 7;
  const int kgl = idx >> 1, cg = idx & 1, kg = hk * 16 + kgl;
  const __amdgpu_buffer_rsrc_t rs = __builtin_amdgcn_make_buffer_rsrc((void*)ws, 0, (int)WS_END, 0x00020000);
  const unsigned cn = (unsigned)(size_t)(LAS float*)(lds + L_CNT);
#define CN(i) (cn + 4u * (i))
  STAMP_INIT();
  STAMP(0);

  if (w >= 4) {
    const int aw = w - 4;
    BAR();
    unsigned tt;
    {
      const unsigned char* tp = ws;
      tp = lane == 0 ? (const unsigned char*)(out + (bid >> 6) * DM) : tp;
      tp = lane == 1 ? ws + WS_P3 : tp;
      tp = lane == 2 ? ws + WS_END - 64 : tp;
      tp = lane == 3 ? (const unsigned char*)b2 : tp;
      tp = lane == 4 ? (const unsigned char*)(wo + (size_t)(32 * kg) * DM) : tp;
      const int fst = 16 * (14 * hk + (kgl < 14 ? kgl : 13));
      tp = lane == 5 ? (const unsigned char*)(w2 + (size_t)fst * DM + 256 * cg) : tp;
      tp = lane == 12 ? (const unsigned char*)(w2 + (size_t)(fst + 15) * DM + 256 * cg) : tp;
      tp = lane == 13 ? (const unsigned char*)(b1 + fst) : tp;
      tp = lane == 6 ? (const unsigned char*)b1 : tp;
      tp = lane == 7 ? ws + WS_P2 : tp;
      tp = lane == 8 ? (const unsigned char*)(vcache + ((size_t)(ab * 8 + hk) * SK + (SK - WINDOW) + 128 * as + 32 * aw) * DH) : tp;
      tp = lane == 9 ? (const unsigned char*)(wo + (size_t)(32 * kg + 31) * DM + 511) - 2 : tp;
      tp = lane == 10 ? ws + WS_HP : tp;
      tp = lane == 11 ? ws + WS_ML : tp;
      tt = *(const GAS unsigned*)tp;
    }
    PIN();
    if (ADLY) __builtin_amdgcn_s_sleep(ADLY);
    v4u kr[8], vf[8];
    {
      const size_t kvoff = ((size_t)(ab * 8 + hk) * SK + (SK - WINDOW) + 128 * as + 32 * aw) * DH;
      const u16* kp = kcache + kvoff + 8 * lane;
      const u16* vp = vcache + kvoff + 4 * g * DH + 8 * c;
#pragma unroll
      for (int i = 0; i < 8; ++i) kr[i] = ldnt<v4u>(kp + i * 512);
#pragma unroll
      for (int j = 0; j < 8; ++j) vf[j] = ldnt<v4u>(vp + (16 * (j >> 2) + (j & 3)) * DH);
    }
    PIN();
    SETPRIO(2);
    const unsigned T = (unsigned)__builtin_amdgcn_readlane(tt, 63) + 1u;
    STAMP(1);
    v4u kf[8];
    {
      unsigned char* ks_ = (unsigned char*)(lds + L_KS) + aw * 8192;
#pragma unroll
      for (int i = 0; i < 8; ++i) { const int row = 4 * i + g; *(v4u*)(ks_ + row * 256 + ((c ^ (row & 15)) << 4)) = kr[i]; }
      asm volatile("s_waitcnt lgkmcnt(0)" ::: "memory");
#pragma unroll
      for (int t = 0; t < 2; ++t)
#pragma unroll
        for (int ks = 0; ks < 4; ++ks) kf[t * 4 + ks] = *(const v4u*)(ks_ + (16 * t + c) * 256 + (((4 * ks + g) ^ c) << 4));
#pragma unroll
      for (int i = 0; i < 8; ++i) KEEP(kf[i]);
    }
    PIN();
    asm volatile("s_waitcnt vmcnt(%0)" :: "n"(QGATE) : "memory");
    signal(CN(C_ISS), lane);
    STAMP(2);
    await<0>(CN(C_QRDY), 1u);
    STAMP(3);
    v4u pf;
    {
      f32x4 s0 = {0.f, 0.f, 0.f, 0.f}, s1 = {0.f, 0.f, 0.f, 0.f};
      const u16* qb = (const u16*)(lds + L_QB) + (c >> 2) * 128 + 8 * g;
      v4u qf[4];
#pragma unroll
      for (int ks = 0; ks < 4; ++ks) qf[ks] = *(const v4u*)(qb + 32 * ks);
#pragma unroll
      for (int ks = 0; ks < 4; ++ks) {
        s0 = mfma32(kf[ks], qf[ks], s0);
        s1 = mfma32(kf[4 + ks], qf[ks], s1);
      }
      float m = __builtin_fmaxf(__builtin_fmaxf(__builtin_fmaxf(s0[0], s0[1]), __builtin_fmaxf(s0[2], s0[3])),
                                __builtin_fmaxf(__builtin_fmaxf(s1[0], s1[1]), __builtin_fmaxf(s1[2], s1[3])));
      m = x32_max(x16_max(m));
      float p[8];
#pragma unroll
      for (int j = 0; j < 4; ++j) { p[j] = __builtin_amdgcn_exp2f((s0[j] - m) * SCL2); p[4 + j] = __builtin_amdgcn_exp2f((s1[j] - m) * SCL2); }
      pf = v4u{pack2(p[0], p[1]), pack2(p[2], p[3]), pack2(p[4], p[5]), pack2(p[6], p[7])};
      float l = ((p[0] + p[1]) + (p[2] + p[3])) + ((p[4] + p[5]) + (p[6] + p[7]));
      l = x32_sum(x16_sum(l));
      if (g == 0 && (c & 3) == 0) *(f32x2*)(lds + L_ML + (aw * 4 + (c >> 2)) * 2) = f32x2{m * SCL2, l};
      KEEP(pf);
    }
    PIN();
    STAMP(4);
    {
      v4u vt[8];
#pragma unroll
      for (int i = 0; i < 8; ++i)
#pragma unroll
        for (int jj = 0; jj < 4; ++jj)
          vt[i][jj] = (i & 1) ? perm_hi(vf[2 * jj][i >> 1], vf[2 * jj + 1][i >> 1]) : perm_lo(vf[2 * jj][i >> 1], vf[2 * jj + 1][i >> 1]);
      f32x4 o[8];
#pragma unroll
      for (int i = 0; i < 8; ++i) o[i] = mfma32(pf, vt[i], f32x4{0.f, 0.f, 0.f, 0.f});
#pragma unroll
      for (int i = 0; i < 8; ++i) KEEP(o[i]);
      float* mo = lds + L_MO + (aw * 4 + g) * 128 + 8 * c;
      *(f32x4*)(mo) = f32x4{o[0][0], o[1][0], o[2][0], o[3][0]};
      *(f32x4*)(mo + 4) = f32x4{o[4][0], o[5][0], o[6][0], o[7][0]};
    }
    if (arrive(CN(C_ATT), lane) != 3u) await<0>(CN(C_ATT), 4u);
    STAMP(5);
    {
      f32x2 ml[4], ov[4];
#pragma unroll
      for (int u = 0; u < 4; ++u) { ml[u] = *(const f32x2*)(lds + L_ML + (u * 4 + aw) * 2); ov[u] = *(const f32x2*)(lds + L_MO + (u * 4 + aw) * 128 + 2 * lane); }
      const float M = __builtin_fmaxf(__builtin_fmaxf(ml[0][0], ml[1][0]), __builtin_fmaxf(ml[2][0], ml[3][0]));
      float L = 0.f, o0 = 0.f, o1 = 0.f;
#pragma unroll
      for (int u = 0; u < 4; ++u) {
        const float f = __builtin_amdgcn_exp2f(ml[u][0] - M);
        L = fmaf(f, ml[u][1], L); o0 = fmaf(f, ov[u][0], o0); o1 = fmaf(f, ov[u][1], o1);
      }
      const int hg = 4 * hk + aw;
      const v4u pv = {__float_as_uint(o0), T, __float_as_uint(o1), T};
      __builtin_amdgcn_raw_buffer_store_b128(pv, rs, WS_P2 + (((hg * 4 + (lane >> 4)) * 4 + ab) * 8 + as) * 256 + (lane & 15) * 16, 0, AUX_SC1);
      if (lane == 0) {
        const v4u mv = {__float_as_uint(M), T, __float_as_uint(L), T};
        __builtin_amdgcn_raw_buffer_store_b128(mv, rs, WS_ML + ((hg * 4 + ab) * 8 + as) * 16, 0, AUX_SC1);
      }
    }
    PIN();
    signal(CN(C_PUB2), lane);
    STAMP(6);
    const int ob = bid >> 6, ocw = (bid >> 3) & 7, om = (bid >> 1) & 3, oh = bid & 1;
    const int col = 64 * ocw + 4 * (4 * om + (lane & 3)) + 2 * oh;
    const unsigned b2v = ldg<unsigned>(b2 + col);
    const unsigned off = WS_P3 + ((aw * 32 + (lane >> 2)) * 8 + ocw) * 2048 + oh * 1024 + (ob * 16 + 4 * om + (lane & 3)) * 16;
    await<2>(CN(C_PUB3), 4u);
    __builtin_amdgcn_s_sleep(DLY3);
    STAMP(8);
    v4u pv0 = __builtin_amdgcn_raw_buffer_load_b128(rs, off, 0, AUX_SC1);
    v4u pv1 = __builtin_amdgcn_raw_buffer_load_b128(rs, off + 16 * 8 * 2048, 0, AUX_SC1);
#pragma clang loop unroll(disable)
    for (unsigned spins = 0; spins < SPIN_CAP; ++spins) {
      const unsigned bad0 = tagbad(pv0, T), bad1 = tagbad(pv1, T);
      if (__all((bad0 | bad1) == 0)) break;
      asm volatile("" ::: "memory");
      if (bad0) pv0 = __builtin_amdgcn_raw_buffer_load_b128(rs, off, 0, AUX_SC1);
      if (bad1) pv1 = __builtin_amdgcn_raw_buffer_load_b128(rs, off + 16 * 8 * 2048, 0, AUX_SC1);
    }
    STAMP(9);
    float sa = __uint_as_float(pv0[0]) + __uint_as_float(pv1[0]), sb = __uint_as_float(pv0[2]) + __uint_as_float(pv1[2]);
    sa += dpp<ROR4>(sa); sb += dpp<ROR4>(sb); sa += dpp<ROR8>(sa); sb += dpp<ROR8>(sb);
    sa = x16_sum(sa); sb = x16_sum(sb); sa = x32_sum(sa); sb = x32_sum(sb);
    if (lane < 4) *(f32x2*)(lds + L_FR + aw * 8 + 2 * lane) = f32x2{sa, sb};
    if (arrive(CN(C_FIN), lane) == 3u) {
      if (lane < 4) {
        const float omg = lds[L_ST + 4 * ob + 3];
        f32x2 t = *(const f32x2*)(lds + L_FR + 2 * lane);
#pragma unroll
        for (int u = 1; u < 4; ++u) t += *(const f32x2*)(lds + L_FR + u * 8 + 2 * lane);
        *(GAS unsigned*)(out + ob * DM + col) = pack2(fmaf(omg, lo2f(b2v), t[0]), fmaf(omg, hi2f(b2v), t[1]));
      }
      if (bid == 0 && lane == 0) __hip_atomic_store((gu32*)ws, T, __ATOMIC_RELAXED, __HIP_MEMORY_SCOPE_AGENT);
    }
  } else {
    const int pw = w;
    SETPRIO(3);
    const int kq = idx & 3, cq = idx >> 2;
    const bool has_ht = idx < 14;
    const int th = 14 * hk + idx;
    const int qrow = 128 * kq + 32 * pw + 8 * g, hrow = 128 * (th & 3) + 32 * pw + 8 * g;
    v2u wv[8], w1v[8];
    {
      const u16* p = wq + (size_t)qrow * NQ + hk * 512 + 64 * cq + 4 * c;
#pragma unroll
      for (int j = 0; j < 8; ++j) wv[j] = ldnt<v2u>(p + j * NQ);
    }
    const v4u xa = ldg<v4u>(x + (c >> 2) * DM + qrow);
    const unsigned ev = __builtin_amdgcn_raw_buffer_load_b32(rs, 0, 0, 0);
    PIN();
    const v4u xs = ldg<v4u>(x + pw * DM + 8 * lane);
    const v4u gwv = ldg<v4u>(gw + 8 * lane);
    const v4u xh = ldg<v4u>(x + (c >> 2) * DM + hrow);
    {
      const __amdgpu_buffer_rsrc_t rw1 = __builtin_amdgcn_make_buffer_rsrc((void*)w1, 0, has_ht ? DM * FF * 2 : 0, 0x00020000);
      const int o = (hrow * FF + 64 * (th >> 2) + 4 * c) * 2;
#pragma unroll
      for (int j = 0; j < 8; ++j) w1v[j] = __builtin_amdgcn_raw_buffer_load_b64(rw1, o + j * FF * 2, 0, AUX_NT);
    }
    if (tid < 16) ((LAS unsigned*)(lds + L_CNT))[tid] = 0u;
    BAR();
    PIN();
    unsigned T;
    {
      f32x4 d[4];
#pragma unroll
      for (int i = 0; i < 4; ++i) d[i] = mfma32(xa, tr8x4(wv, i), f32x4{0.f, 0.f, 0.f, 0.f});
      *(f32x4*)(lds + L_RQ + (pw * 64 + lane) * 4) = f32x4{d[0][0], d[1][0], d[2][0], d[3][0]};
      T = (unsigned)__builtin_amdgcn_readfirstlane(ev) + 1u;
      if (arrive(CN(C_Q), lane) == 3u) {
        f32x4 a = *(const f32x4*)(lds + L_RQ + lane * 4);
#pragma unroll
        for (int p = 1; p < 4; ++p) a += *(const f32x4*)(lds + L_RQ + (p * 64 + lane) * 4);
        const unsigned off = WS_QP + ((hk * 8 + cq) * 4 + kq) * 2048 + lane * 16;
        __builtin_amdgcn_raw_buffer_store_b128(v4u{__float_as_uint(a[0]), T, __float_as_uint(a[1]), T}, rs, off, 0, AUX_SC1);
        __builtin_amdgcn_raw_buffer_store_b128(v4u{__float_as_uint(a[2]), T, __float_as_uint(a[3]), T}, rs, off + 1024, 0, AUX_SC1);
      }
    }
    PIN();
    STAMP(1);
    {
      float xv[8], gv[8];
#pragma unroll
      for (int k = 0; k < 4; ++k) { xv[2 * k] = lo2f(xs[k]); xv[2 * k + 1] = hi2f(xs[k]); gv[2 * k] = lo2f(gwv[k]); gv[2 * k + 1] = hi2f(gwv[k]); }
      float s = 0.f, gs = 0.f;
#pragma unroll
      for (int k = 0; k < 8; ++k) { s += xv[k]; gs += bfr(xv[k] * gv[k]); }
      s = wave_sum(s); gs = wave_sum(gs);
      const float mean = s * (1.0f / DM);
      float vs = 0.f;
#pragma unroll
      for (int k = 0; k < 8; ++k) { const float d = xv[k] - mean; vs = fmaf(d, d, vs); }
      vs = wave_sum(vs);
      if (lane == 0) {
        const float var_b = bfr(vs * (1.0f / DM));
        const float den = bfr(sqrtf(bfr(var_b + 1e-5f)));
        const float gt = bfr(1.0f / (1.0f + __expf(-bfr(gs))));
        *(f32x4*)(lds + L_ST + 4 * pw) = f32x4{bfr(mean), den, gt, bfr(1.0f - gt)};
      }
      if (arrive(CN(C_ST), lane) != 3u) await<0>(CN(C_ST), 4u);
    }
    if (has_ht) {
      const f32x4 st = *(const f32x4*)(lds + L_ST + 4 * (c >> 2));
      v4u ya;
#pragma unroll
      for (int k = 0; k < 4; ++k) ya[k] = pack2(bfr(lo2f(xh[k]) - st[0]) / st[1], bfr(hi2f(xh[k]) - st[0]) / st[1]);
      f32x4 d[4];
#pragma unroll
      for (int i = 0; i < 4; ++i) d[i] = mfma32(ya, tr8x4(w1v, i), f32x4{0.f, 0.f, 0.f, 0.f});
      *(f32x4*)(lds + L_RH + (pw * 64 + lane) * 4) = f32x4{d[0][0], d[1][0], d[2][0], d[3][0]};
      if (arrive(CN(C_H), lane) == 3u) {
        f32x4 a = *(const f32x4*)(lds + L_RH + lane * 4);
#pragma unroll
        for (int p = 1; p < 4; ++p) a += *(const f32x4*)(lds + L_RH + (p * 64 + lane) * 4);
        const unsigned off = WS_HP + th * 2048 + lane * 16;
        __builtin_amdgcn_raw_buffer_store_b128(v4u{__float_as_uint(a[0]), T, __float_as_uint(a[1]), T}, rs, off, 0, AUX_SC1);
        __builtin_amdgcn_raw_buffer_store_b128(v4u{__float_as_uint(a[2]), T, __float_as_uint(a[3]), T}, rs, off + 1024, 0, AUX_SC1);
      }
    }
    f32x4 stg = *(const f32x4*)(lds + L_ST + 4 * g);
    KEEP(stg);
    PIN();
    STAMP(2);
    await<2>(CN(C_ISS), ISSN);
    const bool has_mlp = kgl < 14;
    const int fs = 14 * hk + kgl, f0 = 16 * fs;
    v2u wov[8], w2v[4];
    {
      const unsigned qoff = WS_QP + ((hk * 8 + (lane >> 5)) * 4 + pw) * 2048 + ((lane >> 4) & 1) * 1024 + (16 * ab + c) * 16;
      v4u qv[4];
#pragma unroll
      for (int i = 0; i < 4; ++i) qv[i] = __builtin_amdgcn_raw_buffer_load_b128(rs, qoff + i * 16384, 0, AUX_SC1);
      PIN();
      if (LATE_FIRST) {
        const __amdgpu_buffer_rsrc_t rw2 = __builtin_amdgcn_make_buffer_rsrc((void*)w2, 0, has_mlp ? DM * FF * 2 : 0, 0x00020000);
        const u16* p = wo + (size_t)(32 * kg + 8 * g) * DM + 256 * cg + 64 * pw + 4 * c;
        const int o2 = ((f0 + 4 * g) * DM + 256 * cg + 64 * pw + 4 * c) * 2;
#pragma unroll
        for (int j = 0; j < 8; ++j) wov[j] = ldnt<v2u>(p + j * DM);
#pragma unroll
        for (int j = 0; j < 4; ++j) w2v[j] = __builtin_amdgcn_raw_buffer_load_b64(rw2, o2 + j * DM * 2, 0, AUX_NT);
      }
      PIN();
      unsigned bad0 = tagbad(qv[0], T) | tagbad(qv[1], T) | tagbad(qv[2], T) | tagbad(qv[3], T);
      if (!__all(bad0 == 0)) {
#pragma clang loop unroll(disable)
        for (unsigned spins = 0; spins < SPIN_CAP; ++spins) {
          unsigned bad[4];
#pragma unroll
          for (int i = 0; i < 4; ++i) bad[i] = tagbad(qv[i], T);
          if (__all((bad[0] | bad[1] | bad[2] | bad[3]) == 0)) break;
          asm volatile("" ::: "memory");
#pragma unroll
          for (int i = 0; i < 4; ++i) if (bad[i]) qv[i] = __builtin_amdgcn_raw_buffer_load_b128(rs, qoff + i * 16384, 0, AUX_SC1);
        }
      }
      float* qs = lds + L_QS + (pw * 64 + lane) * 8;
      *(v4u*)(qs) = v4u{qv[0][0], qv[0][2], qv[1][0], qv[1][2]};
      *(v4u*)(qs + 4) = v4u{qv[2][0], qv[2][2], qv[3][0], qv[3][2]};
      if (arrive(CN(C_QG), lane) == 3u) {
        f32x4 a0 = *(const f32x4*)(lds + L_QS + lane * 8), a1 = *(const f32x4*)(lds + L_QS + lane * 8 + 4);
#pragma unroll
        for (int p = 1; p < 4; ++p) { a0 += *(const f32x4*)(lds + L_QS + (p * 64 + lane) * 8); a1 += *(const f32x4*)(lds + L_QS + (p * 64 + lane) * 8 + 4); }
        unsigned* qb = (unsigned*)(lds + L_QB) + 32 * (lane >> 5) + 2 * c + ((lane >> 4) & 1);
        qb[0] = pack2(a0[0], a0[1]); qb[64] = pack2(a0[2], a0[3]); qb[128] = pack2(a1[0], a1[1]); qb[192] = pack2(a1[2], a1[3]);
        signal(CN(C_QRDY), lane);
      }
    }
    STAMP(3);
    if (!LATE_FIRST) {
      const __amdgpu_buffer_rsrc_t rw2 = __builtin_amdgcn_make_buffer_rsrc((void*)w2, 0, has_mlp ? DM * FF * 2 : 0, 0x00020000);
      const u16* p = wo + (size_t)(32 * kg + 8 * g) * DM + 256 * cg + 64 * pw + 4 * c;
      const int o2 = ((f0 + 4 * g) * DM + 256 * cg + 64 * pw + 4 * c) * 2;
#pragma unroll
      for (int j = 0; j < 8; ++j) wov[j] = ldnt<v2u>(p + j * DM);
#pragma unroll
      for (int j = 0; j < 4; ++j) w2v[j] = __builtin_amdgcn_raw_buffer_load_b64(rw2, o2 + j * DM * 2, 0, AUX_NT);
    }
    PIN();
    SETPRIO(3);
    STAMP(4);
    if (pw == 3) {
      float hv = 0.f;
      if (has_mlp) {
        const float bv = lo2f((unsigned)ldg<u16>(b1 + f0 + 4 * (c & 3) + g));
        const unsigned hoff = WS_HP + ((fs >> 2) * 4 + g) * 2048 + (16 * (c >> 2) + 4 * (fs & 3) + (c & 3)) * 16;
        v4u h0 = __builtin_amdgcn_raw_buffer_load_b128(rs, hoff, 0, AUX_SC1);
        v4u h1 = __builtin_amdgcn_raw_buffer_load_b128(rs, hoff + 1024, 0, AUX_SC1);
#pragma clang loop unroll(disable)
        for (unsigned spins = 0; spins < SPIN_CAP; ++spins) {
          const unsigned bad = tagbad(h0, T) | tagbad(h1, T);
          if (__all(bad == 0)) break;
          asm volatile("" ::: "memory");
          if (bad) {
            h0 = __builtin_amdgcn_raw_buffer_load_b128(rs, hoff, 0, AUX_SC1);
            h1 = __builtin_amdgcn_raw_buffer_load_b128(rs, hoff + 1024, 0, AUX_SC1);
          }
        }
        float a[4] = {__uint_as_float(h0[0]), __uint_as_float(h0[2]), __uint_as_float(h1[0]), __uint_as_float(h1[2])};
#pragma unroll
        for (int i = 0; i < 4; ++i) a[i] = x32_sum(x16_sum(a[i]));
        const float av = g == 0 ? a[0] : g == 1 ? a[1] : g == 2 ? a[2] : a[3];
        const float u = bfr(bfr(av) + bv);
        const float inner = 0.7978845608028654f * (u + 0.044715f * u * u * u);
        hv = 0.5f * u * (1.0f + tanhf(inner));
      }
      ((u16*)(lds + L_HB))[(c >> 2) * 16 + 4 * (c & 3) + g] = (u16)pack2(hv, 0.f);
      signal(CN(C_HRDY), lane);
    }
    v4u wof[4];
    float accm[4];
    {
      v2u w2f[4];
#pragma unroll
      for (int i = 0; i < 4; ++i) {
        wof[i] = tr8x4(wov, i);
#pragma unroll
        for (int jj = 0; jj < 2; ++jj)
          w2f[i][jj] = (i & 1) ? perm_hi(w2v[2 * jj][i >> 1], w2v[2 * jj + 1][i >> 1]) : perm_lo(w2v[2 * jj][i >> 1], w2v[2 * jj + 1][i >> 1]);
        KEEP(wof[i]);
      }
      await<1>(CN(C_HRDY), 1u);
      const v2u ha = *(const v2u*)((const u16*)(lds + L_HB) + (c >> 2) * 16 + 4 * g);
#pragma unroll
      for (int i = 0; i < 4; ++i) {
        const f32x4 e = mfma16(ha, w2f[i], f32x4{0.f, 0.f, 0.f, 0.f});
        accm[i] = stg[3] * e[0];
        KEEP(accm[i]);
      }
    }
    PIN();
    STAMP(5);
    const int h2 = 4 * hk + (kgl >> 2);
    const unsigned p2off = WS_P2 + (((h2 * 4 + (kgl & 3)) * 4 + pw) * 8 + (lane >> 3)) * 256 + (lane & 7) * 32;
    const unsigned mloff = WS_ML + ((h2 * 4 + pw) * 8 + (lane >> 3)) * 16;
    await<1>(CN(C_PUB2), 4u);
    __builtin_amdgcn_s_sleep(DLY2);
    STAMP(6);
    v4u a0 = __builtin_amdgcn_raw_buffer_load_b128(rs, p2off, 0, AUX_SC1);
    v4u a1 = __builtin_amdgcn_raw_buffer_load_b128(rs, p2off + 16, 0, AUX_SC1);
    v4u am = __builtin_amdgcn_raw_buffer_load_b128(rs, mloff, 0, AUX_SC1);
#pragma clang loop unroll(disable)
    for (unsigned spins = 0; spins < SPIN_CAP; ++spins) {
      const unsigned bad = tagbad(a0, T) | tagbad(a1, T) | tagbad(am, T);
      if (__all(bad == 0)) break;
      asm volatile("" ::: "memory");
      if (bad) {
        a0 = __builtin_amdgcn_raw_buffer_load_b128(rs, p2off, 0, AUX_SC1);
        a1 = __builtin_amdgcn_raw_buffer_load_b128(rs, p2off + 16, 0, AUX_SC1);
        am = __builtin_amdgcn_raw_buffer_load_b128(rs, mloff, 0, AUX_SC1);
      }
    }
    STAMP(7);
    {
      const float m = __uint_as_float(am[0]), l = __uint_as_float(am[2]);
      float M = __builtin_fmaxf(m, dpp<ROR8>(m)); M = x32_max(x16_max(M));
      const float f = __builtin_amdgcn_exp2f(m - M);
      float v[5] = {f * __uint_as_float(a0[0]), f * __uint_as_float(a0[2]), f * __uint_as_float(a1[0]), f * __uint_as_float(a1[2]), f * l};
#pragma unroll
      for (int i = 0; i < 5; ++i) v[i] += dpp<ROR8>(v[i]);
#pragma unroll
      for (int i = 0; i < 5; ++i) v[i] = x16_sum(v[i]);
#pragma unroll
      for (int i = 0; i < 5; ++i) v[i] = x32_sum(v[i]);
      const float inv = __builtin_amdgcn_rcpf(v[4]);
      if (lane < 8) *(v2u*)((u16*)(lds + L_OB) + pw * 32 + 4 * lane) = v2u{pack2(v[0] * inv, v[1] * inv), pack2(v[2] * inv, v[3] * inv)};
    }
    if (arrive(CN(C_O), lane) != 3u) await<0>(CN(C_O), 4u);
    {
      const v4u oa = *(const v4u*)((const u16*)(lds + L_OB) + (c >> 2) * 32 + 8 * g);
      f32x4 d[4];
#pragma unroll
      for (int i = 0; i < 4; ++i) d[i] = mfma32(oa, wof[i], f32x4{0.f, 0.f, 0.f, 0.f});
#pragma unroll
      for (int i = 0; i < 4; ++i) KEEP(d[i]);
      const unsigned off = WS_P3 + ((kg * 2 + cg) * 4 + pw) * 2048 + lane * 16;
      const v4u s0 = {__float_as_uint(fmaf(stg[2], d[0][0], accm[0])), T, __float_as_uint(fmaf(stg[2], d[1][0], accm[1])), T};
      const v4u s1 = {__float_as_uint(fmaf(stg[2], d[2][0], accm[2])), T, __float_as_uint(fmaf(stg[2], d[3][0], accm[3])), T};
      __builtin_amdgcn_raw_buffer_store_b128(s0, rs, off, 0, AUX_SC1);
      __builtin_amdgcn_raw_buffer_store_b128(s1, rs, off + 1024, 0, AUX_SC1);
    }
    PIN();
    signal(CN(C_PUB3), lane);
    STAMP(8);
  }
  STAMP_DUMP();
}

extern "C" void kernel_launch(void* const* d_in, const int* in_sizes, int n_in,
                              void* d_out, int out_size, void* d_ws, size_t ws_size,
                              hipStream_t stream) {
  (void)in_sizes; (void)n_in; (void)out_size; (void)ws_size;
  fused_kernel<<<256, 512, 0, stream>>>(
      (const u16*)d_in[0], (const u16*)d_in[1], (const u16*)d_in[2], (const u16*)d_in[3], (const u16*)d_in[4],
      (const u16*)d_in[6], (const u16*)d_in[7], (const u16*)d_in[8], (const u16*)d_in[9], (const u16*)d_in[10],
      (u16*)d_out, (unsigned char*)d_ws);
}
```

```cpp
#include <hip/hip_runtime.h>
#include <hip/hip_bf16.h>
#include <stdint.h>

#define GAS __attribute__((address_space(1)))
#define LAS __attribute__((address_space(3)))
typedef unsigned short u16;
typedef unsigned v4u __attribute__((ext_vector_type(4)));
typedef unsigned v2u __attribute__((ext_vector_type(2)));
typedef float f32x4 __attribute__((ext_vector_type(4)));
typedef float f32x2 __attribute__((ext_vector_type(2)));
typedef __bf16 bf16x8 __attribute__((ext_vector_type(8)));
typedef __bf16 bf16x2 __attribute__((ext_vector_type(2)));
typedef short s16x4 __attribute__((ext_vector_type(4)));
typedef GAS unsigned gu32;

constexpr int DM = 512, SK = 4096, DH = 128, WINDOW = 1024, FF = 1792, NQ = 4096;
constexpr unsigned WS_QP = 4096;
constexpr unsigned WS_HP = WS_QP + (512u << 10);
constexpr unsigned WS_P2 = WS_HP + (256u << 10);
constexpr unsigned WS_ML = WS_P2 + (1u << 20);
constexpr unsigned WS_P3 = WS_ML + 16384;
constexpr unsigned WS_END = WS_P3 + (2u << 20);
constexpr unsigned SPIN_CAP = 1u << 17;
constexpr unsigned LSPIN_CAP = 1u << 22;
constexpr int AUX_SC1 = 16, AUX_NT = 2;
constexpr float LOG2E = 1.4426950408889634f;
constexpr float SCL2 = 0.08838834764831845f * 1.4426950408889634f;

#define AWIN 6
#define ADLY 0
#define QGATE 8
#define ISSN 4u
#define LATE_FIRST 0
#define DLY1 16
#define DLY2 12
#define DLY3 14
#define SETPRIO(n) __builtin_amdgcn_s_setprio(n)

__device__ __forceinline__ unsigned pack2(float lo, float hi) {
  return __builtin_bit_cast(unsigned, __builtin_convertvector(f32x2{lo, hi}, bf16x2));
}
__device__ __forceinline__ float bfr(float f) { return (float)(__bf16)f; }
__device__ __forceinline__ float lo2f(unsigned u) { return __builtin_bit_cast(float, u << 16); }
__device__ __forceinline__ float hi2f(unsigned u) { return __builtin_bit_cast(float, u & 0xffff0000u); }
__device__ __forceinline__ unsigned perm_lo(unsigned a, unsigned b) { return __builtin_amdgcn_perm(b, a, 0x05040100u); }
__device__ __forceinline__ unsigned perm_hi(unsigned a, unsigned b) { return __builtin_amdgcn_perm(b, a, 0x07060302u); }

template <int CTRL> __device__ __forceinline__ float dpp(float x) {
  return __builtin_bit_cast(float, __builtin_amdgcn_mov_dpp(__builtin_bit_cast(int, x), CTRL, 0xf, 0xf, true));
}
constexpr int XOR1 = 0xB1, XOR2 = 0x4E, ROR8 = 0x128, ROR4 = 0x124;
__device__ __forceinline__ float x16_max(float x) {
  auto s = __builtin_amdgcn_permlane16_swap(__float_as_uint(x), __float_as_uint(x), false, false);
  return __builtin_fmaxf(__uint_as_float(s[0]), __uint_as_float(s[1]));
}
__device__ __forceinline__ float x32_max(float x) {
  auto s = __builtin_amdgcn_permlane32_swap(__float_as_uint(x), __float_as_uint(x), false, false);
  return __builtin_fmaxf(__uint_as_float(s[0]), __uint_as_float(s[1]));
}
__device__ __forceinline__ float x16_sum(float x) {
  auto s = __builtin_amdgcn_permlane16_swap(__float_as_uint(x), __float_as_uint(x), false, false);
  return __uint_as_float(s[0]) + __uint_as_float(s[1]);
}
__device__ __forceinline__ float x32_sum(float x) {
  auto s = __builtin_amdgcn_permlane32_swap(__float_as_uint(x), __float_as_uint(x), false, false);
  return __uint_as_float(s[0]) + __uint_as_float(s[1]);
}
__device__ __forceinline__ float wave_sum(float x) {
  x += dpp<XOR1>(x); x += dpp<XOR2>(x); x += dpp<ROR4>(x); x += dpp<ROR8>(x);
  return x32_sum(x16_sum(x));
}
__device__ __forceinline__ f32x4 mfma32(v4u a, v4u b, f32x4 c) {
  return __builtin_amdgcn_mfma_f32_16x16x32_bf16(__builtin_bit_cast(bf16x8, a), __builtin_bit_cast(bf16x8, b), c, 0, 0, 0);
}
__device__ __forceinline__ f32x4 mfma16(v2u a, v2u b, f32x4 c) {
  return __builtin_amdgcn_mfma_f32_16x16x16bf16_1k(__builtin_bit_cast(s16x4, a), __builtin_bit_cast(s16x4, b), c, 0, 0, 0);
}
#define PIN() __builtin_amdgcn_sched_barrier(0)
#define BAR() do { PIN(); asm volatile("s_waitcnt lgkmcnt(0)" ::: "memory"); __builtin_amdgcn_s_barrier(); asm volatile("" ::: "memory"); PIN(); } while (0)
#define KEEP(x) asm volatile("" : "+v"(x))
#define STAMP(i)
#define STAMP_INIT()
#define STAMP_DUMP()

template <typename T> __device__ __forceinline__ T ldg(const u16* p) { return *(const GAS T*)p; }
template <typename T> __device__ __forceinline__ T ldnt(const u16* p) { return __builtin_nontemporal_load((const GAS T*)p); }

constexpr int L_CNT = 0;
constexpr int L_ST = 16;
constexpr int L_RQ = 32;
constexpr int L_RH = L_RQ + 1024;
constexpr int L_QS = L_RH + 1024;
constexpr int L_HB = L_QS + 2048;
constexpr int L_MO = L_HB + 32;
constexpr int L_ML = L_MO + 2048;
constexpr int L_OB = L_ML + 32;
constexpr int L_QB = L_OB + 64;
constexpr int L_FR = L_QB + 256;
constexpr int L_KS = L_FR + 32;
constexpr int L_END = L_KS + 8192;
enum { C_Q = 0, C_QG, C_QRDY, C_ISS, C_ATT, C_PUB2, C_H, C_HRDY, C_O, C_PUB3, C_FIN, C_ST, C_QPUB };

__device__ __forceinline__ unsigned arrive(unsigned addr, int lane) {
  unsigned old = 0;
  if (lane == 0)
    asm volatile("s_waitcnt lgkmcnt(0)\n\tds_add_rtn_u32 %0, %1, %2\n\ts_waitcnt lgkmcnt(0)" : "=v"(old) : "v"(addr), "v"(1u) : "memory");
  return __builtin_amdgcn_readfirstlane(old);
}
__device__ __forceinline__ void signal(unsigned addr, int lane) {
  if (lane == 0) asm volatile("s_waitcnt lgkmcnt(0)\n\tds_add_u32 %0, %1" :: "v"(addr), "v"(1u) : "memory");
}
template <int SLEEP> __device__ __forceinline__ void await(unsigned addr, unsigned n) {
#pragma clang loop unroll(disable)
  for (unsigned s = 0; s < LSPIN_CAP; ++s) {
    unsigned v;
    asm volatile("ds_read_b32 %0, %1\n\ts_waitcnt lgkmcnt(0)" : "=v"(v) : "v"(addr) : "memory");
    if ((unsigned)__builtin_amdgcn_readfirstlane(v) >= n) break;
    if (SLEEP) __builtin_amdgcn_s_sleep(SLEEP);
  }
}

__device__ __forceinline__ v4u tr8x4(const v2u (&W)[8], int i) {
  v4u f;
#pragma unroll
  for (int jj = 0; jj < 4; ++jj)
    f[jj] = (i & 1) ? perm_hi(W[2 * jj][i >> 1], W[2 * jj + 1][i >> 1]) : perm_lo(W[2 * jj][i >> 1], W[2 * jj + 1][i >> 1]);
  return f;
}
__device__ __forceinline__ unsigned tagbad(v4u v, unsigned T) { return (v[1] ^ T) | (v[3] ^ T); }

__global__ void __launch_bounds__(512, 2)
fused_kernel(const u16* __restrict__ x, const u16* __restrict__ kcache, const u16* __restrict__ vcache,
             const u16* __restrict__ wq, const u16* __restrict__ wo, const u16* __restrict__ w1,
             const u16* __restrict__ b1, const u16* __restrict__ w2, const u16* __restrict__ b2,
             const u16* __restrict__ gw, u16* __restrict__ out, unsigned char* ws) {
  __shared__ __attribute__((aligned(16))) float lds[L_END];
  unsigned long long ta_, tb_;
  asm volatile("s_memrealtime %0\n\ts_waitcnt lgkmcnt(0)" : "=s"(ta_));
  PIN();
  asm volatile("" :: "s"(x), "s"(kcache), "s"(vcache), "s"(wq), "s"(wo), "s"(w1), "s"(b1), "s"(w2), "s"(b2), "s"(gw), "s"(out), "s"(ws));
  PIN();
  asm volatile("s_memrealtime %0\n\ts_waitcnt lgkmcnt(0)" : "=s"(tb_));
  const int tid = threadIdx.x, lane = tid & 63, w = __builtin_amdgcn_readfirstlane(tid >> 6);
  const int g = lane >> 4, c = lane & 15;
  const int bid = blockIdx.x, hk = bid & 7, idx = bid >> 3;
  const int ab = idx >> 3, as = idx & 7;
  const int kgl = idx >> 1, cg = idx & 1, kg = hk * 16 + kgl;
  const __amdgpu_buffer_rsrc_t rs = __builtin_amdgcn_make_buffer_rsrc((void*)ws, 0, (int)WS_END, 0x00020000);
  const unsigned cn = (unsigned)(size_t)(LAS float*)(lds + L_CNT);
#define CN(i) (cn + 4u * (i))
  STAMP_INIT();
  STAMP(0);

  if (w >= 4) {
    const int aw = w - 4;
    BAR();
    unsigned tt;
    {
      const unsigned char* tp = ws;
      tp = lane == 0 ? (const unsigned char*)(out + (bid >> 6) * DM) : tp;
      tp = lane == 1 ? ws + WS_P3 : tp;
      tp = lane == 2 ? ws + WS_END - 64 : tp;
      tp = lane == 3 ? (const unsigned char*)b2 : tp;
      tp = lane == 4 ? (const unsigned char*)(wo + (size_t)(32 * kg) * DM) : tp;
      const int fst = 16 * (14 * hk + (kgl < 14 ? kgl : 13));
      tp = lane == 5 ? (const unsigned char*)(w2 + (size_t)fst * DM + 256 * cg) : tp;
      tp = lane == 12 ? (const unsigned char*)(w2 + (size_t)(fst + 15) * DM + 256 * cg) : tp;
      tp = lane == 13 ? (const unsigned char*)(b1 + fst) : tp;
      tp = lane == 6 ? (const unsigned char*)b1 : tp;
      tp = lane == 7 ? ws + WS_P2 : tp;
      tp = lane == 8 ? (const unsigned char*)(vcache + ((size_t)(ab * 8 + hk) * SK + (SK - WINDOW) + 128 * as + 32 * aw) * DH) : tp;
      tp = lane == 9 ? (const unsigned char*)(wo + (size_t)(32 * kg + 31) * DM + 511) - 2 : tp;
      tp = lane == 10 ? ws + WS_HP : tp;
      tp = lane == 11 ? ws + WS_ML : tp;
      tt = *(const GAS unsigned*)tp;
    }
    PIN();
    if (ADLY) __builtin_amdgcn_s_sleep(ADLY);
    v4u kr[8], vf[8];
    {
      const size_t kvoff = ((size_t)(ab * 8 + hk) * SK + (SK - WINDOW) + 128 * as + 32 * aw) * DH;
      const u16* kp = kcache + kvoff + 8 * lane;
      const u16* vp = vcache + kvoff + 4 * g * DH + 8 * c;
#pragma unroll
      for (int i = 0; i < 8; ++i) kr[i] = ldnt<v4u>(kp + i * 512);
#pragma unroll
      for (int j = 0; j < 8; ++j) vf[j] = ldnt<v4u>(vp + (16 * (j >> 2) + (j & 3)) * DH);
    }
    PIN();
    SETPRIO(2);
    const unsigned T = (unsigned)__builtin_amdgcn_readlane(tt, 63) + 1u;
    STAMP(1);
    v4u kf[8];
    {
      unsigned char* ks_ = (unsigned char*)(lds + L_KS) + aw * 8192;
#pragma unroll
      for (int i = 0; i < 8; ++i) { const int row = 4 * i + g; *(v4u*)(ks_ + row * 256 + ((c ^ (row & 15)) << 4)) = kr[i]; }
      asm volatile("s_waitcnt lgkmcnt(0)" ::: "memory");
#pragma unroll
      for (int t = 0; t < 2; ++t)
#pragma unroll
        for (int ks = 0; ks < 4; ++ks) kf[t * 4 + ks] = *(const v4u*)(ks_ + (16 * t + c) * 256 + (((4 * ks + g) ^ c) << 4));
#pragma unroll
      for (int i = 0; i < 8; ++i) KEEP(kf[i]);
    }
    PIN();
    asm volatile("s_waitcnt vmcnt(%0)" :: "n"(QGATE) : "memory");
    signal(CN(C_ISS), lane);
    STAMP(2);
    await<0>(CN(C_QRDY), 1u);
    STAMP(3);
    v4u pf;
    {
      f32x4 s0 = {0.f, 0.f, 0.f, 0.f}, s1 = {0.f, 0.f, 0.f, 0.f};
      const u16* qb = (const u16*)(lds + L_QB) + (c >> 2) * 128 + 8 * g;
      v4u qf[4];
#pragma unroll
      for (int ks = 0; ks < 4; ++ks) qf[ks] = *(const v4u*)(qb + 32 * ks);
#pragma unroll
      for (int ks = 0; ks < 4; ++ks) {
        s0 = mfma32(kf[ks], qf[ks], s0);
        s1 = mfma32(kf[4 + ks], qf[ks], s1);
      }
      float m = __builtin_fmaxf(__builtin_fmaxf(__builtin_fmaxf(s0[0], s0[1]), __builtin_fmaxf(s0[2], s0[3])),
                                __builtin_fmaxf(__builtin_fmaxf(s1[0], s1[1]), __builtin_fmaxf(s1[2], s1[3])));
      m = x32_max(x16_max(m));
      float p[8];
#pragma unroll
      for (int j = 0; j < 4; ++j) { p[j] = __builtin_amdgcn_exp2f((s0[j] - m) * SCL2); p[4 + j] = __builtin_amdgcn_exp2f((s1[j] - m) * SCL2); }
      pf = v4u{pack2(p[0], p[1]), pack2(p[2], p[3]), pack2(p[4], p[5]), pack2(p[6], p[7])};
      float l = ((p[0] + p[1]) + (p[2] + p[3])) + ((p[4] + p[5]) + (p[6] + p[7]));
      l = x32_sum(x16_sum(l));
      if (g == 0 && (c & 3) == 0) *(f32x2*)(lds + L_ML + (aw * 4 + (c >> 2)) * 2) = f32x2{m * SCL2, l};
      KEEP(pf);
    }
    PIN();
    STAMP(4);
    {
      v4u vt[8];
#pragma unroll
      for (int i = 0; i < 8; ++i)
#pragma unroll
        for (int jj = 0; jj < 4; ++jj)
          vt[i][jj] = (i & 1) ? perm_hi(vf[2 * jj][i >> 1], vf[2 * jj + 1][i >> 1]) : perm_lo(vf[2 * jj][i >> 1], vf[2 * jj + 1][i >> 1]);
      f32x4 o[8];
#pragma unroll
      for (int i = 0; i < 8; ++i) o[i] = mfma32(pf, vt[i], f32x4{0.f, 0.f, 0.f, 0.f});
#pragma unroll
      for (int i = 0; i < 8; ++i) KEEP(o[i]);
      float* mo = lds + L_MO + (aw * 4 + g) * 128 + 8 * c;
      *(f32x4*)(mo) = f32x4{o[0][0], o[1][0], o[2][0], o[3][0]};
      *(f32x4*)(mo + 4) = f32x4{o[4][0], o[5][0], o[6][0], o[7][0]};
    }
    if (arrive(CN(C_ATT), lane) != 3u) await<0>(CN(C_ATT), 4u);
    STAMP(5);
    {
      f32x2 ml[4], ov[4];
#pragma unroll
      for (int u = 0; u < 4; ++u) { ml[u] = *(const f32x2*)(lds + L_ML + (u * 4 + aw) * 2); ov[u] = *(const f32x2*)(lds + L_MO + (u * 4 + aw) * 128 + 2 * lane); }
      const float M = __builtin_fmaxf(__builtin_fmaxf(ml[0][0], ml[1][0]), __builtin_fmaxf(ml[2][0], ml[3][0]));
      float L = 0.f, o0 = 0.f, o1 = 0.f;
#pragma unroll
      for (int u = 0; u < 4; ++u) {
        const float f = __builtin_amdgcn_exp2f(ml[u][0] - M);
        L = fmaf(f, ml[u][1], L); o0 = fmaf(f, ov[u][0], o0); o1 = fmaf(f, ov[u][1], o1);
      }
      const int hg = 4 * hk + aw;
      const v4u pv = {__float_as_uint(o0), T, __float_as_uint(o1), T};
      __builtin_amdgcn_raw_buffer_store_b128(pv, rs, WS_P2 + (((hg * 4 + (lane >> 4)) * 4 + ab) * 8 + as) * 256 + (lane & 15) * 16, 0, AUX_SC1);
      if (lane == 0) {
        const v4u mv = {__float_as_uint(M), T, __float_as_uint(L), T};
        __builtin_amdgcn_raw_buffer_store_b128(mv, rs, WS_ML + ((hg * 4 + ab) * 8 + as) * 16, 0, AUX_SC1);
      }
    }
    PIN();
    signal(CN(C_PUB2), lane);
    STAMP(6);
    const int ob = bid >> 6, ocw = (bid >> 3) & 7, om = (bid >> 1) & 3, oh = bid & 1;
    const int col = 64 * ocw + 4 * (4 * om + (lane & 3)) + 2 * oh;
    const unsigned b2v = ldg<unsigned>(b2 + col);
    const unsigned off = WS_P3 + ((aw * 32 + (lane >> 2)) * 8 + ocw) * 2048 + oh * 1024 + (ob * 16 + 4 * om + (lane & 3)) * 16;
    await<2>(CN(C_PUB3), 4u);
    __builtin_amdgcn_s_sleep(DLY3);
    STAMP(8);
    v4u pv0 = __builtin_amdgcn_raw_buffer_load_b128(rs, off, 0, AUX_SC1);
    v4u pv1 = __builtin_amdgcn_raw_buffer_load_b128(rs, off + 16 * 8 * 2048, 0, AUX_SC1);
#pragma clang loop unroll(disable)
    for (unsigned spins = 0; spins < SPIN_CAP; ++spins) {
      const unsigned bad0 = tagbad(pv0, T), bad1 = tagbad(pv1, T);
      if (__all((bad0 | bad1) == 0)) break;
      asm volatile("" ::: "memory");
      if (bad0) pv0 = __builtin_amdgcn_raw_buffer_load_b128(rs, off, 0, AUX_SC1);
      if (bad1) pv1 = __builtin_amdgcn_raw_buffer_load_b128(rs, off + 16 * 8 * 2048, 0, AUX_SC1);
    }
    STAMP(9);
    float sa = __uint_as_float(pv0[0]) + __uint_as_float(pv1[0]), sb = __uint_as_float(pv0[2]) + __uint_as_float(pv1[2]);
    sa += dpp<ROR4>(sa); sb += dpp<ROR4>(sb); sa += dpp<ROR8>(sa); sb += dpp<ROR8>(sb);
    sa = x16_sum(sa); sb = x16_sum(sb); sa = x32_sum(sa); sb = x32_sum(sb);
    if (lane < 4) *(f32x2*)(lds + L_FR + aw * 8 + 2 * lane) = f32x2{sa, sb};
    if (arrive(CN(C_FIN), lane) == 3u) {
      if (lane < 4) {
        const float omg = lds[L_ST + 4 * ob + 3];
        f32x2 t = *(const f32x2*)(lds + L_FR + 2 * lane);
#pragma unroll
        for (int u = 1; u < 4; ++u) t += *(const f32x2*)(lds + L_FR + u * 8 + 2 * lane);
        *(GAS unsigned*)(out + ob * DM + col) = pack2(fmaf(omg, lo2f(b2v), t[0]), fmaf(omg, hi2f(b2v), t[1]));
      }
      if (bid == 0) { const unsigned long long d_ = (tb_ - ta_) * 4; const unsigned long long n_ = __builtin_amdgcn_s_memrealtime(); while (__builtin_amdgcn_s_memrealtime() - n_ < d_) __builtin_amdgcn_s_sleep(1); }
      if (bid == 0 && lane == 0) __hip_atomic_store((gu32*)ws, T, __ATOMIC_RELAXED, __HIP_MEMORY_SCOPE_AGENT);
    }
  } else {
    const int pw = w;
    SETPRIO(3);
    const int kq = idx & 3, cq = idx >> 2;
    const bool has_ht = idx < 14;
    const int th = 14 * hk + idx;
    const int qrow = 128 * kq + 32 * pw + 8 * g, hrow = 128 * (th & 3) + 32 * pw + 8 * g;
    v2u wv[8], w1v[8];
    {
      const u16* p = wq + (size_t)qrow * NQ + hk * 512 + 64 * cq + 4 * c;
#pragma unroll
      for (int j = 0; j < 8; ++j) wv[j] = ldnt<v2u>(p + j * NQ);
    }
    const v4u xa = ldg<v4u>(x + (c >> 2) * DM + qrow);
    const unsigned ev = __builtin_amdgcn_raw_buffer_load_b32(rs, 0, 0, 0);
    PIN();
    const v4u xs = ldg<v4u>(x + pw * DM + 8 * lane);
    const v4u gwv = ldg<v4u>(gw + 8 * lane);
    const v4u xh = ldg<v4u>(x + (c >> 2) * DM + hrow);
    {
      const __amdgpu_buffer_rsrc_t rw1 = __builtin_amdgcn_make_buffer_rsrc((void*)w1, 0, has_ht ? DM * FF * 2 : 0, 0x00020000);
      const int o = (hrow * FF + 64 * (th >> 2) + 4 * c) * 2;
#pragma unroll
      for (int j = 0; j < 8; ++j) w1v[j] = __builtin_amdgcn_raw_buffer_load_b64(rw1, o + j * FF * 2, 0, AUX_NT);
    }
    if (tid < 16) ((LAS unsigned*)(lds + L_CNT))[tid] = 0u;
    BAR();
    PIN();
    unsigned T;
    {
      f32x4 d[4];
#pragma unroll
      for (int i = 0; i < 4; ++i) d[i] = mfma32(xa, tr8x4(wv, i), f32x4{0.f, 0.f, 0.f, 0.f});
      *(f32x4*)(lds + L_RQ + (pw * 64 + lane) * 4) = f32x4{d[0][0], d[1][0], d[2][0], d[3][0]};
      T = (unsigned)__builtin_amdgcn_readfirstlane(ev) + 1u;
      if (arrive(CN(C_Q), lane) == 3u) {
        f32x4 a = *(const f32x4*)(lds + L_RQ + lane * 4);
#pragma unroll
        for (int p = 1; p < 4; ++p) a += *(const f32x4*)(lds + L_RQ + (p * 64 + lane) * 4);
        const unsigned off = WS_QP + ((hk * 8 + cq) * 4 + kq) * 2048 + lane * 16;
        __builtin_amdgcn_raw_buffer_store_b128(v4u{__float_as_uint(a[0]), T, __float_as_uint(a[1]), T}, rs, off, 0, AUX_SC1);
        __builtin_amdgcn_raw_buffer_store_b128(v4u{__float_as_uint(a[2]), T, __float_as_uint(a[3]), T}, rs, off + 1024, 0, AUX_SC1);
      }
    }
    PIN();
    STAMP(1);
    {
      float xv[8], gv[8];
#pragma unroll
      for (int k = 0; k < 4; ++k) { xv[2 * k] = lo2f(xs[k]); xv[2 * k + 1] = hi2f(xs[k]); gv[2 * k] = lo2f(gwv[k]); gv[2 * k + 1] = hi2f(gwv[k]); }
      float s = 0.f, gs = 0.f;
#pragma unroll
      for (int k = 0; k < 8; ++k) { s += xv[k]; gs += bfr(xv[k] * gv[k]); }
      s = wave_sum(s); gs = wave_sum(gs);
      const float mean = s * (1.0f / DM);
      float vs = 0.f;
#pragma unroll
      for (int k = 0; k < 8; ++k) { const float d = xv[k] - mean; vs = fmaf(d, d, vs); }
      vs = wave_sum(vs);
      if (lane == 0) {
        const float var_b = bfr(vs * (1.0f / DM));
        const float den = bfr(sqrtf(bfr(var_b + 1e-5f)));
        const float gt = bfr(1.0f / (1.0f + __expf(-bfr(gs))));
        *(f32x4*)(lds + L_ST + 4 * pw) = f32x4{bfr(mean), den, gt, bfr(1.0f - gt)};
      }
      if (arrive(CN(C_ST), lane) != 3u) await<0>(CN(C_ST), 4u);
    }
    if (has_ht) {
      const f32x4 st = *(const f32x4*)(lds + L_ST + 4 * (c >> 2));
      v4u ya;
#pragma unroll
      for (int k = 0; k < 4; ++k) ya[k] = pack2(bfr(lo2f(xh[k]) - st[0]) / st[1], bfr(hi2f(xh[k]) - st[0]) / st[1]);
      f32x4 d[4];
#pragma unroll
      for (int i = 0; i < 4; ++i) d[i] = mfma32(ya, tr8x4(w1v, i), f32x4{0.f, 0.f, 0.f, 0.f});
      *(f32x4*)(lds + L_RH + (pw * 64 + lane) * 4) = f32x4{d[0][0], d[1][0], d[2][0], d[3][0]};
      if (arrive(CN(C_H), lane) == 3u) {
        f32x4 a = *(const f32x4*)(lds + L_RH + lane * 4);
#pragma unroll
        for (int p = 1; p < 4; ++p) a += *(const f32x4*)(lds + L_RH + (p * 64 + lane) * 4);
        const unsigned off = WS_HP + th * 2048 + lane * 16;
        __builtin_amdgcn_raw_buffer_store_b128(v4u{__float_as_uint(a[0]), T, __float_as_uint(a[1]), T}, rs, off, 0, AUX_SC1);
        __builtin_amdgcn_raw_buffer_store_b128(v4u{__float_as_uint(a[2]), T, __float_as_uint(a[3]), T}, rs, off + 1024, 0, AUX_SC1);
      }
    }
    f32x4 stg = *(const f32x4*)(lds + L_ST + 4 * g);
    KEEP(stg);
    PIN();
    STAMP(2);
    await<2>(CN(C_ISS), ISSN);
    const bool has_mlp = kgl < 14;
    const int fs = 14 * hk + kgl, f0 = 16 * fs;
    v2u wov[8], w2v[4];
    {
      const unsigned qoff = WS_QP + ((hk * 8 + (lane >> 5)) * 4 + pw) * 2048 + ((lane >> 4) & 1) * 1024 + (16 * ab + c) * 16;
      v4u qv[4];
#pragma unroll
      for (int i = 0; i < 4; ++i) qv[i] = __builtin_amdgcn_raw_buffer_load_b128(rs, qoff + i * 16384, 0, AUX_SC1);
      PIN();
      if (LATE_FIRST) {
        const __amdgpu_buffer_rsrc_t rw2 = __builtin_amdgcn_make_buffer_rsrc((void*)w2, 0, has_mlp ? DM * FF * 2 : 0, 0x00020000);
        const u16* p = wo + (size_t)(32 * kg + 8 * g) * DM + 256 * cg + 64 * pw + 4 * c;
        const int o2 = ((f0 + 4 * g) * DM + 256 * cg + 64 * pw + 4 * c) * 2;
#pragma unroll
        for (int j = 0; j < 8; ++j) wov[j] = ldnt<v2u>(p + j * DM);
#pragma unroll
        for (int j = 0; j < 4; ++j) w2v[j] = __builtin_amdgcn_raw_buffer_load_b64(rw2, o2 + j * DM * 2, 0, AUX_NT);
      }
      PIN();
      unsigned bad0 = tagbad(qv[0], T) | tagbad(qv[1], T) | tagbad(qv[2], T) | tagbad(qv[3], T);
      if (!__all(bad0 == 0)) {
#pragma clang loop unroll(disable)
        for (unsigned spins = 0; spins < SPIN_CAP; ++spins) {
          unsigned bad[4];
#pragma unroll
          for (int i = 0; i < 4; ++i) bad[i] = tagbad(qv[i], T);
          if (__all((bad[0] | bad[1] | bad[2] | bad[3]) == 0)) break;
          asm volatile("" ::: "memory");
#pragma unroll
          for (int i = 0; i < 4; ++i) if (bad[i]) qv[i] = __builtin_amdgcn_raw_buffer_load_b128(rs, qoff + i * 16384, 0, AUX_SC1);
        }
      }
      float* qs = lds + L_QS + (pw * 64 + lane) * 8;
      *(v4u*)(qs) = v4u{qv[0][0], qv[0][2], qv[1][0], qv[1][2]};
      *(v4u*)(qs + 4) = v4u{qv[2][0], qv[2][2], qv[3][0], qv[3][2]};
      if (arrive(CN(C_QG), lane) == 3u) {
        f32x4 a0 = *(const f32x4*)(lds + L_QS + lane * 8), a1 = *(const f32x4*)(lds + L_QS + lane * 8 + 4);
#pragma unroll
        for (int p = 1; p < 4; ++p) { a0 += *(const f32x4*)(lds + L_QS + (p * 64 + lane) * 8); a1 += *(const f32x4*)(lds + L_QS + (p * 64 + lane) * 8 + 4); }
        unsigned* qb = (unsigned*)(lds + L_QB) + 32 * (lane >> 5) + 2 * c + ((lane >> 4) & 1);
        qb[0] = pack2(a0[0], a0[1]); qb[64] = pack2(a0[2], a0[3]); qb[128] = pack2(a1[0], a1[1]); qb[192] = pack2(a1[2], a1[3]);
        signal(CN(C_QRDY), lane);
      }
    }
    STAMP(3);
    if (!LATE_FIRST) {
      const __amdgpu_buffer_rsrc_t rw2 = __builtin_amdgcn_make_buffer_rsrc((void*)w2, 0, has_mlp ? DM * FF * 2 : 0, 0x00020000);
      const u16* p = wo + (size_t)(32 * kg + 8 * g) * DM + 256 * cg + 64 * pw + 4 * c;
      const int o2 = ((f0 + 4 * g) * DM + 256 * cg + 64 * pw + 4 * c) * 2;
#pragma unroll
      for (int j = 0; j < 8; ++j) wov[j] = ldnt<v2u>(p + j * DM);
#pragma unroll
      for (int j = 0; j < 4; ++j) w2v[j] = __builtin_amdgcn_raw_buffer_load_b64(rw2, o2 + j * DM * 2, 0, AUX_NT);
    }
    PIN();
    SETPRIO(3);
    STAMP(4);
    if (pw == 3) {
      float hv = 0.f;
      if (has_mlp) {
        const float bv = lo2f((unsigned)ldg<u16>(b1 + f0 + 4 * (c & 3) + g));
        const unsigned hoff = WS_HP + ((fs >> 2) * 4 + g) * 2048 + (16 * (c >> 2) + 4 * (fs & 3) + (c & 3)) * 16;
        v4u h0 = __builtin_amdgcn_raw_buffer_load_b128(rs, hoff, 0, AUX_SC1);
        v4u h1 = __builtin_amdgcn_raw_buffer_load_b128(rs, hoff + 1024, 0, AUX_SC1);
#pragma clang loop unroll(disable)
        for (unsigned spins = 0; spins < SPIN_CAP; ++spins) {
          const unsigned bad = tagbad(h0, T) | tagbad(h1, T);
          if (__all(bad == 0)) break;
          asm volatile("" ::: "memory");
          if (bad) {
            h0 = __builtin_amdgcn_raw_buffer_load_b128(rs, hoff, 0, AUX_SC1);
            h1 = __builtin_amdgcn_raw_buffer_load_b128(rs, hoff + 1024, 0, AUX_SC1);
          }
        }
        float a[4] = {__uint_as_float(h0[0]), __uint_as_float(h0[2]), __uint_as_float(h1[0]), __uint_as_float(h1[2])};
#pragma unroll
        for (int i = 0; i < 4; ++i) a[i] = x32_sum(x16_sum(a[i]));
        const float av = g == 0 ? a[0] : g == 1 ? a[1] : g == 2 ? a[2] : a[3];
        const float u = bfr(bfr(av) + bv);
        const float inner = 0.7978845608028654f * (u + 0.044715f * u * u * u);
        hv = 0.5f * u * (1.0f + tanhf(inner));
      }
      ((u16*)(lds + L_HB))[(c >> 2) * 16 + 4 * (c & 3) + g] = (u16)pack2(hv, 0.f);
      signal(CN(C_HRDY), lane);
    }
    v4u wof[4];
    float accm[4];
    {
      v2u w2f[4];
#pragma unroll
      for (int i = 0; i < 4; ++i) {
        wof[i] = tr8x4(wov, i);
#pragma unroll
        for (int jj = 0; jj < 2; ++jj)
          w2f[i][jj] = (i & 1) ? perm_hi(w2v[2 * jj][i >> 1], w2v[2 * jj + 1][i >> 1]) : perm_lo(w2v[2 * jj][i >> 1], w2v[2 * jj + 1][i >> 1]);
        KEEP(wof[i]);
      }
      await<1>(CN(C_HRDY), 1u);
      const v2u ha = *(const v2u*)((const u16*)(lds + L_HB) + (c >> 2) * 16 + 4 * g);
#pragma unroll
      for (int i = 0; i < 4; ++i) {
        const f32x4 e = mfma16(ha, w2f[i], f32x4{0.f, 0.f, 0.f, 0.f});
        accm[i] = stg[3] * e[0];
        KEEP(accm[i]);
      }
    }
    PIN();
    STAMP(5);
    const int h2 = 4 * hk + (kgl >> 2);
    const unsigned p2off = WS_P2 + (((h2 * 4 + (kgl & 3)) * 4 + pw) * 8 + (lane >> 3)) * 256 + (lane & 7) * 32;
    const unsigned mloff = WS_ML + ((h2 * 4 + pw) * 8 + (lane >> 3)) * 16;
    await<1>(CN(C_PUB2), 4u);
    __builtin_amdgcn_s_sleep(DLY2);
    STAMP(6);
    v4u a0 = __builtin_amdgcn_raw_buffer_load_b128(rs, p2off, 0, AUX_SC1);
    v4u a1 = __builtin_amdgcn_raw_buffer_load_b128(rs, p2off + 16, 0, AUX_SC1);
    v4u am = __builtin_amdgcn_raw_buffer_load_b128(rs, mloff, 0, AUX_SC1);
#pragma clang loop unroll(disable)
    for (unsigned spins = 0; spins < SPIN_CAP; ++spins) {
      const unsigned bad = tagbad(a0, T) | tagbad(a1, T) | tagbad(am, T);
      if (__all(bad == 0)) break;
      asm volatile("" ::: "memory");
      if (bad) {
        a0 = __builtin_amdgcn_raw_buffer_load_b128(rs, p2off, 0, AUX_SC1);
        a1 = __builtin_amdgcn_raw_buffer_load_b128(rs, p2off + 16, 0, AUX_SC1);
        am = __builtin_amdgcn_raw_buffer_load_b128(rs, mloff, 0, AUX_SC1);
      }
    }
    STAMP(7);
    {
      const float m = __uint_as_float(am[0]), l = __uint_as_float(am[2]);
      float M = __builtin_fmaxf(m, dpp<ROR8>(m)); M = x32_max(x16_max(M));
      const float f = __builtin_amdgcn_exp2f(m - M);
      float v[5] = {f * __uint_as_float(a0[0]), f * __uint_as_float(a0[2]), f * __uint_as_float(a1[0]), f * __uint_as_float(a1[2]), f * l};
#pragma unroll
      for (int i = 0; i < 5; ++i) v[i] += dpp<ROR8>(v[i]);
#pragma unroll
      for (int i = 0; i < 5; ++i) v[i] = x16_sum(v[i]);
#pragma unroll
      for (int i = 0; i < 5; ++i) v[i] = x32_sum(v[i]);
      const float inv = __builtin_amdgcn_rcpf(v[4]);
      if (lane < 8) *(v2u*)((u16*)(lds + L_OB) + pw * 32 + 4 * lane) = v2u{pack2(v[0] * inv, v[1] * inv), pack2(v[2] * inv, v[3] * inv)};
    }
    if (arrive(CN(C_O), lane) != 3u) await<0>(CN(C_O), 4u);
    {
      const v4u oa = *(const v4u*)((const u16*)(lds + L_OB) + (c >> 2) * 32 + 8 * g);
      f32x4 d[4];
#pragma unroll
      for (int i = 0; i < 4; ++i) d[i] = mfma32(oa, wof[i], f32x4{0.f, 0.f, 0.f, 0.f});
#pragma unroll
      for (int i = 0; i < 4; ++i) KEEP(d[i]);
      const unsigned off = WS_P3 + ((kg * 2 + cg) * 4 + pw) * 2048 + lane * 16;
      const v4u s0 = {__float_as_uint(fmaf(stg[2], d[0][0], accm[0])), T, __float_as_uint(fmaf(stg[2], d[1][0], accm[1])), T};
      const v4u s1 = {__float_as_uint(fmaf(stg[2], d[2][0], accm[2])), T, __float_as_uint(fmaf(stg[2], d[3][0], accm[3])), T};
      __builtin_amdgcn_raw_buffer_store_b128(s0, rs, off, 0, AUX_SC1);
      __builtin_amdgcn_raw_buffer_store_b128(s1, rs, off + 1024, 0, AUX_SC1);
    }
    PIN();
    signal(CN(C_PUB3), lane);
    STAMP(8);
  }
  STAMP_DUMP();
}

extern "C" void kernel_launch(void* const* d_in, const int* in_sizes, int n_in,
                              void* d_out, int out_size, void* d_ws, size_t ws_size,
                              hipStream_t stream) {
  (void)in_sizes; (void)n_in; (void)out_size; (void)ws_size;
  fused_kernel<<<256, 512, 0, stream>>>(
      (const u16*)d_in[0], (const u16*)d_in[1], (const u16*)d_in[2], (const u16*)d_in[3], (const u16*)d_in[4],
      (const u16*)d_in[6], (const u16*)d_in[7], (const u16*)d_in[8], (const u16*)d_in[9], (const u16*)d_in[10],
      (u16*)d_out, (unsigned char*)d_ws);
}
```

```cpp
#include <hip/hip_runtime.h>
#include <hip/hip_bf16.h>
#include <stdint.h>

#define GAS __attribute__((address_space(1)))
#define LAS __attribute__((address_space(3)))
typedef unsigned short u16;
typedef unsigned v4u __attribute__((ext_vector_type(4)));
typedef unsigned v2u __attribute__((ext_vector_type(2)));
typedef float f32x4 __attribute__((ext_vector_type(4)));
typedef float f32x2 __attribute__((ext_vector_type(2)));
typedef __bf16 bf16x8 __attribute__((ext_vector_type(8)));
typedef __bf16 bf16x2 __attribute__((ext_vector_type(2)));
typedef short s16x4 __attribute__((ext_vector_type(4)));
typedef GAS unsigned gu32;

constexpr int DM = 512, SK = 4096, DH = 128, WINDOW = 1024, FF = 1792, NQ = 4096;
constexpr unsigned WS_QP = 4096;
constexpr unsigned WS_HP = WS_QP + (512u << 10);
constexpr unsigned WS_P2 = WS_HP + (256u << 10);
constexpr unsigned WS_ML = WS_P2 + (1u << 20);
constexpr unsigned WS_P3 = WS_ML + 16384;
constexpr unsigned WS_END = WS_P3 + (2u << 20);
constexpr unsigned SPIN_CAP = 1u << 17;
constexpr unsigned LSPIN_CAP = 1u << 22;
constexpr int AUX_SC1 = 16, AUX_NT = 2;
constexpr float LOG2E = 1.4426950408889634f;
constexpr float SCL2 = 0.08838834764831845f * 1.4426950408889634f;

#define AWIN 6
#define ADLY 0
#define QGATE 8
#define ISSN 4u
#define LATE_FIRST 0
#define DLY1 16
#define DLY2 12
#define DLY3 14
#define SETPRIO(n) __builtin_amdgcn_s_setprio(n)

__device__ __forceinline__ unsigned pack2(float lo, float hi) {
  return __builtin_bit_cast(unsigned, __builtin_convertvector(f32x2{lo, hi}, bf16x2));
}
__device__ __forceinline__ float bfr(float f) { return (float)(__bf16)f; }
__device__ __forceinline__ float lo2f(unsigned u) { return __builtin_bit_cast(float, u << 16); }
__device__ __forceinline__ float hi2f(unsigned u) { return __builtin_bit_cast(float, u & 0xffff0000u); }
__device__ __forceinline__ unsigned perm_lo(unsigned a, unsigned b) { return __builtin_amdgcn_perm(b, a, 0x05040100u); }
__device__ __forceinline__ unsigned perm_hi(unsigned a, unsigned b) { return __builtin_amdgcn_perm(b, a, 0x07060302u); }

template <int CTRL> __device__ __forceinline__ float dpp(float x) {
  return __builtin_bit_cast(float, __builtin_amdgcn_mov_dpp(__builtin_bit_cast(int, x), CTRL, 0xf, 0xf, true));
}
constexpr int XOR1 = 0xB1, XOR2 = 0x4E, ROR8 = 0x128, ROR4 = 0x124;
__device__ __forceinline__ float x16_max(float x) {
  auto s = __builtin_amdgcn_permlane16_swap(__float_as_uint(x), __float_as_uint(x), false, false);
  return __builtin_fmaxf(__uint_as_float(s[0]), __uint_as_float(s[1]));
}
__device__ __forceinline__ float x32_max(float x) {
  auto s = __builtin_amdgcn_permlane32_swap(__float_as_uint(x), __float_as_uint(x), false, false);
  return __builtin_fmaxf(__uint_as_float(s[0]), __uint_as_float(s[1]));
}
__device__ __forceinline__ float x16_sum(float x) {
  auto s = __builtin_amdgcn_permlane16_swap(__float_as_uint(x), __float_as_uint(x), false, false);
  return __uint_as_float(s[0]) + __uint_as_float(s[1]);
}
__device__ __forceinline__ float x32_sum(float x) {
  auto s = __builtin_amdgcn_permlane32_swap(__float_as_uint(x), __float_as_uint(x), false, false);
  return __uint_as_float(s[0]) + __uint_as_float(s[1]);
}
__device__ __forceinline__ float wave_sum(float x) {
  x += dpp<XOR1>(x); x += dpp<XOR2>(x); x += dpp<ROR4>(x); x += dpp<ROR8>(x);
  return x32_sum(x16_sum(x));
}
__device__ __forceinline__ f32x4 mfma32(v4u a, v4u b, f32x4 c) {
  return __builtin_amdgcn_mfma_f32_16x16x32_bf16(__builtin_bit_cast(bf16x8, a), __builtin_bit_cast(bf16x8, b), c, 0, 0, 0);
}
__device__ __forceinline__ f32x4 mfma16(v2u a, v2u b, f32x4 c) {
  return __builtin_amdgcn_mfma_f32_16x16x16bf16_1k(__builtin_bit_cast(s16x4, a), __builtin_bit_cast(s16x4, b), c, 0, 0, 0);
}
#define PIN() __builtin_amdgcn_sched_barrier(0)
#define BAR() do { PIN(); asm volatile("s_waitcnt lgkmcnt(0)" ::: "memory"); __builtin_amdgcn_s_barrier(); asm volatile("" ::: "memory"); PIN(); } while (0)
#define KEEP(x) asm volatile("" : "+v"(x))
#define STAMP(i)
#define STAMP_INIT()
#define STAMP_DUMP()

template <typename T> __device__ __forceinline__ T ldg(const u16* p) { return *(const GAS T*)p; }
template <typename T> __device__ __forceinline__ T ldnt(const u16* p) { return __builtin_nontemporal_load((const GAS T*)p); }

constexpr int L_CNT = 0;
constexpr int L_ST = 16;
constexpr int L_RQ = 32;
constexpr int L_RH = L_RQ + 1024;
constexpr int L_QS = L_RH + 1024;
constexpr int L_HB = L_QS + 2048;
constexpr int L_MO = L_HB + 32;
constexpr int L_ML = L_MO + 2048;
constexpr int L_OB = L_ML + 32;
constexpr int L_QB = L_OB + 64;
constexpr int L_FR = L_QB + 256;
constexpr int L_KS = L_FR + 32;
constexpr int L_END = L_KS + 8192;
enum { C_Q = 0, C_QG, C_QRDY, C_ISS, C_ATT, C_PUB2, C_H, C_HRDY, C_O, C_PUB3, C_FIN, C_ST, C_QPUB };

__device__ __forceinline__ unsigned arrive(unsigned addr, int lane) {
  unsigned old = 0;
  if (lane == 0)
    asm volatile("s_waitcnt lgkmcnt(0)\n\tds_add_rtn_u32 %0, %1, %2\n\ts_waitcnt lgkmcnt(0)" : "=v"(old) : "v"(addr), "v"(1u) : "memory");
  return __builtin_amdgcn_readfirstlane(old);
}
__device__ __forceinline__ void signal(unsigned addr, int lane) {
  if (lane == 0) asm volatile("s_waitcnt lgkmcnt(0)\n\tds_add_u32 %0, %1" :: "v"(addr), "v"(1u) : "memory");
}
template <int SLEEP> __device__ __forceinline__ void await(unsigned addr, unsigned n) {
#pragma clang loop unroll(disable)
  for (unsigned s = 0; s < LSPIN_CAP; ++s) {
    unsigned v;
    asm volatile("ds_read_b32 %0, %1\n\ts_waitcnt lgkmcnt(0)" : "=v"(v) : "v"(addr) : "memory");
    if ((unsigned)__builtin_amdgcn_readfirstlane(v) >= n) break;
    if (SLEEP) __builtin_amdgcn_s_sleep(SLEEP);
  }
}

__device__ __forceinline__ v4u tr8x4(const v2u (&W)[8], int i) {
  v4u f;
#pragma unroll
  for (int jj = 0; jj < 4; ++jj)
    f[jj] = (i & 1) ? perm_hi(W[2 * jj][i >> 1], W[2 * jj + 1][i >> 1]) : perm_lo(W[2 * jj][i >> 1], W[2 * jj + 1][i >> 1]);
  return f;
}
__device__ __forceinline__ unsigned tagbad(v4u v, unsigned T) { return (v[1] ^ T) | (v[3] ^ T); }

__global__ void __launch_bounds__(512, 2)
fused_kernel(const u16* __restrict__ x, const u16* __restrict__ kcache, const u16* __restrict__ vcache,
             const u16* __restrict__ wq, const u16* __restrict__ wo, const u16* __restrict__ w1,
             const u16* __restrict__ b1, const u16* __restrict__ w2, const u16* __restrict__ b2,
             const u16* __restrict__ gw, u16* __restrict__ out, unsigned char* ws) {
  __shared__ __attribute__((aligned(16))) float lds[L_END];
  asm volatile("" :: "s"(x), "s"(kcache), "s"(vcache), "s"(wq), "s"(wo), "s"(w1), "s"(b1), "s"(w2), "s"(b2), "s"(gw), "s"(out), "s"(ws));
  const int tid = threadIdx.x, lane = tid & 63, w = __builtin_amdgcn_readfirstlane(tid >> 6);
  const int g = lane >> 4, c = lane & 15;
  const int bid = blockIdx.x, hk = bid & 7, idx = bid >> 3;
  const int ab = idx >> 3, as = idx & 7;
  const int kgl = idx >> 1, cg = idx & 1, kg = hk * 16 + kgl;
  const __amdgpu_buffer_rsrc_t rs = __builtin_amdgcn_make_buffer_rsrc((void*)ws, 0, (int)WS_END, 0x00020000);
  const unsigned cn = (unsigned)(size_t)(LAS float*)(lds + L_CNT);
#define CN(i) (cn + 4u * (i))
  STAMP_INIT();
  STAMP(0);

  if (w >= 4) {
    const int aw = w - 4;
    BAR();
    unsigned tt;
    {
      const unsigned char* tp = ws;
      tp = lane == 0 ? (const unsigned char*)(out + (bid >> 6) * DM) : tp;
      tp = lane == 1 ? ws + WS_P3 : tp;
      tp = lane == 2 ? ws + WS_END - 64 : tp;
      tp = lane == 3 ? (const unsigned char*)b2 : tp;
      tp = lane == 4 ? (const unsigned char*)(wo + (size_t)(32 * kg) * DM) : tp;
      const int fst = 16 * (14 * hk + (kgl < 14 ? kgl : 13));
      tp = lane == 5 ? (const unsigned char*)(w2 + (size_t)fst * DM + 256 * cg) : tp;
      tp = lane == 12 ? (const unsigned char*)(w2 + (size_t)(fst + 15) * DM + 256 * cg) : tp;
      tp = lane == 13 ? (const unsigned char*)(b1 + fst) : tp;
      tp = lane == 6 ? (const unsigned char*)b1 : tp;
      tp = lane == 7 ? ws + WS_P2 : tp;
      tp = lane == 8 ? (const unsigned char*)(vcache + ((size_t)(ab * 8 + hk) * SK + (SK - WINDOW) + 128 * as + 32 * aw) * DH) : tp;
      tp = lane == 9 ? (const unsigned char*)(wo + (size_t)(32 * kg + 31) * DM + 511) - 2 : tp;
      tp = lane == 10 ? ws + WS_HP : tp;
      tp = lane == 11 ? ws + WS_ML : tp;
      tt = *(const GAS unsigned*)tp;
    }
    PIN();
    if (ADLY) __builtin_amdgcn_s_sleep(ADLY);
    v4u kr[8], vf[8];
    {
      const size_t kvoff = ((size_t)(ab * 8 + hk) * SK + (SK - WINDOW) + 128 * as + 32 * aw) * DH;
      const u16* kp = kcache + kvoff + 8 * lane;
      const u16* vp = vcache + kvoff + 4 * g * DH + 8 * c;
#pragma unroll
      for (int i = 0; i < 8; ++i) kr[i] = ldnt<v4u>(kp + i * 512);
#pragma unroll
      for (int j = 0; j < 8; ++j) vf[j] = ldnt<v4u>(vp + (16 * (j >> 2) + (j & 3)) * DH);
    }
    PIN();
    SETPRIO(2);
    const unsigned T = (unsigned)__builtin_amdgcn_readlane(tt, 63) + 1u;
    STAMP(1);
    v4u kf[8];
    {
      unsigned char* ks_ = (unsigned char*)(lds + L_KS) + aw * 8192;
#pragma unroll
      for (int i = 0; i < 8; ++i) { const int row = 4 * i + g; *(v4u*)(ks_ + row * 256 + ((c ^ (row & 15)) << 4)) = kr[i]; }
      asm volatile("s_waitcnt lgkmcnt(0)" ::: "memory");
#pragma unroll
      for (int t = 0; t < 2; ++t)
#pragma unroll
        for (int ks = 0; ks < 4; ++ks) kf[t * 4 + ks] = *(const v4u*)(ks_ + (16 * t + c) * 256 + (((4 * ks + g) ^ c) << 4));
#pragma unroll
      for (int i = 0; i < 8; ++i) KEEP(kf[i]);
    }
    PIN();
    asm volatile("s_waitcnt vmcnt(%0)" :: "n"(QGATE) : "memory");
    signal(CN(C_ISS), lane);
    STAMP(2);
    await<0>(CN(C_QRDY), 1u);
    STAMP(3);
    v4u pf;
    {
      f32x4 s0 = {0.f, 0.f, 0.f, 0.f}, s1 = {0.f, 0.f, 0.f, 0.f};
      const u16* qb = (const u16*)(lds + L_QB) + (c >> 2) * 128 + 8 * g;
      v4u qf[4];
#pragma unroll
      for (int ks = 0; ks < 4; ++ks) qf[ks] = *(const v4u*)(qb + 32 * ks);
#pragma unroll
      for (int ks = 0; ks < 4; ++ks) {
        s0 = mfma32(kf[ks], qf[ks], s0);
        s1 = mfma32(kf[4 + ks], qf[ks], s1);
      }
      float m = __builtin_fmaxf(__builtin_fmaxf(__builtin_fmaxf(s0[0], s0[1]), __builtin_fmaxf(s0[2], s0[3])),
                                __builtin_fmaxf(__builtin_fmaxf(s1[0], s1[1]), __builtin_fmaxf(s1[2], s1[3])));
      m = x32_max(x16_max(m));
      float p[8];
#pragma unroll
      for (int j = 0; j < 4; ++j) { p[j] = __builtin_amdgcn_exp2f((s0[j] - m) * SCL2); p[4 + j] = __builtin_amdgcn_exp2f((s1[j] - m) * SCL2); }
      pf = v4u{pack2(p[0], p[1]), pack2(p[2], p[3]), pack2(p[4], p[5]), pack2(p[6], p[7])};
      float l = ((p[0] + p[1]) + (p[2] + p[3])) + ((p[4] + p[5]) + (p[6] + p[7]));
      l = x32_sum(x16_sum(l));
      if (g == 0 && (c & 3) == 0) *(f32x2*)(lds + L_ML + (aw * 4 + (c >> 2)) * 2) = f32x2{m * SCL2, l};
      KEEP(pf);
    }
    PIN();
    STAMP(4);
    {
      v4u vt[8];
#pragma unroll
      for (int i = 0; i < 8; ++i)
#pragma unroll
        for (int jj = 0; jj < 4; ++jj)
          vt[i][jj] = (i & 1) ? perm_hi(vf[2 * jj][i >> 1], vf[2 * jj + 1][i >> 1]) : perm_lo(vf[2 * jj][i >> 1], vf[2 * jj + 1][i >> 1]);
      f32x4 o[8];
#pragma unroll
      for (int i = 0; i < 8; ++i) o[i] = mfma32(pf, vt[i], f32x4{0.f, 0.f, 0.f, 0.f});
#pragma unroll
      for (int i = 0; i < 8; ++i) KEEP(o[i]);
      float* mo = lds + L_MO + (aw * 4 + g) * 128 + 8 * c;
      *(f32x4*)(mo) = f32x4{o[0][0], o[1][0], o[2][0], o[3][0]};
      *(f32x4*)(mo + 4) = f32x4{o[4][0], o[5][0], o[6][0], o[7][0]};
    }
    if (arrive(CN(C_ATT), lane) != 3u) await<0>(CN(C_ATT), 4u);
    STAMP(5);
    {
      f32x2 ml[4], ov[4];
#pragma unroll
      for (int u = 0; u < 4; ++u) { ml[u] = *(const f32x2*)(lds + L_ML + (u * 4 + aw) * 2); ov[u] = *(const f32x2*)(lds + L_MO + (u * 4 + aw) * 128 + 2 * lane); }
      const float M = __builtin_fmaxf(__builtin_fmaxf(ml[0][0], ml[1][0]), __builtin_fmaxf(ml[2][0], ml[3][0]));
      float L = 0.f, o0 = 0.f, o1 = 0.f;
#pragma unroll
      for (int u = 0; u < 4; ++u) {
        const float f = __builtin_amdgcn_exp2f(ml[u][0] - M);
        L = fmaf(f, ml[u][1], L); o0 = fmaf(f, ov[u][0], o0); o1 = fmaf(f, ov[u][1], o1);
      }
      const int hg = 4 * hk + aw;
      const v4u pv = {__float_as_uint(o0), T, __float_as_uint(o1), T};
      __builtin_amdgcn_raw_buffer_store_b128(pv, rs, WS_P2 + (((hg * 4 + (lane >> 4)) * 4 + ab) * 8 + as) * 256 + (lane & 15) * 16, 0, AUX_SC1);
      if (lane == 0) {
        const v4u mv = {__float_as_uint(M), T, __float_as_uint(L), T};
        __builtin_amdgcn_raw_buffer_store_b128(mv, rs, WS_ML + ((hg * 4 + ab) * 8 + as) * 16, 0, AUX_SC1);
      }
    }
    PIN();
    signal(CN(C_PUB2), lane);
    STAMP(6);
    const int ob = bid >> 6, ocw = (bid >> 3) & 7, om = (bid >> 1) & 3, oh = bid & 1;
    const int col = 64 * ocw + 4 * (4 * om + (lane & 3)) + 2 * oh;
    const unsigned b2v = ldg<unsigned>(b2 + col);
    const unsigned off = WS_P3 + ((aw * 32 + (lane >> 2)) * 8 + ocw) * 2048 + oh * 1024 + (ob * 16 + 4 * om + (lane & 3)) * 16;
    await<2>(CN(C_PUB3), 4u);
    __builtin_amdgcn_s_sleep(DLY3);
    STAMP(8);
    v4u pv0 = __builtin_amdgcn_raw_buffer_load_b128(rs, off, 0, AUX_SC1);
    v4u pv1 = __builtin_amdgcn_raw_buffer_load_b128(rs, off + 16 * 8 * 2048, 0, AUX_SC1);
#pragma clang loop unroll(disable)
    for (unsigned spins = 0; spins < SPIN_CAP; ++spins) {
      const unsigned bad0 = tagbad(pv0, T), bad1 = tagbad(pv1, T);
      if (__all((bad0 | bad1) == 0)) break;
      asm volatile("" ::: "memory");
      if (bad0) pv0 = __builtin_amdgcn_raw_buffer_load_b128(rs, off, 0, AUX_SC1);
      if (bad1) pv1 = __builtin_amdgcn_raw_buffer_load_b128(rs, off + 16 * 8 * 2048, 0, AUX_SC1);
    }
    STAMP(9);
    float sa = __uint_as_float(pv0[0]) + __uint_as_float(pv1[0]), sb = __uint_as_float(pv0[2]) + __uint_as_float(pv1[2]);
    sa += dpp<ROR4>(sa); sb += dpp<ROR4>(sb); sa += dpp<ROR8>(sa); sb += dpp<ROR8>(sb);
    sa = x16_sum(sa); sb = x16_sum(sb); sa = x32_sum(sa); sb = x32_sum(sb);
    if (lane < 4) *(f32x2*)(lds + L_FR + aw * 8 + 2 * lane) = f32x2{sa, sb};
    if (arrive(CN(C_FIN), lane) == 3u) {
      if (lane < 4) {
        const float omg = lds[L_ST + 4 * ob + 3];
        f32x2 t = *(const f32x2*)(lds + L_FR + 2 * lane);
#pragma unroll
        for (int u = 1; u < 4; ++u) t += *(const f32x2*)(lds + L_FR + u * 8 + 2 * lane);
        *(GAS unsigned*)(out + ob * DM + col) = pack2(fmaf(omg, lo2f(b2v), t[0]), fmaf(omg, hi2f(b2v), t[1]));
      }
      if (bid == 0 && lane == 0) __hip_atomic_store((gu32*)ws, T, __ATOMIC_RELAXED, __HIP_MEMORY_SCOPE_AGENT);
    }
  } else {
    const int pw = w;
    SETPRIO(3);
    const int kq = hk & 3, cgq = (hk >> 2) * 32 + idx;
    const bool has_ht = idx < 14;
    const int hcg = (hk >> 2) * 14 + idx;
    const int qrow = 128 * kq + 32 * pw + 8 * g;
    v2u wv[8], w1v[8];
    {
      const u16* p = wq + (size_t)qrow * NQ + 64 * cgq + 4 * c;
#pragma unroll
      for (int j = 0; j < 8; ++j) wv[j] = ldnt<v2u>(p + j * NQ);
    }
    const v4u xa = ldg<v4u>(x + (c >> 2) * DM + qrow);
    const unsigned ev = __builtin_amdgcn_raw_buffer_load_b32(rs, 0, 0, 0);
    PIN();
    const v4u xs = ldg<v4u>(x + pw * DM + 8 * lane);
    const v4u gwv = ldg<v4u>(gw + 8 * lane);
    {
      const __amdgpu_buffer_rsrc_t rw1 = __builtin_amdgcn_make_buffer_rsrc((void*)w1, 0, has_ht ? DM * FF * 2 : 0, 0x00020000);
      const int o = (qrow * FF + 64 * hcg + 4 * c) * 2;
#pragma unroll
      for (int j = 0; j < 8; ++j) w1v[j] = __builtin_amdgcn_raw_buffer_load_b64(rw1, o + j * FF * 2, 0, AUX_NT);
    }
    if (tid < 16) ((LAS unsigned*)(lds + L_CNT))[tid] = 0u;
    BAR();
    PIN();
    unsigned T;
    {
      f32x4 d[4];
#pragma unroll
      for (int i = 0; i < 4; ++i) d[i] = mfma32(xa, tr8x4(wv, i), f32x4{0.f, 0.f, 0.f, 0.f});
      *(f32x4*)(lds + L_RQ + (pw * 64 + lane) * 4) = f32x4{d[0][0], d[1][0], d[2][0], d[3][0]};
      T = (unsigned)__builtin_amdgcn_readfirstlane(ev) + 1u;
      if (arrive(CN(C_Q), lane) == 3u) {
        f32x4 a = *(const f32x4*)(lds + L_RQ + lane * 4);
#pragma unroll
        for (int p = 1; p < 4; ++p) a += *(const f32x4*)(lds + L_RQ + (p * 64 + lane) * 4);
        const unsigned off = WS_QP + (cgq * 4 + kq) * 2048 + lane * 16;
        __builtin_amdgcn_raw_buffer_store_b128(v4u{__float_as_uint(a[0]), T, __float_as_uint(a[1]), T}, rs, off, 0, AUX_SC1);
        __builtin_amdgcn_raw_buffer_store_b128(v4u{__float_as_uint(a[2]), T, __float_as_uint(a[3]), T}, rs, off + 1024, 0, AUX_SC1);
      }
    }
    PIN();
    STAMP(1);
    {
      float xv[8], gv[8];
#pragma unroll
      for (int k = 0; k < 4; ++k) { xv[2 * k] = lo2f(xs[k]); xv[2 * k + 1] = hi2f(xs[k]); gv[2 * k] = lo2f(gwv[k]); gv[2 * k + 1] = hi2f(gwv[k]); }
      float s = 0.f, gs = 0.f;
#pragma unroll
      for (int k = 0; k < 8; ++k) { s += xv[k]; gs += bfr(xv[k] * gv[k]); }
      s = wave_sum(s); gs = wave_sum(gs);
      const float mean = s * (1.0f / DM);
      float vs = 0.f;
#pragma unroll
      for (int k = 0; k < 8; ++k) { const float d = xv[k] - mean; vs = fmaf(d, d, vs); }
      vs = wave_sum(vs);
      if (lane == 0) {
        const float var_b = bfr(vs * (1.0f / DM));
        const float den = bfr(sqrtf(bfr(var_b + 1e-5f)));
        const float gt = bfr(1.0f / (1.0f + __expf(-bfr(gs))));
        *(f32x4*)(lds + L_ST + 4 * pw) = f32x4{bfr(mean), den, gt, bfr(1.0f - gt)};
      }
      if (arrive(CN(C_ST), lane) != 3u) await<0>(CN(C_ST), 4u);
    }
    if (has_ht) {
      const f32x4 st = *(const f32x4*)(lds + L_ST + 4 * (c >> 2));
      v4u ya;
#pragma unroll
      for (int k = 0; k < 4; ++k) ya[k] = pack2(bfr(lo2f(xa[k]) - st[0]) / st[1], bfr(hi2f(xa[k]) - st[0]) / st[1]);
      f32x4 d[4];
#pragma unroll
      for (int i = 0; i < 4; ++i) d[i] = mfma32(ya, tr8x4(w1v, i), f32x4{0.f, 0.f, 0.f, 0.f});
      *(f32x4*)(lds + L_RH + (pw * 64 + lane) * 4) = f32x4{d[0][0], d[1][0], d[2][0], d[3][0]};
      if (arrive(CN(C_H), lane) == 3u) {
        f32x4 a = *(const f32x4*)(lds + L_RH + lane * 4);
#pragma unroll
        for (int p = 1; p < 4; ++p) a += *(const f32x4*)(lds + L_RH + (p * 64 + lane) * 4);
        const unsigned off = WS_HP + (hcg * 4 + kq) * 2048 + lane * 16;
        __builtin_amdgcn_raw_buffer_store_b128(v4u{__float_as_uint(a[0]), T, __float_as_uint(a[1]), T}, rs, off, 0, AUX_SC1);
        __builtin_amdgcn_raw_buffer_store_b128(v4u{__float_as_uint(a[2]), T, __float_as_uint(a[3]), T}, rs, off + 1024, 0, AUX_SC1);
      }
    }
    f32x4 stg = *(const f32x4*)(lds + L_ST + 4 * g);
    KEEP(stg);
    PIN();
    STAMP(2);
    await<2>(CN(C_ISS), ISSN);
    const bool has_mlp = kgl < 14;
    const int fs = 14 * hk + kgl, f0 = 16 * fs;
    v2u wov[8], w2v[4];
    {
      const unsigned qoff = WS_QP + ((hk * 8 + (lane >> 5)) * 4 + pw) * 2048 + ((lane >> 4) & 1) * 1024 + (16 * ab + c) * 16;
      v4u qv[4];
#pragma unroll
      for (int i = 0; i < 4; ++i) qv[i] = __builtin_amdgcn_raw_buffer_load_b128(rs, qoff + i * 16384, 0, AUX_SC1);
      PIN();
      if (LATE_FIRST) {
        const __amdgpu_buffer_rsrc_t rw2 = __builtin_amdgcn_make_buffer_rsrc((void*)w2, 0, has_mlp ? DM * FF * 2 : 0, 0x00020000);
        const u16* p = wo + (size_t)(32 * kg + 8 * g) * DM + 256 * cg + 64 * pw + 4 * c;
        const int o2 = ((f0 + 4 * g) * DM + 256 * cg + 64 * pw + 4 * c) * 2;
#pragma unroll
        for (int j = 0; j < 8; ++j) wov[j] = ldnt<v2u>(p + j * DM);
#pragma unroll
        for (int j = 0; j < 4; ++j) w2v[j] = __builtin_amdgcn_raw_buffer_load_b64(rw2, o2 + j * DM * 2, 0, AUX_NT);
      }
      PIN();
      unsigned bad0 = tagbad(qv[0], T) | tagbad(qv[1], T) | tagbad(qv[2], T) | tagbad(qv[3], T);
      if (!__all(bad0 == 0)) {
#pragma clang loop unroll(disable)
        for (unsigned spins = 0; spins < SPIN_CAP; ++spins) {
          unsigned bad[4];
#pragma unroll
          for (int i = 0; i < 4; ++i) bad[i] = tagbad(qv[i], T);
          if (__all((bad[0] | bad[1] | bad[2] | bad[3]) == 0)) break;
          asm volatile("" ::: "memory");
#pragma unroll
          for (int i = 0; i < 4; ++i) if (bad[i]) qv[i] = __builtin_amdgcn_raw_buffer_load_b128(rs, qoff + i * 16384, 0, AUX_SC1);
        }
      }
      float* qs = lds + L_QS + (pw * 64 + lane) * 8;
      *(v4u*)(qs) = v4u{qv[0][0], qv[0][2], qv[1][0], qv[1][2]};
      *(v4u*)(qs + 4) = v4u{qv[2][0], qv[2][2], qv[3][0], qv[3][2]};
      if (arrive(CN(C_QG), lane) == 3u) {
        f32x4 a0 = *(const f32x4*)(lds + L_QS + lane * 8), a1 = *(const f32x4*)(lds + L_QS + lane * 8 + 4);
#pragma unroll
        for (int p = 1; p < 4; ++p) { a0 += *(const f32x4*)(lds + L_QS + (p * 64 + lane) * 8); a1 += *(const f32x4*)(lds + L_QS + (p * 64 + lane) * 8 + 4); }
        unsigned* qb = (unsigned*)(lds + L_QB) + 32 * (lane >> 5) + 2 * c + ((lane >> 4) & 1);
        qb[0] = pack2(a0[0], a0[1]); qb[64] = pack2(a0[2], a0[3]); qb[128] = pack2(a1[0], a1[1]); qb[192] = pack2(a1[2], a1[3]);
        signal(CN(C_QRDY), lane);
      }
    }
    STAMP(3);
    if (!LATE_FIRST) {
      const __amdgpu_buffer_rsrc_t rw2 = __builtin_amdgcn_make_buffer_rsrc((void*)w2, 0, has_mlp ? DM * FF * 2 : 0, 0x00020000);
      const u16* p = wo + (size_t)(32 * kg + 8 * g) * DM + 256 * cg + 64 * pw + 4 * c;
      const int o2 = ((f0 + 4 * g) * DM + 256 * cg + 64 * pw + 4 * c) * 2;
#pragma unroll
      for (int j = 0; j < 8; ++j) wov[j] = ldnt<v2u>(p + j * DM);
#pragma unroll
      for (int j = 0; j < 4; ++j) w2v[j] = __builtin_amdgcn_raw_buffer_load_b64(rw2, o2 + j * DM * 2, 0, AUX_NT);
    }
    PIN();
    SETPRIO(3);
    STAMP(4);
    if (pw == 3) {
      float hv = 0.f;
      if (has_mlp) {
        const float bv = lo2f((unsigned)ldg<u16>(b1 + f0 + 4 * (c & 3) + g));
        const unsigned hoff = WS_HP + ((fs >> 2) * 4 + g) * 2048 + (16 * (c >> 2) + 4 * (fs & 3) + (c & 3)) * 16;
        v4u h0 = __builtin_amdgcn_raw_buffer_load_b128(rs, hoff, 0, AUX_SC1);
        v4u h1 = __builtin_amdgcn_raw_buffer_load_b128(rs, hoff + 1024, 0, AUX_SC1);
#pragma clang loop unroll(disable)
        for (unsigned spins = 0; spins < SPIN_CAP; ++spins) {
          const unsigned bad = tagbad(h0, T) | tagbad(h1, T);
          if (__all(bad == 0)) break;
          asm volatile("" ::: "memory");
          if (bad) {
            h0 = __builtin_amdgcn_raw_buffer_load_b128(rs, hoff, 0, AUX_SC1);
            h1 = __builtin_amdgcn_raw_buffer_load_b128(rs, hoff + 1024, 0, AUX_SC1);
          }
        }
        float a[4] = {__uint_as_float(h0[0]), __uint_as_float(h0[2]), __uint_as_float(h1[0]), __uint_as_float(h1[2])};
#pragma unroll
        for (int i = 0; i < 4; ++i) a[i] = x32_sum(x16_sum(a[i]));
        const float av = g == 0 ? a[0] : g == 1 ? a[1] : g == 2 ? a[2] : a[3];
        const float u = bfr(bfr(av) + bv);
        const float inner = 0.7978845608028654f * (u + 0.044715f * u * u * u);
        hv = 0.5f * u * (1.0f + tanhf(inner));
      }
      ((u16*)(lds + L_HB))[(c >> 2) * 16 + 4 * (c & 3) + g] = (u16)pack2(hv, 0.f);
      signal(CN(C_HRDY), lane);
    }
    v4u wof[4];
    float accm[4];
    {
      v2u w2f[4];
#pragma unroll
      for (int i = 0; i < 4; ++i) {
        wof[i] = tr8x4(wov, i);
#pragma unroll
        for (int jj = 0; jj < 2; ++jj)
          w2f[i][jj] = (i & 1) ? perm_hi(w2v[2 * jj][i >> 1], w2v[2 * jj + 1][i >> 1]) : perm_lo(w2v[2 * jj][i >> 1], w2v[2 * jj + 1][i >> 1]);
        KEEP(wof[i]);
      }
      await<1>(CN(C_HRDY), 1u);
      const v2u ha = *(const v2u*)((const u16*)(lds + L_HB) + (c >> 2) * 16 + 4 * g);
#pragma unroll
      for (int i = 0; i < 4; ++i) {
        const f32x4 e = mfma16(ha, w2f[i], f32x4{0.f, 0.f, 0.f, 0.f});
        accm[i] = stg[3] * e[0];
        KEEP(accm[i]);
      }
    }
    PIN();
    STAMP(5);
    const int h2 = 4 * hk + (kgl >> 2);
    const unsigned p2off = WS_P2 + (((h2 * 4 + (kgl & 3)) * 4 + pw) * 8 + (lane >> 3)) * 256 + (lane & 7) * 32;
    const unsigned mloff = WS_ML + ((h2 * 4 + pw) * 8 + (lane >> 3)) * 16;
    await<1>(CN(C_PUB2), 4u);
    __builtin_amdgcn_s_sleep(DLY2);
    STAMP(6);
    v4u a0 = __builtin_amdgcn_raw_buffer_load_b128(rs, p2off, 0, AUX_SC1);
    v4u a1 = __builtin_amdgcn_raw_buffer_load_b128(rs, p2off + 16, 0, AUX_SC1);
    v4u am = __builtin_amdgcn_raw_buffer_load_b128(rs, mloff, 0, AUX_SC1);
#pragma clang loop unroll(disable)
    for (unsigned spins = 0; spins < SPIN_CAP; ++spins) {
      const unsigned bad = tagbad(a0, T) | tagbad(a1, T) | tagbad(am, T);
      if (__all(bad == 0)) break;
      asm volatile("" ::: "memory");
      if (bad) {
        a0 = __builtin_amdgcn_raw_buffer_load_b128(rs, p2off, 0, AUX_SC1);
        a1 = __builtin_amdgcn_raw_buffer_load_b128(rs, p2off + 16, 0, AUX_SC1);
        am = __builtin_amdgcn_raw_buffer_load_b128(rs, mloff, 0, AUX_SC1);
      }
    }
    STAMP(7);
    {
      const float m = __uint_as_float(am[0]), l = __uint_as_float(am[2]);
      float M = __builtin_fmaxf(m, dpp<ROR8>(m)); M = x32_max(x16_max(M));
      const float f = __builtin_amdgcn_exp2f(m - M);
      float v[5] = {f * __uint_as_float(a0[0]), f * __uint_as_float(a0[2]), f * __uint_as_float(a1[0]), f * __uint_as_float(a1[2]), f * l};
#pragma unroll
      for (int i = 0; i < 5; ++i) v[i] += dpp<ROR8>(v[i]);
#pragma unroll
      for (int i = 0; i < 5; ++i) v[i] = x16_sum(v[i]);
#pragma unroll
      for (int i = 0; i < 5; ++i) v[i] = x32_sum(v[i]);
      const float inv = __builtin_amdgcn_rcpf(v[4]);
      if (lane < 8) *(v2u*)((u16*)(lds + L_OB) + pw * 32 + 4 * lane) = v2u{pack2(v[0] * inv, v[1] * inv), pack2(v[2] * inv, v[3] * inv)};
    }
    if (arrive(CN(C_O), lane) != 3u) await<0>(CN(C_O), 4u);
    {
      const v4u oa = *(const v4u*)((const u16*)(lds + L_OB) + (c >> 2) * 32 + 8 * g);
      f32x4 d[4];
#pragma unroll
      for (int i = 0; i < 4; ++i) d[i] = mfma32(oa, wof[i], f32x4{0.f, 0.f, 0.f, 0.f});
#pragma unroll
      for (int i = 0; i < 4; ++i) KEEP(d[i]);
      const unsigned off = WS_P3 + ((kg * 2 + cg) * 4 + pw) * 2048 + lane * 16;
      const v4u s0 = {__float_as_uint(fmaf(stg[2], d[0][0], accm[0])), T, __float_as_uint(fmaf(stg[2], d[1][0], accm[1])), T};
      const v4u s1 = {__float_as_uint(fmaf(stg[2], d[2][0], accm[2])), T, __float_as_uint(fmaf(stg[2], d[3][0], accm[3])), T};
      __builtin_amdgcn_raw_buffer_store_b128(s0, rs, off, 0, AUX_SC1);
      __builtin_amdgcn_raw_buffer_store_b128(s1, rs, off + 1024, 0, AUX_SC1);
    }
    PIN();
    signal(CN(C_PUB3), lane);
    STAMP(8);
  }
  STAMP_DUMP();
}

extern "C" void kernel_launch(void* const* d_in, const int* in_sizes, int n_in,
                              void* d_out, int out_size, void* d_ws, size_t ws_size,
                              hipStream_t stream) {
  (void)in_sizes; (void)n_in; (void)out_size; (void)ws_size;
  fused_kernel<<<256, 512, 0, stream>>>(
      (const u16*)d_in[0], (const u16*)d_in[1], (const u16*)d_in[2], (const u16*)d_in[3], (const u16*)d_in[4],
      (const u16*)d_in[6], (const u16*)d_in[7], (const u16*)d_in[8], (const u16*)d_in[9], (const u16*)d_in[10],
      (u16*)d_out, (unsigned char*)d_ws);
}
```

```cpp
#include <hip/hip_runtime.h>
#include <hip/hip_bf16.h>
#include <stdint.h>

#define GAS __attribute__((address_space(1)))
#define LAS __attribute__((address_space(3)))
typedef unsigned short u16;
typedef unsigned v4u __attribute__((ext_vector_type(4)));
typedef unsigned v2u __attribute__((ext_vector_type(2)));
typedef float f32x4 __attribute__((ext_vector_type(4)));
typedef float f32x2 __attribute__((ext_vector_type(2)));
typedef __bf16 bf16x8 __attribute__((ext_vector_type(8)));
typedef __bf16 bf16x2 __attribute__((ext_vector_type(2)));
typedef short s16x4 __attribute__((ext_vector_type(4)));
typedef GAS unsigned gu32;

constexpr int DM = 512, SK = 4096, DH = 128, WINDOW = 1024, FF = 1792, NQ = 4096;
constexpr unsigned WS_QP = 4096;
constexpr unsigned WS_HP = WS_QP + (512u << 10);
constexpr unsigned WS_P2 = WS_HP + (256u << 10);
constexpr unsigned WS_ML = WS_P2 + (1u << 20);
constexpr unsigned WS_P3 = WS_ML + 16384;
constexpr unsigned WS_END = WS_P3 + (2u << 20);
constexpr unsigned SPIN_CAP = 1u << 17;
constexpr unsigned LSPIN_CAP = 1u << 22;
constexpr int AUX_SC1 = 16, AUX_NT = 2;
constexpr float LOG2E = 1.4426950408889634f;
constexpr float SCL2 = 0.08838834764831845f * 1.4426950408889634f;

#define AWIN 6
#define ADLY 0
#define QGATE 8
#define ISSN 4u
#define LATE_FIRST 0
#define DLY1 16
#define DLY2 12
#define DLY3 14
#define SETPRIO(n) __builtin_amdgcn_s_setprio(n)

__device__ __forceinline__ unsigned pack2(float lo, float hi) {
  return __builtin_bit_cast(unsigned, __builtin_convertvector(f32x2{lo, hi}, bf16x2));
}
__device__ __forceinline__ float bfr(float f) { return (float)(__bf16)f; }
__device__ __forceinline__ float lo2f(unsigned u) { return __builtin_bit_cast(float, u << 16); }
__device__ __forceinline__ float hi2f(unsigned u) { return __builtin_bit_cast(float, u & 0xffff0000u); }
__device__ __forceinline__ unsigned perm_lo(unsigned a, unsigned b) { return __builtin_amdgcn_perm(b, a, 0x05040100u); }
__device__ __forceinline__ unsigned perm_hi(unsigned a, unsigned b) { return __builtin_amdgcn_perm(b, a, 0x07060302u); }

template <int CTRL> __device__ __forceinline__ float dpp(float x) {
  return __builtin_bit_cast(float, __builtin_amdgcn_mov_dpp(__builtin_bit_cast(int, x), CTRL, 0xf, 0xf, true));
}
constexpr int XOR1 = 0xB1, XOR2 = 0x4E, ROR8 = 0x128, ROR4 = 0x124;
__device__ __forceinline__ float x16_max(float x) {
  auto s = __builtin_amdgcn_permlane16_swap(__float_as_uint(x), __float_as_uint(x), false, false);
  return __builtin_fmaxf(__uint_as_float(s[0]), __uint_as_float(s[1]));
}
__device__ __forceinline__ float x32_max(float x) {
  auto s = __builtin_amdgcn_permlane32_swap(__float_as_uint(x), __float_as_uint(x), false, false);
  return __builtin_fmaxf(__uint_as_float(s[0]), __uint_as_float(s[1]));
}
__device__ __forceinline__ float x16_sum(float x) {
  auto s = __builtin_amdgcn_permlane16_swap(__float_as_uint(x), __float_as_uint(x), false, false);
  return __uint_as_float(s[0]) + __uint_as_float(s[1]);
}
__device__ __forceinline__ float x32_sum(float x) {
  auto s = __builtin_amdgcn_permlane32_swap(__float_as_uint(x), __float_as_uint(x), false, false);
  return __uint_as_float(s[0]) + __uint_as_float(s[1]);
}
__device__ __forceinline__ float wave_sum(float x) {
  x += dpp<XOR1>(x); x += dpp<XOR2>(x); x += dpp<ROR4>(x); x += dpp<ROR8>(x);
  return x32_sum(x16_sum(x));
}
__device__ __forceinline__ f32x4 mfma32(v4u a, v4u b, f32x4 c) {
  return __builtin_amdgcn_mfma_f32_16x16x32_bf16(__builtin_bit_cast(bf16x8, a), __builtin_bit_cast(bf16x8, b), c, 0, 0, 0);
}
__device__ __forceinline__ f32x4 mfma16(v2u a, v2u b, f32x4 c) {
  return __builtin_amdgcn_mfma_f32_16x16x16bf16_1k(__builtin_bit_cast(s16x4, a), __builtin_bit_cast(s16x4, b), c, 0, 0, 0);
}
#define PIN() __builtin_amdgcn_sched_barrier(0)
#define BAR() do { PIN(); asm volatile("s_waitcnt lgkmcnt(0)" ::: "memory"); __builtin_amdgcn_s_barrier(); asm volatile("" ::: "memory"); PIN(); } while (0)
#define KEEP(x) asm volatile("" : "+v"(x))
#define STAMP(i)
#define STAMP_INIT()
#define STAMP_DUMP()

template <typename T> __device__ __forceinline__ T ldg(const u16* p) { return *(const GAS T*)p; }
template <typename T> __device__ __forceinline__ T ldnt(const u16* p) { return __builtin_nontemporal_load((const GAS T*)p); }

constexpr int L_CNT = 0;
constexpr int L_ST = 16;
constexpr int L_RQ = 32;
constexpr int L_RH = L_RQ + 1024;
constexpr int L_QS = L_RH + 1024;
constexpr int L_HB = L_QS + 2048;
constexpr int L_MO = L_HB + 32;
constexpr int L_ML = L_MO + 2048;
constexpr int L_OB = L_ML + 32;
constexpr int L_QB = L_OB + 64;
constexpr int L_FR = L_QB + 256;
constexpr int L_KS = L_FR + 32;
constexpr int L_END = L_KS + 8192;
enum { C_Q = 0, C_QG, C_QRDY, C_ISS, C_ATT, C_PUB2, C_H, C_HRDY, C_O, C_PUB3, C_FIN, C_ST, C_QPUB };

__device__ __forceinline__ unsigned arrive(unsigned addr, int lane) {
  unsigned old = 0;
  if (lane == 0)
    asm volatile("s_waitcnt lgkmcnt(0)\n\tds_add_rtn_u32 %0, %1, %2\n\ts_waitcnt lgkmcnt(0)" : "=v"(old) : "v"(addr), "v"(1u) : "memory");
  return __builtin_amdgcn_readfirstlane(old);
}
__device__ __forceinline__ void signal(unsigned addr, int lane) {
  if (lane == 0) asm volatile("s_waitcnt lgkmcnt(0)\n\tds_add_u32 %0, %1" :: "v"(addr), "v"(1u) : "memory");
}
template <int SLEEP> __device__ __forceinline__ void await(unsigned addr, unsigned n) {
#pragma clang loop unroll(disable)
  for (unsigned s = 0; s < LSPIN_CAP; ++s) {
    unsigned v;
    asm volatile("ds_read_b32 %0, %1\n\ts_waitcnt lgkmcnt(0)" : "=v"(v) : "v"(addr) : "memory");
    if ((unsigned)__builtin_amdgcn_readfirstlane(v) >= n) break;
    if (SLEEP) __builtin_amdgcn_s_sleep(SLEEP);
  }
}

__device__ __forceinline__ v4u tr8x4(const v2u (&W)[8], int i) {
  v4u f;
#pragma unroll
  for (int jj = 0; jj < 4; ++jj)
    f[jj] = (i & 1) ? perm_hi(W[2 * jj][i >> 1], W[2 * jj + 1][i >> 1]) : perm_lo(W[2 * jj][i >> 1], W[2 * jj + 1][i >> 1]);
  return f;
}
__device__ __forceinline__ unsigned tagbad(v4u v, unsigned T) { return (v[1] ^ T) | (v[3] ^ T); }

__global__ void __launch_bounds__(512, 2)
fused_kernel(const u16* __restrict__ x, const u16* __restrict__ kcache, const u16* __restrict__ vcache,
             const u16* __restrict__ wq, const u16* __restrict__ wo, const u16* __restrict__ w1,
             const u16* __restrict__ b1, const u16* __restrict__ w2, const u16* __restrict__ b2,
             const u16* __restrict__ gw, u16* __restrict__ out, unsigned char* ws) {
  __shared__ __attribute__((aligned(16))) float lds[L_END];
  asm volatile("" :: "s"(x), "s"(kcache), "s"(vcache), "s"(wq), "s"(wo), "s"(w1), "s"(b1), "s"(w2), "s"(b2), "s"(gw), "s"(out), "s"(ws));
  const int tid = threadIdx.x, lane = tid & 63, w = __builtin_amdgcn_readfirstlane(tid >> 6);
  const int g = lane >> 4, c = lane & 15;
  const int bid = blockIdx.x, hk = bid & 7, idx = bid >> 3;
  const int ab = idx >> 3, as = idx & 7;
  const int kgl = idx >> 1, cg = idx & 1, kg = hk * 16 + kgl;
  const __amdgpu_buffer_rsrc_t rs = __builtin_amdgcn_make_buffer_rsrc((void*)ws, 0, (int)WS_END, 0x00020000);
  const unsigned cn = (unsigned)(size_t)(LAS float*)(lds + L_CNT);
#define CN(i) (cn + 4u * (i))
  STAMP_INIT();
  STAMP(0);

  if (w >= 4) {
    const int aw = w - 4;
    BAR();
    unsigned tt;
    {
      const unsigned char* tp = ws;
      tp = lane == 0 ? (const unsigned char*)(out + (bid >> 6) * DM) : tp;
      tp = lane == 1 ? ws + WS_P3 : tp;
      tp = lane == 2 ? ws + WS_END - 64 : tp;
      tp = lane == 3 ? (const unsigned char*)b2 : tp;
      tp = lane == 4 ? (const unsigned char*)(wo + (size_t)(32 * kg) * DM) : tp;
      const int fst = 16 * (14 * hk + (kgl < 14 ? kgl : 13));
      tp = lane == 5 ? (const unsigned char*)(w2 + (size_t)fst * DM + 256 * cg) : tp;
      tp = lane == 12 ? (const unsigned char*)(w2 + (size_t)(fst + 15) * DM + 256 * cg) : tp;
      tp = lane == 13 ? (const unsigned char*)(b1 + fst) : tp;
      tp = lane == 6 ? (const unsigned char*)b1 : tp;
      tp = lane == 7 ? ws + WS_P2 : tp;
      tp = lane == 8 ? (const unsigned char*)(vcache + ((size_t)(ab * 8 + hk) * SK + (SK - WINDOW) + 128 * as + 32 * aw) * DH) : tp;
      tp = lane == 9 ? (const unsigned char*)(wo + (size_t)(32 * kg + 31) * DM + 511) - 2 : tp;
      tp = lane == 10 ? ws + WS_HP : tp;
      tp = lane == 11 ? ws + WS_ML : tp;
      tt = *(const GAS unsigned*)tp;
    }
    PIN();
    if (ADLY) __builtin_amdgcn_s_sleep(ADLY);
    v4u kr[8], vf[8];
    {
      const size_t kvoff = ((size_t)(ab * 8 + hk) * SK + (SK - WINDOW) + 128 * as + 32 * aw) * DH;
      const u16* kp = kcache + kvoff + 8 * lane;
      const u16* vp = vcache + kvoff + 4 * g * DH + 8 * c;
#pragma unroll
      for (int i = 0; i < 8; ++i) kr[i] = ldnt<v4u>(kp + i * 512);
#pragma unroll
      for (int j = 0; j < 8; ++j) vf[j] = ldnt<v4u>(vp + (16 * (j >> 2) + (j & 3)) * DH);
    }
    PIN();
    SETPRIO(2);
    const unsigned T = (unsigned)__builtin_amdgcn_readlane(tt, 63) + 1u;
    STAMP(1);
    v4u kf[8];
    {
      unsigned char* ks_ = (unsigned char*)(lds + L_KS) + aw * 8192;
#pragma unroll
      for (int i = 0; i < 8; ++i) { const int row = 4 * i + g; *(v4u*)(ks_ + row * 256 + ((c ^ (row & 15)) << 4)) = kr[i]; }
      asm volatile("s_waitcnt lgkmcnt(0)" ::: "memory");
#pragma unroll
      for (int t = 0; t < 2; ++t)
#pragma unroll
        for (int ks = 0; ks < 4; ++ks) kf[t * 4 + ks] = *(const v4u*)(ks_ + (16 * t + c) * 256 + (((4 * ks + g) ^ c) << 4));
#pragma unroll
      for (int i = 0; i < 8; ++i) KEEP(kf[i]);
    }
    PIN();
    asm volatile("s_waitcnt vmcnt(%0)" :: "n"(QGATE) : "memory");
    signal(CN(C_ISS), lane);
    STAMP(2);
    await<0>(CN(C_QRDY), 1u);
    STAMP(3);
    v4u pf;
    {
      f32x4 s0 = {0.f, 0.f, 0.f, 0.f}, s1 = {0.f, 0.f, 0.f, 0.f};
      const u16* qb = (const u16*)(lds + L_QB) + (c >> 2) * 128 + 8 * g;
      v4u qf[4];
#pragma unroll
      for (int ks = 0; ks < 4; ++ks) qf[ks] = *(const v4u*)(qb + 32 * ks);
#pragma unroll
      for (int ks = 0; ks < 4; ++ks) {
        s0 = mfma32(kf[ks], qf[ks], s0);
        s1 = mfma32(kf[4 + ks], qf[ks], s1);
      }
      float m = __builtin_fmaxf(__builtin_fmaxf(__builtin_fmaxf(s0[0], s0[1]), __builtin_fmaxf(s0[2], s0[3])),
                                __builtin_fmaxf(__builtin_fmaxf(s1[0], s1[1]), __builtin_fmaxf(s1[2], s1[3])));
      m = x32_max(x16_max(m));
      float p[8];
#pragma unroll
      for (int j = 0; j < 4; ++j) { p[j] = __builtin_amdgcn_exp2f((s0[j] - m) * SCL2); p[4 + j] = __builtin_amdgcn_exp2f((s1[j] - m) * SCL2); }
      pf = v4u{pack2(p[0], p[1]), pack2(p[2], p[3]), pack2(p[4], p[5]), pack2(p[6], p[7])};
      float l = ((p[0] + p[1]) + (p[2] + p[3])) + ((p[4] + p[5]) + (p[6] + p[7]));
      l = x32_sum(x16_sum(l));
      if (g == 0 && (c & 3) == 0) *(f32x2*)(lds + L_ML + (aw * 4 + (c >> 2)) * 2) = f32x2{m * SCL2, l};
      KEEP(pf);
    }
    PIN();
    STAMP(4);
    {
      v4u vt[8];
#pragma unroll
      for (int i = 0; i < 8; ++i)
#pragma unroll
        for (int jj = 0; jj < 4; ++jj)
          vt[i][jj] = (i & 1) ? perm_hi(vf[2 * jj][i >> 1], vf[2 * jj + 1][i >> 1]) : perm_lo(vf[2 * jj][i >> 1], vf[2 * jj + 1][i >> 1]);
      f32x4 o[8];
#pragma unroll
      for (int i = 0; i < 8; ++i) o[i] = mfma32(pf, vt[i], f32x4{0.f, 0.f, 0.f, 0.f});
#pragma unroll
      for (int i = 0; i < 8; ++i) KEEP(o[i]);
      float* mo = lds + L_MO + (aw * 4 + g) * 128 + 8 * c;
      *(f32x4*)(mo) = f32x4{o[0][0], o[1][0], o[2][0], o[3][0]};
      *(f32x4*)(mo + 4) = f32x4{o[4][0], o[5][0], o[6][0], o[7][0]};
    }
    if (arrive(CN(C_ATT), lane) != 3u) await<0>(CN(C_ATT), 4u);
    STAMP(5);
    {
      f32x2 ml[4], ov[4];
#pragma unroll
      for (int u = 0; u < 4; ++u) { ml[u] = *(const f32x2*)(lds + L_ML + (u * 4 + aw) * 2); ov[u] = *(const f32x2*)(lds + L_MO + (u * 4 + aw) * 128 + 2 * lane); }
      const float M = __builtin_fmaxf(__builtin_fmaxf(ml[0][0], ml[1][0]), __builtin_fmaxf(ml[2][0], ml[3][0]));
      float L = 0.f, o0 = 0.f, o1 = 0.f;
#pragma unroll
      for (int u = 0; u < 4; ++u) {
        const float f = __builtin_amdgcn_exp2f(ml[u][0] - M);
        L = fmaf(f, ml[u][1], L); o0 = fmaf(f, ov[u][0], o0); o1 = fmaf(f, ov[u][1], o1);
      }
      const int hg = 4 * hk + aw;
      const v4u pv = {__float_as_uint(o0), T, __float_as_uint(o1), T};
      __builtin_amdgcn_raw_buffer_store_b128(pv, rs, WS_P2 + (((hg * 4 + (lane >> 4)) * 4 + ab) * 8 + as) * 256 + (lane & 15) * 16, 0, AUX_SC1);
      if (lane == 0) {
        const v4u mv = {__float_as_uint(M), T, __float_as_uint(L), T};
        __builtin_amdgcn_raw_buffer_store_b128(mv, rs, WS_ML + ((hg * 4 + ab) * 8 + as) * 16, 0, AUX_SC1);
      }
    }
    PIN();
    __builtin_amdgcn_s_sleep(DLY2);
    signal(CN(C_PUB2), lane);
    STAMP(6);
    const int ob = bid >> 6, ocw = (bid >> 3) & 7, om = (bid >> 1) & 3, oh = bid & 1;
    const int col = 64 * ocw + 4 * (4 * om + (lane & 3)) + 2 * oh;
    const unsigned b2v = ldg<unsigned>(b2 + col);
    const unsigned off = WS_P3 + ((aw * 32 + (lane >> 2)) * 8 + ocw) * 2048 + oh * 1024 + (ob * 16 + 4 * om + (lane & 3)) * 16;
    await<1>(CN(C_PUB3), 4u);
    STAMP(8);
    v4u pv0 = __builtin_amdgcn_raw_buffer_load_b128(rs, off, 0, AUX_SC1);
    v4u pv1 = __builtin_amdgcn_raw_buffer_load_b128(rs, off + 16 * 8 * 2048, 0, AUX_SC1);
#pragma clang loop unroll(disable)
    for (unsigned spins = 0; spins < SPIN_CAP; ++spins) {
      const unsigned bad0 = tagbad(pv0, T), bad1 = tagbad(pv1, T);
      if (__all((bad0 | bad1) == 0)) break;
      asm volatile("" ::: "memory");
      if (bad0) pv0 = __builtin_amdgcn_raw_buffer_load_b128(rs, off, 0, AUX_SC1);
      if (bad1) pv1 = __builtin_amdgcn_raw_buffer_load_b128(rs, off + 16 * 8 * 2048, 0, AUX_SC1);
    }
    STAMP(9);
    float sa = __uint_as_float(pv0[0]) + __uint_as_float(pv1[0]), sb = __uint_as_float(pv0[2]) + __uint_as_float(pv1[2]);
    sa += dpp<ROR4>(sa); sb += dpp<ROR4>(sb); sa += dpp<ROR8>(sa); sb += dpp<ROR8>(sb);
    sa = x16_sum(sa); sb = x16_sum(sb); sa = x32_sum(sa); sb = x32_sum(sb);
    if (lane < 4) *(f32x2*)(lds + L_FR + aw * 8 + 2 * lane) = f32x2{sa, sb};
    if (arrive(CN(C_FIN), lane) == 3u) {
      if (lane < 4) {
        const float omg = lds[L_ST + 4 * ob + 3];
        f32x2 t = *(const f32x2*)(lds + L_FR + 2 * lane);
#pragma unroll
        for (int u = 1; u < 4; ++u) t += *(const f32x2*)(lds + L_FR + u * 8 + 2 * lane);
        *(GAS unsigned*)(out + ob * DM + col) = pack2(fmaf(omg, lo2f(b2v), t[0]), fmaf(omg, hi2f(b2v), t[1]));
      }
      if (bid == 0 && lane == 0) __hip_atomic_store((gu32*)ws, T, __ATOMIC_RELAXED, __HIP_MEMORY_SCOPE_AGENT);
    }
  } else {
    const int pw = w;
    SETPRIO(3);
    const int kq = idx & 3, cq = idx >> 2;
    const bool has_ht = idx < 14;
    const int th = 14 * hk + idx;
    const int qrow = 128 * kq + 32 * pw + 8 * g, hrow = 128 * (th & 3) + 32 * pw + 8 * g;
    v2u wv[8], w1v[8];
    {
      const u16* p = wq + (size_t)qrow * NQ + hk * 512 + 64 * cq + 4 * c;
#pragma unroll
      for (int j = 0; j < 8; ++j) wv[j] = ldnt<v2u>(p + j * NQ);
    }
    const v4u xa = ldg<v4u>(x + (c >> 2) * DM + qrow);
    const unsigned ev = __builtin_amdgcn_raw_buffer_load_b32(rs, 0, 0, 0);
    PIN();
    const v4u xs = ldg<v4u>(x + pw * DM + 8 * lane);
    const v4u gwv = ldg<v4u>(gw + 8 * lane);
    const v4u xh = ldg<v4u>(x + (c >> 2) * DM + hrow);
    {
      const __amdgpu_buffer_rsrc_t rw1 = __builtin_amdgcn_make_buffer_rsrc((void*)w1, 0, has_ht ? DM * FF * 2 : 0, 0x00020000);
      const int o = (hrow * FF + 64 * (th >> 2) + 4 * c) * 2;
#pragma unroll
      for (int j = 0; j < 8; ++j) w1v[j] = __builtin_amdgcn_raw_buffer_load_b64(rw1, o + j * FF * 2, 0, AUX_NT);
    }
    if (tid < 16) ((LAS unsigned*)(lds + L_CNT))[tid] = 0u;
    BAR();
    PIN();
    unsigned T;
    {
      f32x4 d[4];
#pragma unroll
      for (int i = 0; i < 4; ++i) d[i] = mfma32(xa, tr8x4(wv, i), f32x4{0.f, 0.f, 0.f, 0.f});
      *(f32x4*)(lds + L_RQ + (pw * 64 + lane) * 4) = f32x4{d[0][0], d[1][0], d[2][0], d[3][0]};
      T = (unsigned)__builtin_amdgcn_readfirstlane(ev) + 1u;
      if (arrive(CN(C_Q), lane) == 3u) {
        f32x4 a = *(const f32x4*)(lds + L_RQ + lane * 4);
#pragma unroll
        for (int p = 1; p < 4; ++p) a += *(const f32x4*)(lds + L_RQ + (p * 64 + lane) * 4);
        const unsigned off = WS_QP + ((hk * 8 + cq) * 4 + kq) * 2048 + lane * 16;
        __builtin_amdgcn_raw_buffer_store_b128(v4u{__float_as_uint(a[0]), T, __float_as_uint(a[1]), T}, rs, off, 0, AUX_SC1);
        __builtin_amdgcn_raw_buffer_store_b128(v4u{__float_as_uint(a[2]), T, __float_as_uint(a[3]), T}, rs, off + 1024, 0, AUX_SC1);
      }
    }
    PIN();
    STAMP(1);
    {
      float xv[8], gv[8];
#pragma unroll
      for (int k = 0; k < 4; ++k) { xv[2 * k] = lo2f(xs[k]); xv[2 * k + 1] = hi2f(xs[k]); gv[2 * k] = lo2f(gwv[k]); gv[2 * k + 1] = hi2f(gwv[k]); }
      float s = 0.f, gs = 0.f;
#pragma unroll
      for (int k = 0; k < 8; ++k) { s += xv[k]; gs += bfr(xv[k] * gv[k]); }
      s = wave_sum(s); gs = wave_sum(gs);
      const float mean = s * (1.0f / DM);
      float vs = 0.f;
#pragma unroll
      for (int k = 0; k < 8; ++k) { const float d = xv[k] - mean; vs = fmaf(d, d, vs); }
      vs = wave_sum(vs);
      if (lane == 0) {
        const float var_b = bfr(vs * (1.0f / DM));
        const float den = bfr(sqrtf(bfr(var_b + 1e-5f)));
        const float gt = bfr(1.0f / (1.0f + __expf(-bfr(gs))));
        *(f32x4*)(lds + L_ST + 4 * pw) = f32x4{bfr(mean), den, gt, bfr(1.0f - gt)};
      }
      if (arrive(CN(C_ST), lane) != 3u) await<0>(CN(C_ST), 4u);
    }
    if (has_ht) {
      const f32x4 st = *(const f32x4*)(lds + L_ST + 4 * (c >> 2));
      v4u ya;
#pragma unroll
      for (int k = 0; k < 4; ++k) ya[k] = pack2(bfr(lo2f(xh[k]) - st[0]) / st[1], bfr(hi2f(xh[k]) - st[0]) / st[1]);
      f32x4 d[4];
#pragma unroll
      for (int i = 0; i < 4; ++i) d[i] = mfma32(ya, tr8x4(w1v, i), f32x4{0.f, 0.f, 0.f, 0.f});
      *(f32x4*)(lds + L_RH + (pw * 64 + lane) * 4) = f32x4{d[0][0], d[1][0], d[2][0], d[3][0]};
      if (arrive(CN(C_H), lane) == 3u) {
        f32x4 a = *(const f32x4*)(lds + L_RH + lane * 4);
#pragma unroll
        for (int p = 1; p < 4; ++p) a += *(const f32x4*)(lds + L_RH + (p * 64 + lane) * 4);
        const unsigned off = WS_HP + th * 2048 + lane * 16;
        __builtin_amdgcn_raw_buffer_store_b128(v4u{__float_as_uint(a[0]), T, __float_as_uint(a[1]), T}, rs, off, 0, AUX_SC1);
        __builtin_amdgcn_raw_buffer_store_b128(v4u{__float_as_uint(a[2]), T, __float_as_uint(a[3]), T}, rs, off + 1024, 0, AUX_SC1);
      }
    }
    f32x4 stg = *(const f32x4*)(lds + L_ST + 4 * g);
    KEEP(stg);
    PIN();
    STAMP(2);
    await<2>(CN(C_ISS), ISSN);
    const bool has_mlp = kgl < 14;
    const int fs = 14 * hk + kgl, f0 = 16 * fs;
    v2u wov[8], w2v[4];
    {
      const unsigned qoff = WS_QP + ((hk * 8 + (lane >> 5)) * 4 + pw) * 2048 + ((lane >> 4) & 1) * 1024 + (16 * ab + c) * 16;
      v4u qv[4];
#pragma unroll
      for (int i = 0; i < 4; ++i) qv[i] = __builtin_amdgcn_raw_buffer_load_b128(rs, qoff + i * 16384, 0, AUX_SC1);
      PIN();
      if (LATE_FIRST) {
        const __amdgpu_buffer_rsrc_t rw2 = __builtin_amdgcn_make_buffer_rsrc((void*)w2, 0, has_mlp ? DM * FF * 2 : 0, 0x00020000);
        const u16* p = wo + (size_t)(32 * kg + 8 * g) * DM + 256 * cg + 64 * pw + 4 * c;
        const int o2 = ((f0 + 4 * g) * DM + 256 * cg + 64 * pw + 4 * c) * 2;
#pragma unroll
        for (int j = 0; j < 8; ++j) wov[j] = ldnt<v2u>(p + j * DM);
#pragma unroll
        for (int j = 0; j < 4; ++j) w2v[j] = __builtin_amdgcn_raw_buffer_load_b64(rw2, o2 + j * DM * 2, 0, AUX_NT);
      }
      PIN();
      unsigned bad0 = tagbad(qv[0], T) | tagbad(qv[1], T) | tagbad(qv[2], T) | tagbad(qv[3], T);
      if (!__all(bad0 == 0)) {
#pragma clang loop unroll(disable)
        for (unsigned spins = 0; spins < SPIN_CAP; ++spins) {
          unsigned bad[4];
#pragma unroll
          for (int i = 0; i < 4; ++i) bad[i] = tagbad(qv[i], T);
          if (__all((bad[0] | bad[1] | bad[2] | bad[3]) == 0)) break;
          asm volatile("" ::: "memory");
#pragma unroll
          for (int i = 0; i < 4; ++i) if (bad[i]) qv[i] = __builtin_amdgcn_raw_buffer_load_b128(rs, qoff + i * 16384, 0, AUX_SC1);
        }
      }
      float* qs = lds + L_QS + (pw * 64 + lane) * 8;
      *(v4u*)(qs) = v4u{qv[0][0], qv[0][2], qv[1][0], qv[1][2]};
      *(v4u*)(qs + 4) = v4u{qv[2][0], qv[2][2], qv[3][0], qv[3][2]};
      if (arrive(CN(C_QG), lane) == 3u) {
        f32x4 a0 = *(const f32x4*)(lds + L_QS + lane * 8), a1 = *(const f32x4*)(lds + L_QS + lane * 8 + 4);
#pragma unroll
        for (int p = 1; p < 4; ++p) { a0 += *(const f32x4*)(lds + L_QS + (p * 64 + lane) * 8); a1 += *(const f32x4*)(lds + L_QS + (p * 64 + lane) * 8 + 4); }
        unsigned* qb = (unsigned*)(lds + L_QB) + 32 * (lane >> 5) + 2 * c + ((lane >> 4) & 1);
        qb[0] = pack2(a0[0], a0[1]); qb[64] = pack2(a0[2], a0[3]); qb[128] = pack2(a1[0], a1[1]); qb[192] = pack2(a1[2], a1[3]);
        signal(CN(C_QRDY), lane);
      }
    }
    STAMP(3);
    const bool hgat = pw == 3 && has_mlp;
    const unsigned hoff = WS_HP + ((fs >> 2) * 4 + g) * 2048 + (16 * (c >> 2) + 4 * (fs & 3) + (c & 3)) * 16;
    v4u h0, h1;
    unsigned b1raw;
    {
      const __amdgpu_buffer_rsrc_t rsh = __builtin_amdgcn_make_buffer_rsrc((void*)ws, 0, hgat ? (int)WS_END : 0, 0x00020000);
      const __amdgpu_buffer_rsrc_t rb1 = __builtin_amdgcn_make_buffer_rsrc((void*)b1, 0, hgat ? FF * 2 : 0, 0x00020000);
      h0 = __builtin_amdgcn_raw_buffer_load_b128(rsh, hoff, 0, AUX_SC1);
      h1 = __builtin_amdgcn_raw_buffer_load_b128(rsh, hoff + 1024, 0, AUX_SC1);
      b1raw = __builtin_amdgcn_raw_buffer_load_b16(rb1, (f0 + 4 * (c & 3) + g) * 2, 0, 0);
      PIN();
      const __amdgpu_buffer_rsrc_t rw2 = __builtin_amdgcn_make_buffer_rsrc((void*)w2, 0, has_mlp ? DM * FF * 2 : 0, 0x00020000);
      const u16* p = wo + (size_t)(32 * kg + 8 * g) * DM + 256 * cg + 64 * pw + 4 * c;
      const int o2 = ((f0 + 4 * g) * DM + 256 * cg + 64 * pw + 4 * c) * 2;
#pragma unroll
      for (int j = 0; j < 8; ++j) wov[j] = ldnt<v2u>(p + j * DM);
#pragma unroll
      for (int j = 0; j < 4; ++j) w2v[j] = __builtin_amdgcn_raw_buffer_load_b64(rw2, o2 + j * DM * 2, 0, AUX_NT);
    }
    PIN();
    STAMP(4);
    if (pw == 3) {
      float hv = 0.f;
      if (has_mlp) {
        if (!__all((tagbad(h0, T) | tagbad(h1, T)) == 0)) {
#pragma clang loop unroll(disable)
          for (unsigned spins = 0; spins < SPIN_CAP; ++spins) {
            const unsigned bad = tagbad(h0, T) | tagbad(h1, T);
            if (__all(bad == 0)) break;
            asm volatile("" ::: "memory");
            if (bad) {
              h0 = __builtin_amdgcn_raw_buffer_load_b128(rs, hoff, 0, AUX_SC1);
              h1 = __builtin_amdgcn_raw_buffer_load_b128(rs, hoff + 1024, 0, AUX_SC1);
            }
          }
        }
        float a[4] = {__uint_as_float(h0[0]), __uint_as_float(h0[2]), __uint_as_float(h1[0]), __uint_as_float(h1[2])};
#pragma unroll
        for (int i = 0; i < 4; ++i) a[i] = x32_sum(x16_sum(a[i]));
        const float av = g == 0 ? a[0] : g == 1 ? a[1] : g == 2 ? a[2] : a[3];
        const float u = bfr(bfr(av) + lo2f(b1raw));
        const float inner = 0.7978845608028654f * (u + 0.044715f * u * u * u);
        hv = 0.5f * u * (1.0f + tanhf(inner));
      }
      ((u16*)(lds + L_HB))[(c >> 2) * 16 + 4 * (c & 3) + g] = (u16)pack2(hv, 0.f);
      signal(CN(C_HRDY), lane);
    }
    const int h2 = 4 * hk + (kgl >> 2);
    const unsigned p2off = WS_P2 + (((h2 * 4 + (kgl & 3)) * 4 + pw) * 8 + (lane >> 3)) * 256 + (lane & 7) * 32;
    const unsigned mloff = WS_ML + ((h2 * 4 + pw) * 8 + (lane >> 3)) * 16;
    await<1>(CN(C_PUB2), 4u);
    STAMP(5);
    v4u a0 = __builtin_amdgcn_raw_buffer_load_b128(rs, p2off, 0, AUX_SC1);
    v4u a1 = __builtin_amdgcn_raw_buffer_load_b128(rs, p2off + 16, 0, AUX_SC1);
    v4u am = __builtin_amdgcn_raw_buffer_load_b128(rs, mloff, 0, AUX_SC1);
    PIN();
    v4u wof[4];
    float accm[4];
    {
      v2u w2f[4];
#pragma unroll
      for (int i = 0; i < 4; ++i) {
        wof[i] = tr8x4(wov, i);
#pragma unroll
        for (int jj = 0; jj < 2; ++jj)
          w2f[i][jj] = (i & 1) ? perm_hi(w2v[2 * jj][i >> 1], w2v[2 * jj + 1][i >> 1]) : perm_lo(w2v[2 * jj][i >> 1], w2v[2 * jj + 1][i >> 1]);
        KEEP(wof[i]);
      }
      await<1>(CN(C_HRDY), 1u);
      const v2u ha = *(const v2u*)((const u16*)(lds + L_HB) + (c >> 2) * 16 + 4 * g);
#pragma unroll
      for (int i = 0; i < 4; ++i) {
        const f32x4 e = mfma16(ha, w2f[i], f32x4{0.f, 0.f, 0.f, 0.f});
        accm[i] = stg[3] * e[0];
        KEEP(accm[i]);
      }
    }
    PIN();
    STAMP(6);
#pragma clang loop unroll(disable)
    for (unsigned spins = 0; spins < SPIN_CAP; ++spins) {
      const unsigned bad = tagbad(a0, T) | tagbad(a1, T) | tagbad(am, T);
      if (__all(bad == 0)) break;
      asm volatile("" ::: "memory");
      if (bad) {
        a0 = __builtin_amdgcn_raw_buffer_load_b128(rs, p2off, 0, AUX_SC1);
        a1 = __builtin_amdgcn_raw_buffer_load_b128(rs, p2off + 16, 0, AUX_SC1);
        am = __builtin_amdgcn_raw_buffer_load_b128(rs, mloff, 0, AUX_SC1);
      }
    }
    STAMP(7);
    {
      const float m = __uint_as_float(am[0]), l = __uint_as_float(am[2]);
      float M = __builtin_fmaxf(m, dpp<ROR8>(m)); M = x32_max(x16_max(M));
      const float f = __builtin_amdgcn_exp2f(m - M);
      float v[5] = {f * __uint_as_float(a0[0]), f * __uint_as_float(a0[2]), f * __uint_as_float(a1[0]), f * __uint_as_float(a1[2]), f * l};
#pragma unroll
      for (int i = 0; i < 5; ++i) v[i] += dpp<ROR8>(v[i]);
#pragma unroll
      for (int i = 0; i < 5; ++i) v[i] = x16_sum(v[i]);
#pragma unroll
      for (int i = 0; i < 5; ++i) v[i] = x32_sum(v[i]);
      const float inv = __builtin_amdgcn_rcpf(v[4]);
      if (lane < 8) *(v2u*)((u16*)(lds + L_OB) + pw * 32 + 4 * lane) = v2u{pack2(v[0] * inv, v[1] * inv), pack2(v[2] * inv, v[3] * inv)};
    }
    if (arrive(CN(C_O), lane) != 3u) await<0>(CN(C_O), 4u);
    {
      const v4u oa = *(const v4u*)((const u16*)(lds + L_OB) + (c >> 2) * 32 + 8 * g);
      f32x4 d[4];
#pragma unroll
      for (int i = 0; i < 4; ++i) d[i] = mfma32(oa, wof[i], f32x4{0.f, 0.f, 0.f, 0.f});
#pragma unroll
      for (int i = 0; i < 4; ++i) KEEP(d[i]);
      const unsigned off = WS_P3 + ((kg * 2 + cg) * 4 + pw) * 2048 + lane * 16;
      const v4u s0 = {__float_as_uint(fmaf(stg[2], d[0][0], accm[0])), T, __float_as_uint(fmaf(stg[2], d[1][0], accm[1])), T};
      const v4u s1 = {__float_as_uint(fmaf(stg[2], d[2][0], accm[2])), T, __float_as_uint(fmaf(stg[2], d[3][0], accm[3])), T};
      __builtin_amdgcn_raw_buffer_store_b128(s0, rs, off, 0, AUX_SC1);
      __builtin_amdgcn_raw_buffer_store_b128(s1, rs, off + 1024, 0, AUX_SC1);
    }
    PIN();
    __builtin_amdgcn_s_sleep(DLY3);
    signal(CN(C_PUB3), lane);
    STAMP(8);
  }
  STAMP_DUMP();
}

extern "C" void kernel_launch(void* const* d_in, const int* in_sizes, int n_in,
                              void* d_out, int out_size, void* d_ws, size_t ws_size,
                              hipStream_t stream) {
  (void)in_sizes; (void)n_in; (void)out_size; (void)ws_size;
  fused_kernel<<<256, 512, 0, stream>>>(
      (const u16*)d_in[0], (const u16*)d_in[1], (const u16*)d_in[2], (const u16*)d_in[3], (const u16*)d_in[4],
      (const u16*)d_in[6], (const u16*)d_in[7], (const u16*)d_in[8], (const u16*)d_in[9], (const u16*)d_in[10],
      (u16*)d_out, (unsigned char*)d_ws);
}
```

```cpp
#define DLY2 12
#define DLY3 14
#include <hip/hip_runtime.h>
#include <hip/hip_bf16.h>
#include <stdint.h>

#define GAS __attribute__((address_space(1)))
#define LAS __attribute__((address_space(3)))
typedef unsigned short u16;
typedef unsigned v4u __attribute__((ext_vector_type(4)));
typedef unsigned v2u __attribute__((ext_vector_type(2)));
typedef float f32x4 __attribute__((ext_vector_type(4)));
typedef float f32x2 __attribute__((ext_vector_type(2)));
typedef __bf16 bf16x8 __attribute__((ext_vector_type(8)));
typedef __bf16 bf16x2 __attribute__((ext_vector_type(2)));
typedef short s16x4 __attribute__((ext_vector_type(4)));
typedef GAS unsigned gu32;

constexpr int DM = 512, SK = 4096, DH = 128, WINDOW = 1024, FF = 1792, NQ = 4096;
constexpr unsigned WS_QP = 4096;
constexpr unsigned WS_HP = WS_QP + (512u << 10);
constexpr unsigned WS_P2 = WS_HP + (256u << 10);
constexpr unsigned WS_ML = WS_P2 + (1u << 20);
constexpr unsigned WS_P3 = WS_ML + 131072;
constexpr unsigned WS_END = WS_P3 + (2u << 20);
constexpr unsigned SPIN_CAP = 1u << 17;
constexpr unsigned LSPIN_CAP = 1u << 22;
constexpr int AUX_SC1 = 16, AUX_NT = 2;
constexpr float LOG2E = 1.4426950408889634f;
constexpr float SCL2 = 0.08838834764831845f * 1.4426950408889634f;

#define AWIN 6
#define ADLY 0
#define QGATE 8
#define ISSN 4u
#define LATE_FIRST 0
#define DLY1 16
#define SETPRIO(n) __builtin_amdgcn_s_setprio(n)

__device__ __forceinline__ unsigned pack2(float lo, float hi) {
  return __builtin_bit_cast(unsigned, __builtin_convertvector(f32x2{lo, hi}, bf16x2));
}
__device__ __forceinline__ float bfr(float f) { return (float)(__bf16)f; }
__device__ __forceinline__ float lo2f(unsigned u) { return __builtin_bit_cast(float, u << 16); }
__device__ __forceinline__ float hi2f(unsigned u) { return __builtin_bit_cast(float, u & 0xffff0000u); }
__device__ __forceinline__ unsigned perm_lo(unsigned a, unsigned b) { return __builtin_amdgcn_perm(b, a, 0x05040100u); }
__device__ __forceinline__ unsigned perm_hi(unsigned a, unsigned b) { return __builtin_amdgcn_perm(b, a, 0x07060302u); }

template <int CTRL> __device__ __forceinline__ float dpp(float x) {
  return __builtin_bit_cast(float, __builtin_amdgcn_mov_dpp(__builtin_bit_cast(int, x), CTRL, 0xf, 0xf, true));
}
constexpr int XOR1 = 0xB1, XOR2 = 0x4E, ROR8 = 0x128, ROR4 = 0x124;
__device__ __forceinline__ float x16_max(float x) {
  auto s = __builtin_amdgcn_permlane16_swap(__float_as_uint(x), __float_as_uint(x), false, false);
  return __builtin_fmaxf(__uint_as_float(s[0]), __uint_as_float(s[1]));
}
__device__ __forceinline__ float x32_max(float x) {
  auto s = __builtin_amdgcn_permlane32_swap(__float_as_uint(x), __float_as_uint(x), false, false);
  return __builtin_fmaxf(__uint_as_float(s[0]), __uint_as_float(s[1]));
}
__device__ __forceinline__ float x16_sum(float x) {
  auto s = __builtin_amdgcn_permlane16_swap(__float_as_uint(x), __float_as_uint(x), false, false);
  return __uint_as_float(s[0]) + __uint_as_float(s[1]);
}
__device__ __forceinline__ float x32_sum(float x) {
  auto s = __builtin_amdgcn_permlane32_swap(__float_as_uint(x), __float_as_uint(x), false, false);
  return __uint_as_float(s[0]) + __uint_as_float(s[1]);
}
__device__ __forceinline__ float wave_sum(float x) {
  x += dpp<XOR1>(x); x += dpp<XOR2>(x); x += dpp<ROR4>(x); x += dpp<ROR8>(x);
  return x32_sum(x16_sum(x));
}
__device__ __forceinline__ f32x4 mfma32(v4u a, v4u b, f32x4 c) {
  return __builtin_amdgcn_mfma_f32_16x16x32_bf16(__builtin_bit_cast(bf16x8, a), __builtin_bit_cast(bf16x8, b), c, 0, 0, 0);
}
__device__ __forceinline__ f32x4 mfma16(v2u a, v2u b, f32x4 c) {
  return __builtin_amdgcn_mfma_f32_16x16x16bf16_1k(__builtin_bit_cast(s16x4, a), __builtin_bit_cast(s16x4, b), c, 0, 0, 0);
}
#define PIN() __builtin_amdgcn_sched_barrier(0)
#define BAR() do { PIN(); asm volatile("s_waitcnt lgkmcnt(0)" ::: "memory"); __builtin_amdgcn_s_barrier(); asm volatile("" ::: "memory"); PIN(); } while (0)
#define KEEP(x) asm volatile("" : "+v"(x))
#define STAMP(i)
#define STAMP_INIT()
#define STAMP_DUMP()

template <typename T> __device__ __forceinline__ T ldg(const u16* p) { return *(const GAS T*)p; }
template <typename T> __device__ __forceinline__ T ldnt(const u16* p) { return __builtin_nontemporal_load((const GAS T*)p); }

constexpr int L_CNT = 0;
constexpr int L_ST = 16;
constexpr int L_RQ = 32;
constexpr int L_RH = L_RQ + 1024;
constexpr int L_QS = L_RH + 1024;
constexpr int L_HB = L_QS + 2048;
constexpr int L_MO = L_HB + 32;
constexpr int L_ML = L_MO + 2048;
constexpr int L_OB = L_ML + 32;
constexpr int L_QB = L_OB + 64;
constexpr int L_FR = L_QB + 256;
constexpr int L_KS = L_FR + 32;
constexpr int L_END = L_KS + 8192;
enum { C_Q = 0, C_QG, C_QRDY, C_ISS, C_ATT, C_PUB2, C_H, C_HRDY, C_O, C_PUB3, C_FIN, C_ST, C_QPUB };

__device__ __forceinline__ unsigned arrive(unsigned addr, int lane) {
  unsigned old = 0;
  if (lane == 0)
    asm volatile("s_waitcnt lgkmcnt(0)\n\tds_add_rtn_u32 %0, %1, %2\n\ts_waitcnt lgkmcnt(0)" : "=v"(old) : "v"(addr), "v"(1u) : "memory");
  return __builtin_amdgcn_readfirstlane(old);
}
__device__ __forceinline__ void signal(unsigned addr, int lane) {
  if (lane == 0) asm volatile("s_waitcnt lgkmcnt(0)\n\tds_add_u32 %0, %1" :: "v"(addr), "v"(1u) : "memory");
}
template <int SLEEP> __device__ __forceinline__ void await(unsigned addr, unsigned n) {
#pragma clang loop unroll(disable)
  for (unsigned s = 0; s < LSPIN_CAP; ++s) {
    unsigned v;
    asm volatile("ds_read_b32 %0, %1\n\ts_waitcnt lgkmcnt(0)" : "=v"(v) : "v"(addr) : "memory");
    if ((unsigned)__builtin_amdgcn_readfirstlane(v) >= n) break;
    if (SLEEP) __builtin_amdgcn_s_sleep(SLEEP);
  }
}

__device__ __forceinline__ v4u tr8x4(const v2u (&W)[8], int i) {
  v4u f;
#pragma unroll
  for (int jj = 0; jj < 4; ++jj)
    f[jj] = (i & 1) ? perm_hi(W[2 * jj][i >> 1], W[2 * jj + 1][i >> 1]) : perm_lo(W[2 * jj][i >> 1], W[2 * jj + 1][i >> 1]);
  return f;
}
__device__ __forceinline__ unsigned tagbad(v4u v, unsigned T) { return (v[1] ^ T) | (v[3] ^ T); }

__global__ void __launch_bounds__(512, 2)
fused_kernel(const u16* __restrict__ x, const u16* __restrict__ kcache, const u16* __restrict__ vcache,
             const u16* __restrict__ wq, const u16* __restrict__ wo, const u16* __restrict__ w1,
             const u16* __restrict__ b1, const u16* __restrict__ w2, const u16* __restrict__ b2,
             const u16* __restrict__ gw, u16* __restrict__ out, unsigned char* ws) {
  __shared__ __attribute__((aligned(16))) float lds[L_END];
  asm volatile("" :: "s"(x), "s"(kcache), "s"(vcache), "s"(wq), "s"(wo), "s"(w1), "s"(b1), "s"(w2), "s"(b2), "s"(gw), "s"(out), "s"(ws));
  const int tid = threadIdx.x, lane = tid & 63, w = __builtin_amdgcn_readfirstlane(tid >> 6);
  const int g = lane >> 4, c = lane & 15;
  const int bid = blockIdx.x, hk = bid & 7, idx = bid >> 3;
  const int ab = idx >> 3, as = idx & 7;
  const int kgl = idx >> 1, cg = idx & 1, kg = hk * 16 + kgl;
  const __amdgpu_buffer_rsrc_t rs = __builtin_amdgcn_make_buffer_rsrc((void*)ws, 0, (int)WS_END, 0x00020000);
  const unsigned cn = (unsigned)(size_t)(LAS float*)(lds + L_CNT);
#define CN(i) (cn + 4u * (i))
  STAMP_INIT();
  STAMP(0);

  if (w >= 4) {
    const int aw = w - 4;
    BAR();
    unsigned tt;
    {
      const unsigned char* tp = ws;
      tp = lane == 0 ? (const unsigned char*)(out + (bid >> 6) * DM) : tp;
      tp = lane == 1 ? ws + WS_P3 : tp;
      tp = lane == 2 ? ws + WS_END - 64 : tp;
      tp = lane == 3 ? (const unsigned char*)b2 : tp;
      tp = lane == 4 ? (const unsigned char*)(wo + (size_t)(32 * kg) * DM) : tp;
      const int fst = 16 * (14 * hk + (kgl < 14 ? kgl : 13));
      tp = lane == 5 ? (const unsigned char*)(w2 + (size_t)fst * DM + 256 * cg) : tp;
      tp = lane == 12 ? (const unsigned char*)(w2 + (size_t)(fst + 15) * DM + 256 * cg) : tp;
      tp = lane == 13 ? (const unsigned char*)(b1 + fst) : tp;
      tp = lane == 6 ? (const unsigned char*)b1 : tp;
      tp = lane == 7 ? ws + WS_P2 : tp;
      tp = lane == 8 ? (const unsigned char*)(vcache + ((size_t)(ab * 8 + hk) * SK + (SK - WINDOW) + 128 * as + 32 * aw) * DH) : tp;
      tp = lane == 9 ? (const unsigned char*)(wo + (size_t)(32 * kg + 31) * DM + 511) - 2 : tp;
      tp = lane == 10 ? ws + WS_HP : tp;
      tp = lane == 11 ? ws + WS_ML : tp;
      tt = *(const GAS unsigned*)tp;
    }
    PIN();
    if (ADLY) __builtin_amdgcn_s_sleep(ADLY);
    v4u kr[8], vf[8];
    {
      const size_t kvoff = ((size_t)(ab * 8 + hk) * SK + (SK - WINDOW) + 128 * as + 32 * aw) * DH;
      const u16* kp = kcache + kvoff + 8 * lane;
      const u16* vp = vcache + kvoff + 4 * g * DH + 8 * c;
#pragma unroll
      for (int i = 0; i < 8; ++i) kr[i] = ldnt<v4u>(kp + i * 512);
#pragma unroll
      for (int j = 0; j < 8; ++j) vf[j] = ldnt<v4u>(vp + (16 * (j >> 2) + (j & 3)) * DH);
    }
    PIN();
    SETPRIO(2);
    const unsigned T = (unsigned)__builtin_amdgcn_readlane(tt, 63) + 1u;
    STAMP(1);
    v4u kf[8];
    {
      unsigned char* ks_ = (unsigned char*)(lds + L_KS) + aw * 8192;
#pragma unroll
      for (int i = 0; i < 8; ++i) { const int row = 4 * i + g; *(v4u*)(ks_ + row * 256 + ((c ^ (row & 15)) << 4)) = kr[i]; }
      asm volatile("s_waitcnt lgkmcnt(0)" ::: "memory");
#pragma unroll
      for (int t = 0; t < 2; ++t)
#pragma unroll
        for (int ks = 0; ks < 4; ++ks) kf[t * 4 + ks] = *(const v4u*)(ks_ + (16 * t + c) * 256 + (((4 * ks + g) ^ c) << 4));
#pragma unroll
      for (int i = 0; i < 8; ++i) KEEP(kf[i]);
    }
    PIN();
    asm volatile("s_waitcnt vmcnt(%0)" :: "n"(QGATE) : "memory");
    signal(CN(C_ISS), lane);
    STAMP(2);
    await<0>(CN(C_QRDY), 1u);
    STAMP(3);
    v4u pf;
    {
      f32x4 s0 = {0.f, 0.f, 0.f, 0.f}, s1 = {0.f, 0.f, 0.f, 0.f};
      const u16* qb = (const u16*)(lds + L_QB) + (c >> 2) * 128 + 8 * g;
      v4u qf[4];
#pragma unroll
      for (int ks = 0; ks < 4; ++ks) qf[ks] = *(const v4u*)(qb + 32 * ks);
#pragma unroll
      for (int ks = 0; ks < 4; ++ks) {
        s0 = mfma32(kf[ks], qf[ks], s0);
        s1 = mfma32(kf[4 + ks], qf[ks], s1);
      }
      float m = __builtin_fmaxf(__builtin_fmaxf(__builtin_fmaxf(s0[0], s0[1]), __builtin_fmaxf(s0[2], s0[3])),
                                __builtin_fmaxf(__builtin_fmaxf(s1[0], s1[1]), __builtin_fmaxf(s1[2], s1[3])));
      m = x32_max(x16_max(m));
      float p[8];
#pragma unroll
      for (int j = 0; j < 4; ++j) { p[j] = __builtin_amdgcn_exp2f((s0[j] - m) * SCL2); p[4 + j] = __builtin_amdgcn_exp2f((s1[j] - m) * SCL2); }
      pf = v4u{pack2(p[0], p[1]), pack2(p[2], p[3]), pack2(p[4], p[5]), pack2(p[6], p[7])};
      float l = ((p[0] + p[1]) + (p[2] + p[3])) + ((p[4] + p[5]) + (p[6] + p[7]));
      l = x32_sum(x16_sum(l));
      if (g == 0 && (c & 3) == 0) *(f32x2*)(lds + L_ML + (aw * 4 + (c >> 2)) * 2) = f32x2{m * SCL2, l};
      KEEP(pf);
    }
    PIN();
    STAMP(4);
    {
      v4u vt[8];
#pragma unroll
      for (int i = 0; i < 8; ++i)
#pragma unroll
        for (int jj = 0; jj < 4; ++jj)
          vt[i][jj] = (i & 1) ? perm_hi(vf[2 * jj][i >> 1], vf[2 * jj + 1][i >> 1]) : perm_lo(vf[2 * jj][i >> 1], vf[2 * jj + 1][i >> 1]);
      f32x4 o[8];
#pragma unroll
      for (int i = 0; i < 8; ++i) o[i] = mfma32(pf, vt[i], f32x4{0.f, 0.f, 0.f, 0.f});
#pragma unroll
      for (int i = 0; i < 8; ++i) KEEP(o[i]);
      float* mo = lds + L_MO + (aw * 4 + g) * 128 + 8 * c;
      *(f32x4*)(mo) = f32x4{o[0][0], o[1][0], o[2][0], o[3][0]};
      *(f32x4*)(mo + 4) = f32x4{o[4][0], o[5][0], o[6][0], o[7][0]};
    }
    if (arrive(CN(C_ATT), lane) != 3u) await<0>(CN(C_ATT), 4u);
    STAMP(5);
    {
      f32x2 ml[4], ov[4];
#pragma unroll
      for (int u = 0; u < 4; ++u) { ml[u] = *(const f32x2*)(lds + L_ML + (u * 4 + aw) * 2); ov[u] = *(const f32x2*)(lds + L_MO + (u * 4 + aw) * 128 + 2 * lane); }
      const float M = __builtin_fmaxf(__builtin_fmaxf(ml[0][0], ml[1][0]), __builtin_fmaxf(ml[2][0], ml[3][0]));
      float L = 0.f, o0 = 0.f, o1 = 0.f;
#pragma unroll
      for (int u = 0; u < 4; ++u) {
        const float f = __builtin_amdgcn_exp2f(ml[u][0] - M);
        L = fmaf(f, ml[u][1], L); o0 = fmaf(f, ov[u][0], o0); o1 = fmaf(f, ov[u][1], o1);
      }
      const int hg = 4 * hk + aw;
      const v4u pv = {__float_as_uint(o0), T, __float_as_uint(o1), T};
      __builtin_amdgcn_raw_buffer_store_b128(pv, rs, WS_P2 + (((hg * 4 + (lane >> 4)) * 4 + ab) * 8 + as) * 256 + (lane & 15) * 16, 0, AUX_SC1);
      if (lane < 8) {
        const v4u mv = {__float_as_uint(M), T, __float_as_uint(L), T};
        __builtin_amdgcn_raw_buffer_store_b128(mv, rs, WS_ML + ((hg * 4 + ab) * 8 + as) * 128 + lane * 16, 0, AUX_SC1);
      }
    }
    PIN();
    __builtin_amdgcn_s_sleep(DLY2);
    signal(CN(C_PUB2), lane);
    STAMP(6);
    const int ob = bid >> 6, ocw = bid & 7, om = (bid >> 4) & 3, oh = (bid >> 3) & 1;
    const int col = 64 * ocw + 4 * (4 * om + (lane & 3)) + 2 * oh;
    const unsigned b2v = ldg<unsigned>(b2 + col);
    const unsigned off = WS_P3 + ((aw * 32 + (lane >> 2)) * 8 + ocw) * 2048 + oh * 1024 + (ob * 16 + 4 * om + (lane & 3)) * 16;
    await<1>(CN(C_PUB3), 4u);
    STAMP(8);
    v4u pv0 = __builtin_amdgcn_raw_buffer_load_b128(rs, off, 0, AUX_SC1);
    v4u pv1 = __builtin_amdgcn_raw_buffer_load_b128(rs, off + 16 * 8 * 2048, 0, AUX_SC1);
#pragma clang loop unroll(disable)
    for (unsigned spins = 0; spins < SPIN_CAP; ++spins) {
      const unsigned bad0 = tagbad(pv0, T), bad1 = tagbad(pv1, T);
      if (__all((bad0 | bad1) == 0)) break;
      asm volatile("" ::: "memory");
      if (bad0) pv0 = __builtin_amdgcn_raw_buffer_load_b128(rs, off, 0, AUX_SC1);
      if (bad1) pv1 = __builtin_amdgcn_raw_buffer_load_b128(rs, off + 16 * 8 * 2048, 0, AUX_SC1);
    }
    STAMP(9);
    float sa = __uint_as_float(pv0[0]) + __uint_as_float(pv1[0]), sb = __uint_as_float(pv0[2]) + __uint_as_float(pv1[2]);
    sa += dpp<ROR4>(sa); sb += dpp<ROR4>(sb); sa += dpp<ROR8>(sa); sb += dpp<ROR8>(sb);
    sa = x16_sum(sa); sb = x16_sum(sb); sa = x32_sum(sa); sb = x32_sum(sb);
    if (lane < 4) *(f32x2*)(lds + L_FR + aw * 8 + 2 * lane) = f32x2{sa, sb};
    if (arrive(CN(C_FIN), lane) == 3u) {
      if (lane < 4) {
        const float omg = lds[L_ST + 4 * ob + 3];
        f32x2 t = *(const f32x2*)(lds + L_FR + 2 * lane);
#pragma unroll
        for (int u = 1; u < 4; ++u) t += *(const f32x2*)(lds + L_FR + u * 8 + 2 * lane);
        *(GAS unsigned*)(out + ob * DM + col) = pack2(fmaf(omg, lo2f(b2v), t[0]), fmaf(omg, hi2f(b2v), t[1]));
      }
      if (bid == 0 && lane == 0) __hip_atomic_store((gu32*)ws, T, __ATOMIC_RELAXED, __HIP_MEMORY_SCOPE_AGENT);
    }
  } else {
    const int pw = w;
    SETPRIO(3);
    const int kq = idx & 3, cq = idx >> 2;
    const bool has_ht = idx < 14;
    const int th = 14 * hk + idx;
    const int qrow = 128 * kq + 32 * pw + 8 * g, hrow = 128 * (th & 3) + 32 * pw + 8 * g;
    v2u wv[8], w1v[8];
    {
      const u16* p = wq + (size_t)qrow * NQ + hk * 512 + 64 * cq + 4 * c;
#pragma unroll
      for (int j = 0; j < 8; ++j) wv[j] = ldnt<v2u>(p + j * NQ);
    }
    const v4u xa = ldg<v4u>(x + (c >> 2) * DM + qrow);
    const unsigned ev = __builtin_amdgcn_raw_buffer_load_b32(rs, 0, 0, 0);
    PIN();
    const v4u xs = ldg<v4u>(x + pw * DM + 8 * lane);
    const v4u gwv = ldg<v4u>(gw + 8 * lane);
    const v4u xh = ldg<v4u>(x + (c >> 2) * DM + hrow);
    {
      const __amdgpu_buffer_rsrc_t rw1 = __builtin_amdgcn_make_buffer_rsrc((void*)w1, 0, has_ht ? DM * FF * 2 : 0, 0x00020000);
      const int o = (hrow * FF + 64 * (th >> 2) + 4 * c) * 2;
#pragma unroll
      for (int j = 0; j < 8; ++j) w1v[j] = __builtin_amdgcn_raw_buffer_load_b64(rw1, o + j * FF * 2, 0, AUX_NT);
    }
    if (tid < 16) ((LAS unsigned*)(lds + L_CNT))[tid] = 0u;
    BAR();
    PIN();
    unsigned T;
    {
      f32x4 d[4];
#pragma unroll
      for (int i = 0; i < 4; ++i) d[i] = mfma32(xa, tr8x4(wv, i), f32x4{0.f, 0.f, 0.f, 0.f});
      *(f32x4*)(lds + L_RQ + (pw * 64 + lane) * 4) = f32x4{d[0][0], d[1][0], d[2][0], d[3][0]};
      T = (unsigned)__builtin_amdgcn_readfirstlane(ev) + 1u;
      if (arrive(CN(C_Q), lane) == 3u) {
        f32x4 a = *(const f32x4*)(lds + L_RQ + lane * 4);
#pragma unroll
        for (int p = 1; p < 4; ++p) a += *(const f32x4*)(lds + L_RQ + (p * 64 + lane) * 4);
        const unsigned off = WS_QP + ((hk * 8 + cq) * 4 + kq) * 2048 + lane * 16;
        __builtin_amdgcn_raw_buffer_store_b128(v4u{__float_as_uint(a[0]), T, __float_as_uint(a[1]), T}, rs, off, 0, AUX_SC1);
        __builtin_amdgcn_raw_buffer_store_b128(v4u{__float_as_uint(a[2]), T, __float_as_uint(a[3]), T}, rs, off + 1024, 0, AUX_SC1);
      }
    }
    PIN();
    STAMP(1);
    {
      float xv[8], gv[8];
#pragma unroll
      for (int k = 0; k < 4; ++k) { xv[2 * k] = lo2f(xs[k]); xv[2 * k + 1] = hi2f(xs[k]); gv[2 * k] = lo2f(gwv[k]); gv[2 * k + 1] = hi2f(gwv[k]); }
      float s = 0.f, gs = 0.f;
#pragma unroll
      for (int k = 0; k < 8; ++k) { s += xv[k]; gs += bfr(xv[k] * gv[k]); }
      s = wave_sum(s); gs = wave_sum(gs);
      const float mean = s * (1.0f / DM);
      float vs = 0.f;
#pragma unroll
      for (int k = 0; k < 8; ++k) { const float d = xv[k] - mean; vs = fmaf(d, d, vs); }
      vs = wave_sum(vs);
      if (lane == 0) {
        const float var_b = bfr(vs * (1.0f / DM));
        const float den = bfr(sqrtf(bfr(var_b + 1e-5f)));
        const float gt = bfr(1.0f / (1.0f + __expf(-bfr(gs))));
        *(f32x4*)(lds + L_ST + 4 * pw) = f32x4{bfr(mean), den, gt, bfr(1.0f - gt)};
      }
      if (arrive(CN(C_ST), lane) != 3u) await<0>(CN(C_ST), 4u);
    }
    if (has_ht) {
      const f32x4 st = *(const f32x4*)(lds + L_ST + 4 * (c >> 2));
      v4u ya;
#pragma unroll
      for (int k = 0; k < 4; ++k) ya[k] = pack2(bfr(lo2f(xh[k]) - st[0]) / st[1], bfr(hi2f(xh[k]) - st[0]) / st[1]);
      f32x4 d[4];
#pragma unroll
      for (int i = 0; i < 4; ++i) d[i] = mfma32(ya, tr8x4(w1v, i), f32x4{0.f, 0.f, 0.f, 0.f});
      *(f32x4*)(lds + L_RH + (pw * 64 + lane) * 4) = f32x4{d[0][0], d[1][0], d[2][0], d[3][0]};
      if (arrive(CN(C_H), lane) == 3u) {
        f32x4 a = *(const f32x4*)(lds + L_RH + lane * 4);
#pragma unroll
        for (int p = 1; p < 4; ++p) a += *(const f32x4*)(lds + L_RH + (p * 64 + lane) * 4);
        const unsigned off = WS_HP + th * 2048 + lane * 16;
        __builtin_amdgcn_raw_buffer_store_b128(v4u{__float_as_uint(a[0]), T, __float_as_uint(a[1]), T}, rs, off, 0, AUX_SC1);
        __builtin_amdgcn_raw_buffer_store_b128(v4u{__float_as_uint(a[2]), T, __float_as_uint(a[3]), T}, rs, off + 1024, 0, AUX_SC1);
      }
    }
    f32x4 stg = *(const f32x4*)(lds + L_ST + 4 * g);
    KEEP(stg);
    PIN();
    STAMP(2);
    await<2>(CN(C_ISS), ISSN);
    const bool has_mlp = kgl < 14;
    const int fs = 14 * hk + kgl, f0 = 16 * fs;
    v2u wov[8], w2v[4];
    {
      const unsigned qoff = WS_QP + ((hk * 8 + (lane >> 5)) * 4 + pw) * 2048 + ((lane >> 4) & 1) * 1024 + (16 * ab + c) * 16;
      v4u qv[4];
#pragma unroll
      for (int i = 0; i < 4; ++i) qv[i] = __builtin_amdgcn_raw_buffer_load_b128(rs, qoff + i * 16384, 0, AUX_SC1);
      PIN();
      if (LATE_FIRST) {
        const __amdgpu_buffer_rsrc_t rw2 = __builtin_amdgcn_make_buffer_rsrc((void*)w2, 0, has_mlp ? DM * FF * 2 : 0, 0x00020000);
        const u16* p = wo + (size_t)(32 * kg + 8 * g) * DM + 256 * cg + 64 * pw + 4 * c;
        const int o2 = ((f0 + 4 * g) * DM + 256 * cg + 64 * pw + 4 * c) * 2;
#pragma unroll
        for (int j = 0; j < 8; ++j) wov[j] = ldnt<v2u>(p + j * DM);
#pragma unroll
        for (int j = 0; j < 4; ++j) w2v[j] = __builtin_amdgcn_raw_buffer_load_b64(rw2, o2 + j * DM * 2, 0, AUX_NT);
      }
      PIN();
      unsigned bad0 = tagbad(qv[0], T) | tagbad(qv[1], T) | tagbad(qv[2], T) | tagbad(qv[3], T);
      if (!__all(bad0 == 0)) {
#pragma clang loop unroll(disable)
        for (unsigned spins = 0; spins < SPIN_CAP; ++spins) {
          unsigned bad[4];
#pragma unroll
          for (int i = 0; i < 4; ++i) bad[i] = tagbad(qv[i], T);
          if (__all((bad[0] | bad[1] | bad[2] | bad[3]) == 0)) break;
          asm volatile("" ::: "memory");
#pragma unroll
          for (int i = 0; i < 4; ++i) if (bad[i]) qv[i] = __builtin_amdgcn_raw_buffer_load_b128(rs, qoff + i * 16384, 0, AUX_SC1);
        }
      }
      float* qs = lds + L_QS + (pw * 64 + lane) * 8;
      *(v4u*)(qs) = v4u{qv[0][0], qv[0][2], qv[1][0], qv[1][2]};
      *(v4u*)(qs + 4) = v4u{qv[2][0], qv[2][2], qv[3][0], qv[3][2]};
      if (arrive(CN(C_QG), lane) == 3u) {
        f32x4 a0 = *(const f32x4*)(lds + L_QS + lane * 8), a1 = *(const f32x4*)(lds + L_QS + lane * 8 + 4);
#pragma unroll
        for (int p = 1; p < 4; ++p) { a0 += *(const f32x4*)(lds + L_QS + (p * 64 + lane) * 8); a1 += *(const f32x4*)(lds + L_QS + (p * 64 + lane) * 8 + 4); }
        unsigned* qb = (unsigned*)(lds + L_QB) + 32 * (lane >> 5) + 2 * c + ((lane >> 4) & 1);
        qb[0] = pack2(a0[0], a0[1]); qb[64] = pack2(a0[2], a0[3]); qb[128] = pack2(a1[0], a1[1]); qb[192] = pack2(a1[2], a1[3]);
        signal(CN(C_QRDY), lane);
      }
    }
    STAMP(3);
    const bool hgat = pw == 3 && has_mlp;
    const unsigned hoff = WS_HP + ((fs >> 2) * 4 + g) * 2048 + (16 * (c >> 2) + 4 * (fs & 3) + (c & 3)) * 16;
    v4u h0, h1;
    unsigned b1raw;
    {
      const __amdgpu_buffer_rsrc_t rsh = __builtin_amdgcn_make_buffer_rsrc((void*)ws, 0, hgat ? (int)WS_END : 0, 0x00020000);
      const __amdgpu_buffer_rsrc_t rb1 = __builtin_amdgcn_make_buffer_rsrc((void*)b1, 0, hgat ? FF * 2 : 0, 0x00020000);
      h0 = __builtin_amdgcn_raw_buffer_load_b128(rsh, hoff, 0, AUX_SC1);
      h1 = __builtin_amdgcn_raw_buffer_load_b128(rsh, hoff + 1024, 0, AUX_SC1);
      b1raw = __builtin_amdgcn_raw_buffer_load_b16(rb1, (f0 + 4 * (c & 3) + g) * 2, 0, 0);
      PIN();
      const __amdgpu_buffer_rsrc_t rw2 = __builtin_amdgcn_make_buffer_rsrc((void*)w2, 0, has_mlp ? DM * FF * 2 : 0, 0x00020000);
      const u16* p = wo + (size_t)(32 * kg + 8 * g) * DM + 256 * cg + 64 * pw + 4 * c;
      const int o2 = ((f0 + 4 * g) * DM + 256 * cg + 64 * pw + 4 * c) * 2;
#pragma unroll
      for (int j = 0; j < 8; ++j) wov[j] = ldnt<v2u>(p + j * DM);
#pragma unroll
      for (int j = 0; j < 4; ++j) w2v[j] = __builtin_amdgcn_raw_buffer_load_b64(rw2, o2 + j * DM * 2, 0, AUX_NT);
    }
    PIN();
    STAMP(4);
    if (pw == 3) {
      float hv = 0.f;
      if (has_mlp) {
        if (!__all((tagbad(h0, T) | tagbad(h1, T)) == 0)) {
#pragma clang loop unroll(disable)
          for (unsigned spins = 0; spins < SPIN_CAP; ++spins) {
            const unsigned bad = tagbad(h0, T) | tagbad(h1, T);
            if (__all(bad == 0)) break;
            asm volatile("" ::: "memory");
            if (bad) {
              h0 = __builtin_amdgcn_raw_buffer_load_b128(rs, hoff, 0, AUX_SC1);
              h1 = __builtin_amdgcn_raw_buffer_load_b128(rs, hoff + 1024, 0, AUX_SC1);
            }
          }
        }
        float a[4] = {__uint_as_float(h0[0]), __uint_as_float(h0[2]), __uint_as_float(h1[0]), __uint_as_float(h1[2])};
#pragma unroll
        for (int i = 0; i < 4; ++i) a[i] = x32_sum(x16_sum(a[i]));
        const float av = g == 0 ? a[0] : g == 1 ? a[1] : g == 2 ? a[2] : a[3];
        const float u = bfr(bfr(av) + lo2f(b1raw));
        const float inner = 0.7978845608028654f * (u + 0.044715f * u * u * u);
        hv = 0.5f * u * (1.0f + tanhf(inner));
      }
      ((u16*)(lds + L_HB))[(c >> 2) * 16 + 4 * (c & 3) + g] = (u16)pack2(hv, 0.f);
      signal(CN(C_HRDY), lane);
    }
    const int h2 = 4 * hk + (kgl >> 2);
    const unsigned p2off = WS_P2 + (((h2 * 4 + (kgl & 3)) * 4 + pw) * 8 + (lane >> 3)) * 256 + (lane & 7) * 32;
    const unsigned mloff = WS_ML + ((h2 * 4 + pw) * 8 + (lane >> 3)) * 128 + (lane & 7) * 16;
    await<1>(CN(C_PUB2), 4u);
    STAMP(5);
    v4u a0 = __builtin_amdgcn_raw_buffer_load_b128(rs, p2off, 0, AUX_SC1);
    v4u a1 = __builtin_amdgcn_raw_buffer_load_b128(rs, p2off + 16, 0, AUX_SC1);
    v4u am = __builtin_amdgcn_raw_buffer_load_b128(rs, mloff, 0, AUX_SC1);
    PIN();
    v4u wof[4];
    float accm[4];
    {
      v2u w2f[4];
#pragma unroll
      for (int i = 0; i < 4; ++i) {
        wof[i] = tr8x4(wov, i);
#pragma unroll
        for (int jj = 0; jj < 2; ++jj)
          w2f[i][jj] = (i & 1) ? perm_hi(w2v[2 * jj][i >> 1], w2v[2 * jj + 1][i >> 1]) : perm_lo(w2v[2 * jj][i >> 1], w2v[2 * jj + 1][i >> 1]);
        KEEP(wof[i]);
      }
      await<1>(CN(C_HRDY), 1u);
      const v2u ha = *(const v2u*)((const u16*)(lds + L_HB) + (c >> 2) * 16 + 4 * g);
#pragma unroll
      for (int i = 0; i < 4; ++i) {
        const f32x4 e = mfma16(ha, w2f[i], f32x4{0.f, 0.f, 0.f, 0.f});
        accm[i] = stg[3] * e[0];
        KEEP(accm[i]);
      }
    }
    PIN();
    STAMP(6);
#pragma clang loop unroll(disable)
    for (unsigned spins = 0; spins < SPIN_CAP; ++spins) {
      const unsigned bad = tagbad(a0, T) | tagbad(a1, T) | tagbad(am, T);
      if (__all(bad == 0)) break;
      asm volatile("" ::: "memory");
      if (bad) {
        a0 = __builtin_amdgcn_raw_buffer_load_b128(rs, p2off, 0, AUX_SC1);
        a1 = __builtin_amdgcn_raw_buffer_load_b128(rs, p2off + 16, 0, AUX_SC1);
        am = __builtin_amdgcn_raw_buffer_load_b128(rs, mloff, 0, AUX_SC1);
      }
    }
    STAMP(7);
    {
      const float m = __uint_as_float(am[0]), l = __uint_as_float(am[2]);
      float M = __builtin_fmaxf(m, dpp<ROR8>(m)); M = x32_max(x16_max(M));
      const float f = __builtin_amdgcn_exp2f(m - M);
      float v[5] = {f * __uint_as_float(a0[0]), f * __uint_as_float(a0[2]), f * __uint_as_float(a1[0]), f * __uint_as_float(a1[2]), f * l};
#pragma unroll
      for (int i = 0; i < 5; ++i) v[i] += dpp<ROR8>(v[i]);
#pragma unroll
      for (int i = 0; i < 5; ++i) v[i] = x16_sum(v[i]);
#pragma unroll
      for (int i = 0; i < 5; ++i) v[i] = x32_sum(v[i]);
      const float inv = __builtin_amdgcn_rcpf(v[4]);
      if (lane < 8) *(v2u*)((u16*)(lds + L_OB) + pw * 32 + 4 * lane) = v2u{pack2(v[0] * inv, v[1] * inv), pack2(v[2] * inv, v[3] * inv)};
    }
    if (arrive(CN(C_O), lane) != 3u) await<0>(CN(C_O), 4u);
    {
      const v4u oa = *(const v4u*)((const u16*)(lds + L_OB) + (c >> 2) * 32 + 8 * g);
      f32x4 d[4];
#pragma unroll
      for (int i = 0; i < 4; ++i) d[i] = mfma32(oa, wof[i], f32x4{0.f, 0.f, 0.f, 0.f});
#pragma unroll
      for (int i = 0; i < 4; ++i) KEEP(d[i]);
      const unsigned off = WS_P3 + ((kg * 2 + cg) * 4 + pw) * 2048 + lane * 16;
      const v4u s0 = {__float_as_uint(fmaf(stg[2], d[0][0], accm[0])), T, __float_as_uint(fmaf(stg[2], d[1][0], accm[1])), T};
      const v4u s1 = {__float_as_uint(fmaf(stg[2], d[2][0], accm[2])), T, __float_as_uint(fmaf(stg[2], d[3][0], accm[3])), T};
      __builtin_amdgcn_raw_buffer_store_b128(s0, rs, off, 0, AUX_SC1);
      __builtin_amdgcn_raw_buffer_store_b128(s1, rs, off + 1024, 0, AUX_SC1);
    }
    PIN();
    __builtin_amdgcn_s_sleep(DLY3);
    signal(CN(C_PUB3), lane);
    STAMP(8);
  }
  STAMP_DUMP();
}

extern "C" void kernel_launch(void* const* d_in, const int* in_sizes, int n_in,
                              void* d_out, int out_size, void* d_ws, size_t ws_size,
                              hipStream_t stream) {
  (void)in_sizes; (void)n_in; (void)out_size; (void)ws_size;
  fused_kernel<<<256, 512, 0, stream>>>(
      (const u16*)d_in[0], (const u16*)d_in[1], (const u16*)d_in[2], (const u16*)d_in[3], (const u16*)d_in[4],
      (const u16*)d_in[6], (const u16*)d_in[7], (const u16*)d_in[8], (const u16*)d_in[9], (const u16*)d_in[10],
      (u16*)d_out, (unsigned char*)d_ws);
}
```

```cpp
#include <hip/hip_runtime.h>
#include <hip/hip_bf16.h>
#include <stdint.h>

#define GAS __attribute__((address_space(1)))
#define LAS __attribute__((address_space(3)))
typedef unsigned short u16;
typedef unsigned v4u __attribute__((ext_vector_type(4)));
typedef unsigned v2u __attribute__((ext_vector_type(2)));
typedef float f32x4 __attribute__((ext_vector_type(4)));
typedef float f32x2 __attribute__((ext_vector_type(2)));
typedef __bf16 bf16x8 __attribute__((ext_vector_type(8)));
typedef __bf16 bf16x2 __attribute__((ext_vector_type(2)));
typedef short s16x4 __attribute__((ext_vector_type(4)));
typedef GAS unsigned gu32;

constexpr int DM = 512, SK = 4096, DH = 128, WINDOW = 1024, FF = 1792, NQ = 4096;
constexpr unsigned WS_QP = 4096;
constexpr unsigned WS_HP = WS_QP + (512u << 10);
constexpr unsigned WS_P2 = WS_HP + (256u << 10);
constexpr unsigned WS_ML = WS_P2 + (1u << 20);
constexpr unsigned WS_P3 = WS_ML + 131072;
constexpr unsigned WS_END = WS_P3 + (2u << 20);
constexpr unsigned SPIN_CAP = 1u << 17;
constexpr unsigned LSPIN_CAP = 1u << 22;
constexpr int AUX_SC1 = 16, AUX_NT = 2;
constexpr float SCL2 = 0.08838834764831845f * 1.4426950408889634f;

constexpr int DLY2 = 17, DLY3 = 19;
constexpr int QGATE = 8;
#define SETPRIO(n) __builtin_amdgcn_s_setprio(n)

__device__ __forceinline__ unsigned pack2(float lo, float hi) {
  return __builtin_bit_cast(unsigned, __builtin_convertvector(f32x2{lo, hi}, bf16x2));
}
__device__ __forceinline__ float bfr(float f) { return (float)(__bf16)f; }
__device__ __forceinline__ float lo2f(unsigned u) { return __builtin_bit_cast(float, u << 16); }
__device__ __forceinline__ float hi2f(unsigned u) { return __builtin_bit_cast(float, u & 0xffff0000u); }
__device__ __forceinline__ unsigned perm_lo(unsigned a, unsigned b) { return __builtin_amdgcn_perm(b, a, 0x05040100u); }
__device__ __forceinline__ unsigned perm_hi(unsigned a, unsigned b) { return __builtin_amdgcn_perm(b, a, 0x07060302u); }

template <int CTRL> __device__ __forceinline__ float dpp(float x) {
  return __builtin_bit_cast(float, __builtin_amdgcn_mov_dpp(__builtin_bit_cast(int, x), CTRL, 0xf, 0xf, true));
}
constexpr int XOR1 = 0xB1, XOR2 = 0x4E, ROR8 = 0x128, ROR4 = 0x124;
__device__ __forceinline__ float x16_max(float x) {
  auto s = __builtin_amdgcn_permlane16_swap(__float_as_uint(x), __float_as_uint(x), false, false);
  return __builtin_fmaxf(__uint_as_float(s[0]), __uint_as_float(s[1]));
}
__device__ __forceinline__ float x32_max(float x) {
  auto s = __builtin_amdgcn_permlane32_swap(__float_as_uint(x), __float_as_uint(x), false, false);
  return __builtin_fmaxf(__uint_as_float(s[0]), __uint_as_float(s[1]));
}
__device__ __forceinline__ float x16_sum(float x) {
  auto s = __builtin_amdgcn_permlane16_swap(__float_as_uint(x), __float_as_uint(x), false, false);
  return __uint_as_float(s[0]) + __uint_as_float(s[1]);
}
__device__ __forceinline__ float x32_sum(float x) {
  auto s = __builtin_amdgcn_permlane32_swap(__float_as_uint(x), __float_as_uint(x), false, false);
  return __uint_as_float(s[0]) + __uint_as_float(s[1]);
}
__device__ __forceinline__ float wave_sum(float x) {
  x += dpp<XOR1>(x); x += dpp<XOR2>(x); x += dpp<ROR4>(x); x += dpp<ROR8>(x);
  return x32_sum(x16_sum(x));
}
__device__ __forceinline__ f32x4 mfma32(v4u a, v4u b, f32x4 c) {
  return __builtin_amdgcn_mfma_f32_16x16x32_bf16(__builtin_bit_cast(bf16x8, a), __builtin_bit_cast(bf16x8, b), c, 0, 0, 0);
}
__device__ __forceinline__ f32x4 mfma16(v2u a, v2u b, f32x4 c) {
  return __builtin_amdgcn_mfma_f32_16x16x16bf16_1k(__builtin_bit_cast(s16x4, a), __builtin_bit_cast(s16x4, b), c, 0, 0, 0);
}
#define PIN() __builtin_amdgcn_sched_barrier(0)
#define BAR() do { PIN(); asm volatile("s_waitcnt lgkmcnt(0)" ::: "memory"); __builtin_amdgcn_s_barrier(); asm volatile("" ::: "memory"); PIN(); } while (0)
#define KEEP(x) asm volatile("" : "+v"(x))

template <typename T> __device__ __forceinline__ T ldg(const u16* p) { return *(const GAS T*)p; }
template <typename T> __device__ __forceinline__ T ldnt(const u16* p) { return __builtin_nontemporal_load((const GAS T*)p); }

constexpr int L_CNT = 0;
constexpr int L_ST = 16;
constexpr int L_RQ = 32;
constexpr int L_RH = L_RQ + 1024;
constexpr int L_QS = L_RH + 1024;
constexpr int L_HB = L_QS + 2048;
constexpr int L_MO = L_HB + 32;
constexpr int L_ML = L_MO + 2048;
constexpr int L_OB = L_ML + 32;
constexpr int L_QB = L_OB + 64;
constexpr int L_FR = L_QB + 256;
constexpr int L_KS = L_FR + 32;
constexpr int L_END = L_KS + 8192;
enum { C_Q = 0, C_QG, C_QRDY, C_ISS, C_ATT, C_PUB2, C_H, C_HRDY, C_O, C_PUB3, C_FIN, C_ST };

__device__ __forceinline__ unsigned arrive(unsigned addr, int lane) {
  unsigned old = 0;
  if (lane == 0)
    asm volatile("s_waitcnt lgkmcnt(0)\n\tds_add_rtn_u32 %0, %1, %2\n\ts_waitcnt lgkmcnt(0)" : "=v"(old) : "v"(addr), "v"(1u) : "memory");
  return __builtin_amdgcn_readfirstlane(old);
}
__device__ __forceinline__ void signal(unsigned addr, int lane) {
  if (lane == 0) asm volatile("s_waitcnt lgkmcnt(0)\n\tds_add_u32 %0, %1" :: "v"(addr), "v"(1u) : "memory");
}
template <int SLEEP> __device__ __forceinline__ void await(unsigned addr, unsigned n) {
#pragma clang loop unroll(disable)
  for (unsigned s = 0; s < LSPIN_CAP; ++s) {
    unsigned v;
    asm volatile("ds_read_b32 %0, %1\n\ts_waitcnt lgkmcnt(0)" : "=v"(v) : "v"(addr) : "memory");
    if ((unsigned)__builtin_amdgcn_readfirstlane(v) >= n) break;
    if (SLEEP) __builtin_amdgcn_s_sleep(SLEEP);
  }
}

__device__ __forceinline__ v4u tr8x4(const v2u (&W)[8], int i) {
  v4u f;
#pragma unroll
  for (int jj = 0; jj < 4; ++jj)
    f[jj] = (i & 1) ? perm_hi(W[2 * jj][i >> 1], W[2 * jj + 1][i >> 1]) : perm_lo(W[2 * jj][i >> 1], W[2 * jj + 1][i >> 1]);
  return f;
}
__device__ __forceinline__ unsigned tagbad(v4u v, unsigned T) { return (v[1] ^ T) | (v[3] ^ T); }

__global__ void __launch_bounds__(512, 2)
fused_kernel(const u16* __restrict__ x, const u16* __restrict__ kcache, const u16* __restrict__ vcache,
             const u16* __restrict__ wq, const u16* __restrict__ wo, const u16* __restrict__ w1,
             const u16* __restrict__ b1, const u16* __restrict__ w2, const u16* __restrict__ b2,
             const u16* __restrict__ gw, u16* __restrict__ out, unsigned char* ws) {
  __shared__ __attribute__((aligned(16))) float lds[L_END];
  asm volatile("" :: "s"(x), "s"(kcache), "s"(vcache), "s"(wq), "s"(wo), "s"(w1), "s"(b1), "s"(w2), "s"(b2), "s"(gw), "s"(out), "s"(ws));
  const int tid = threadIdx.x, lane = tid & 63, w = __builtin_amdgcn_readfirstlane(tid >> 6);
  const int g = lane >> 4, c = lane & 15;
  const int bid = blockIdx.x, hk = bid & 7, idx = bid >> 3;
  const int ab = idx >> 3, as = idx & 7;
  const int kgl = idx >> 1, cg = idx & 1, kg = hk * 16 + kgl;
  const __amdgpu_buffer_rsrc_t rs = __builtin_amdgcn_make_buffer_rsrc((void*)ws, 0, (int)WS_END, 0x00020000);
  const unsigned cn = (unsigned)(size_t)(LAS float*)(lds + L_CNT);
#define CN(i) (cn + 4u * (i))

  if (w >= 4) {
    const int aw = w - 4;
    BAR();
    unsigned tt;
    {
      const unsigned char* tp = ws;
      const int fst = 16 * (14 * hk + (kgl < 14 ? kgl : 13));
      tp = lane == 0 ? (const unsigned char*)(out + (bid >> 6) * DM) : tp;
      tp = lane == 1 ? ws + WS_END + 64 : tp;
      tp = lane == 2 ? (const unsigned char*)b2 : tp;
      tp = lane == 3 ? (const unsigned char*)(wo + (size_t)(32 * kg) * DM) : tp;
      tp = lane == 4 ? (const unsigned char*)(wo + (size_t)(32 * kg + 31) * DM + 510) : tp;
      tp = lane == 5 ? (const unsigned char*)(w2 + (size_t)fst * DM + 256 * cg) : tp;
      tp = lane == 6 ? (const unsigned char*)(w2 + (size_t)(fst + 15) * DM + 256 * cg) : tp;
      tp = lane == 7 ? (const unsigned char*)(b1 + fst) : tp;
      tt = *(const GAS unsigned*)tp;
    }
    PIN();
    v4u kr[8], vf[8];
    {
      const size_t kvoff = ((size_t)(ab * 8 + hk) * SK + (SK - WINDOW) + 128 * as + 32 * aw) * DH;
      const u16* kp = kcache + kvoff + 8 * lane;
      const u16* vp = vcache + kvoff + 4 * g * DH + 8 * c;
#pragma unroll
      for (int i = 0; i < 8; ++i) kr[i] = ldnt<v4u>(kp + i * 512);
#pragma unroll
      for (int j = 0; j < 8; ++j) vf[j] = ldnt<v4u>(vp + (16 * (j >> 2) + (j & 3)) * DH);
    }
    PIN();
    SETPRIO(2);
    const unsigned T = (unsigned)__builtin_amdgcn_readlane(tt, 63) + 1u;
    v4u kf[8];
    {
      unsigned char* ks_ = (unsigned char*)(lds + L_KS) + aw * 8192;
#pragma unroll
      for (int i = 0; i < 8; ++i) { const int row = 4 * i + g; *(v4u*)(ks_ + row * 256 + ((c ^ (row & 15)) << 4)) = kr[i]; }
      asm volatile("s_waitcnt lgkmcnt(0)" ::: "memory");
#pragma unroll
      for (int t = 0; t < 2; ++t)
#pragma unroll
        for (int ks = 0; ks < 4; ++ks) kf[t * 4 + ks] = *(const v4u*)(ks_ + (16 * t + c) * 256 + (((4 * ks + g) ^ c) << 4));
#pragma unroll
      for (int i = 0; i < 8; ++i) KEEP(kf[i]);
    }
    PIN();
    asm volatile("s_waitcnt vmcnt(%0)" :: "n"(QGATE) : "memory");
    signal(CN(C_ISS), lane);
    await<0>(CN(C_QRDY), 1u);
    v4u pf;
    {
      f32x4 s0 = {0.f, 0.f, 0.f, 0.f}, s1 = {0.f, 0.f, 0.f, 0.f};
      const u16* qb = (const u16*)(lds + L_QB) + (c >> 2) * 128 + 8 * g;
      v4u qf[4];
#pragma unroll
      for (int ks = 0; ks < 4; ++ks) qf[ks] = *(const v4u*)(qb + 32 * ks);
#pragma unroll
      for (int ks = 0; ks < 4; ++ks) {
        s0 = mfma32(kf[ks], qf[ks], s0);
        s1 = mfma32(kf[4 + ks], qf[ks], s1);
      }
      float m = __builtin_fmaxf(__builtin_fmaxf(__builtin_fmaxf(s0[0], s0[1]), __builtin_fmaxf(s0[2], s0[3])),
                                __builtin_fmaxf(__builtin_fmaxf(s1[0], s1[1]), __builtin_fmaxf(s1[2], s1[3])));
      m = x32_max(x16_max(m));
      float p[8];
#pragma unroll
      for (int j = 0; j < 4; ++j) { p[j] = __builtin_amdgcn_exp2f((s0[j] - m) * SCL2); p[4 + j] = __builtin_amdgcn_exp2f((s1[j] - m) * SCL2); }
      pf = v4u{pack2(p[0], p[1]), pack2(p[2], p[3]), pack2(p[4], p[5]), pack2(p[6], p[7])};
      float l = ((p[0] + p[1]) + (p[2] + p[3])) + ((p[4] + p[5]) + (p[6] + p[7]));
      l = x32_sum(x16_sum(l));
      if (g == 0 && (c & 3) == 0) *(f32x2*)(lds + L_ML + (aw * 4 + (c >> 2)) * 2) = f32x2{m * SCL2, l};
      KEEP(pf);
    }
    PIN();
    {
      v4u vt[8];
#pragma unroll
      for (int i = 0; i < 8; ++i)
#pragma unroll
        for (int jj = 0; jj < 4; ++jj)
          vt[i][jj] = (i & 1) ? perm_hi(vf[2 * jj][i >> 1], vf[2 * jj + 1][i >> 1]) : perm_lo(vf[2 * jj][i >> 1], vf[2 * jj + 1][i >> 1]);
      f32x4 o[8];
#pragma unroll
      for (int i = 0; i < 8; ++i) o[i] = mfma32(pf, vt[i], f32x4{0.f, 0.f, 0.f, 0.f});
#pragma unroll
      for (int i = 0; i < 8; ++i) KEEP(o[i]);
      float* mo = lds + L_MO + (aw * 4 + g) * 128 + 8 * c;
      *(f32x4*)(mo) = f32x4{o[0][0], o[1][0], o[2][0], o[3][0]};
      *(f32x4*)(mo + 4) = f32x4{o[4][0], o[5][0], o[6][0], o[7][0]};
    }
    if (arrive(CN(C_ATT), lane) != 3u) await<0>(CN(C_ATT), 4u);
    {
      f32x2 ml[4], ov[4];
#pragma unroll
      for (int u = 0; u < 4; ++u) { ml[u] = *(const f32x2*)(lds + L_ML + (u * 4 + aw) * 2); ov[u] = *(const f32x2*)(lds + L_MO + (u * 4 + aw) * 128 + 2 * lane); }
      const float M = __builtin_fmaxf(__builtin_fmaxf(ml[0][0], ml[1][0]), __builtin_fmaxf(ml[2][0], ml[3][0]));
      float L = 0.f, o0 = 0.f, o1 = 0.f;
#pragma unroll
      for (int u = 0; u < 4; ++u) {
        const float f = __builtin_amdgcn_exp2f(ml[u][0] - M);
        L = fmaf(f, ml[u][1], L); o0 = fmaf(f, ov[u][0], o0); o1 = fmaf(f, ov[u][1], o1);
      }
      const int hg = 4 * hk + aw;
      const v4u pv = {__float_as_uint(o0), T, __float_as_uint(o1), T};
      __builtin_amdgcn_raw_buffer_store_b128(pv, rs, WS_P2 + (((hg * 4 + (lane >> 4)) * 4 + ab) * 8 + as) * 256 + (lane & 15) * 16, 0, AUX_SC1);
      if (lane < 8) {
        const v4u mv = {__float_as_uint(M), T, __float_as_uint(L), T};
        __builtin_amdgcn_raw_buffer_store_b128(mv, rs, WS_ML + ((hg * 4 + ab) * 8 + as) * 128 + lane * 16, 0, AUX_SC1);
      }
    }
    PIN();
    __builtin_amdgcn_s_sleep(DLY2);
    signal(CN(C_PUB2), lane);
    const int ob = bid >> 6, ocw = bid & 7, om = (bid >> 4) & 3, oh = (bid >> 3) & 1;
    const int col = 64 * ocw + 4 * (4 * om + (lane & 3)) + 2 * oh;
    const unsigned b2v = ldg<unsigned>(b2 + col);
    const unsigned off = WS_P3 + ((aw * 32 + (lane >> 2)) * 8 + ocw) * 2048 + oh * 1024 + (ob * 16 + 4 * om + (lane & 3)) * 16;
    await<1>(CN(C_PUB3), 4u);
    v4u pv0 = __builtin_amdgcn_raw_buffer_load_b128(rs, off, 0, AUX_SC1);
    v4u pv1 = __builtin_amdgcn_raw_buffer_load_b128(rs, off + 16 * 8 * 2048, 0, AUX_SC1);
#pragma clang loop unroll(disable)
    for (unsigned spins = 0; spins < SPIN_CAP; ++spins) {
      const unsigned bad0 = tagbad(pv0, T), bad1 = tagbad(pv1, T);
      if (__all((bad0 | bad1) == 0)) break;
      asm volatile("" ::: "memory");
      if (bad0) pv0 = __builtin_amdgcn_raw_buffer_load_b128(rs, off, 0, AUX_SC1);
      if (bad1) pv1 = __builtin_amdgcn_raw_buffer_load_b128(rs, off + 16 * 8 * 2048, 0, AUX_SC1);
    }
    float sa = __uint_as_float(pv0[0]) + __uint_as_float(pv1[0]), sb = __uint_as_float(pv0[2]) + __uint_as_float(pv1[2]);
    sa += dpp<ROR4>(sa); sb += dpp<ROR4>(sb); sa += dpp<ROR8>(sa); sb += dpp<ROR8>(sb);
    sa = x16_sum(sa); sb = x16_sum(sb); sa = x32_sum(sa); sb = x32_sum(sb);
    if (lane < 4) *(f32x2*)(lds + L_FR + aw * 8 + 2 * lane) = f32x2{sa, sb};
    if (arrive(CN(C_FIN), lane) == 3u) {
      if (lane < 4) {
        const float omg = lds[L_ST + 4 * ob + 3];
        f32x2 t = *(const f32x2*)(lds + L_FR + 2 * lane);
#pragma unroll
        for (int u = 1; u < 4; ++u) t += *(const f32x2*)(lds + L_FR + u * 8 + 2 * lane);
        *(GAS unsigned*)(out + ob * DM + col) = pack2(fmaf(omg, lo2f(b2v), t[0]), fmaf(omg, hi2f(b2v), t[1]));
      }
      if (bid == 0 && lane == 0) __hip_atomic_store((gu32*)ws, T, __ATOMIC_RELAXED, __HIP_MEMORY_SCOPE_AGENT);
    }
  } else {
    const int pw = w;
    SETPRIO(3);
    const int kq = idx & 3, cq = idx >> 2;
    const bool has_ht = idx < 14;
    const int th = 14 * hk + idx;
    const int qrow = 128 * kq + 32 * pw + 8 * g, hrow = 128 * (th & 3) + 32 * pw + 8 * g;
    v2u wv[8], w1v[8];
    {
      const u16* p = wq + (size_t)qrow * NQ + hk * 512 + 64 * cq + 4 * c;
#pragma unroll
      for (int j = 0; j < 8; ++j) wv[j] = ldnt<v2u>(p + j * NQ);
    }
    const v4u xa = ldg<v4u>(x + (c >> 2) * DM + qrow);
    const unsigned ev = __builtin_amdgcn_raw_buffer_load_b32(rs, 0, 0, 0);
    PIN();
    const v4u xs = ldg<v4u>(x + pw * DM + 8 * lane);
    const v4u gwv = ldg<v4u>(gw + 8 * lane);
    const v4u xh = ldg<v4u>(x + (c >> 2) * DM + hrow);
    {
      const __amdgpu_buffer_rsrc_t rw1 = __builtin_amdgcn_make_buffer_rsrc((void*)w1, 0, has_ht ? DM * FF * 2 : 0, 0x00020000);
      const int o = (hrow * FF + 64 * (th >> 2) + 4 * c) * 2;
#pragma unroll
      for (int j = 0; j < 8; ++j) w1v[j] = __builtin_amdgcn_raw_buffer_load_b64(rw1, o + j * FF * 2, 0, AUX_NT);
    }
    if (tid < 16) ((LAS unsigned*)(lds + L_CNT))[tid] = 0u;
    BAR();
    PIN();
    unsigned T;
    {
      f32x4 d[4];
#pragma unroll
      for (int i = 0; i < 4; ++i) d[i] = mfma32(xa, tr8x4(wv, i), f32x4{0.f, 0.f, 0.f, 0.f});
      *(f32x4*)(lds + L_RQ + (pw * 64 + lane) * 4) = f32x4{d[0][0], d[1][0], d[2][0], d[3][0]};
      T = (unsigned)__builtin_amdgcn_readfirstlane(ev) + 1u;
      if (arrive(CN(C_Q), lane) == 3u) {
        f32x4 a = *(const f32x4*)(lds + L_RQ + lane * 4);
#pragma unroll
        for (int p = 1; p < 4; ++p) a += *(const f32x4*)(lds + L_RQ + (p * 64 + lane) * 4);
        const unsigned off = WS_QP + ((hk * 8 + cq) * 4 + kq) * 2048 + lane * 16;
        __builtin_amdgcn_raw_buffer_store_b128(v4u{__float_as_uint(a[0]), T, __float_as_uint(a[1]), T}, rs, off, 0, AUX_SC1);
        __builtin_amdgcn_raw_buffer_store_b128(v4u{__float_as_uint(a[2]), T, __float_as_uint(a[3]), T}, rs, off + 1024, 0, AUX_SC1);
      }
    }
    PIN();
    {
      float xv[8], gv[8];
#pragma unroll
      for (int k = 0; k < 4; ++k) { xv[2 * k] = lo2f(xs[k]); xv[2 * k + 1] = hi2f(xs[k]); gv[2 * k] = lo2f(gwv[k]); gv[2 * k + 1] = hi2f(gwv[k]); }
      float s = 0.f, gs = 0.f;
#pragma unroll
      for (int k = 0; k < 8; ++k) { s += xv[k]; gs += bfr(xv[k] * gv[k]); }
      s = wave_sum(s); gs = wave_sum(gs);
      const float mean = s * (1.0f / DM);
      float vs = 0.f;
#pragma unroll
      for (int k = 0; k < 8; ++k) { const float d = xv[k] - mean; vs = fmaf(d, d, vs); }
      vs = wave_sum(vs);
      if (lane == 0) {
        const float var_b = bfr(vs * (1.0f / DM));
        const float den = bfr(sqrtf(bfr(var_b + 1e-5f)));
        const float gt = bfr(1.0f / (1.0f + __expf(-bfr(gs))));
        *(f32x4*)(lds + L_ST + 4 * pw) = f32x4{bfr(mean), den, gt, bfr(1.0f - gt)};
      }
      if (arrive(CN(C_ST), lane) != 3u) await<0>(CN(C_ST), 4u);
    }
    if (has_ht) {
      const f32x4 st = *(const f32x4*)(lds + L_ST + 4 * (c >> 2));
      v4u ya;
#pragma unroll
      for (int k = 0; k < 4; ++k) ya[k] = pack2(bfr(lo2f(xh[k]) - st[0]) / st[1], bfr(hi2f(xh[k]) - st[0]) / st[1]);
      f32x4 d[4];
#pragma unroll
      for (int i = 0; i < 4; ++i) d[i] = mfma32(ya, tr8x4(w1v, i), f32x4{0.f, 0.f, 0.f, 0.f});
      *(f32x4*)(lds + L_RH + (pw * 64 + lane) * 4) = f32x4{d[0][0], d[1][0], d[2][0], d[3][0]};
      if (arrive(CN(C_H), lane) == 3u) {
        f32x4 a = *(const f32x4*)(lds + L_RH + lane * 4);
#pragma unroll
        for (int p = 1; p < 4; ++p) a += *(const f32x4*)(lds + L_RH + (p * 64 + lane) * 4);
        const unsigned off = WS_HP + th * 2048 + lane * 16;
        __builtin_amdgcn_raw_buffer_store_b128(v4u{__float_as_uint(a[0]), T, __float_as_uint(a[1]), T}, rs, off, 0, AUX_SC1);
        __builtin_amdgcn_raw_buffer_store_b128(v4u{__float_as_uint(a[2]), T, __float_as_uint(a[3]), T}, rs, off + 1024, 0, AUX_SC1);
      }
    }
    f32x4 stg = *(const f32x4*)(lds + L_ST + 4 * g);
    KEEP(stg);
    PIN();
    await<2>(CN(C_ISS), 4u);
    const bool has_mlp = kgl < 14;
    const int fs = 14 * hk + kgl, f0 = 16 * fs;
    v2u wov[8], w2v[4];
    {
      const unsigned qoff = WS_QP + ((hk * 8 + (lane >> 5)) * 4 + pw) * 2048 + ((lane >> 4) & 1) * 1024 + (16 * ab + c) * 16;
      v4u qv[4];
#pragma unroll
      for (int i = 0; i < 4; ++i) qv[i] = __builtin_amdgcn_raw_buffer_load_b128(rs, qoff + i * 16384, 0, AUX_SC1);
      PIN();
      unsigned bad0 = tagbad(qv[0], T) | tagbad(qv[1], T) | tagbad(qv[2], T) | tagbad(qv[3], T);
      if (!__all(bad0 == 0)) {
#pragma clang loop unroll(disable)
        for (unsigned spins = 0; spins < SPIN_CAP; ++spins) {
          unsigned bad[4];
#pragma unroll
          for (int i = 0; i < 4; ++i) bad[i] = tagbad(qv[i], T);
          if (__all((bad[0] | bad[1] | bad[2] | bad[3]) == 0)) break;
          asm volatile("" ::: "memory");
#pragma unroll
          for (int i = 0; i < 4; ++i) if (bad[i]) qv[i] = __builtin_amdgcn_raw_buffer_load_b128(rs, qoff + i * 16384, 0, AUX_SC1);
        }
      }
      float* qs = lds + L_QS + (pw * 64 + lane) * 8;
      *(v4u*)(qs) = v4u{qv[0][0], qv[0][2], qv[1][0], qv[1][2]};
      *(v4u*)(qs + 4) = v4u{qv[2][0], qv[2][2], qv[3][0], qv[3][2]};
      if (arrive(CN(C_QG), lane) == 3u) {
        f32x4 a0 = *(const f32x4*)(lds + L_QS + lane * 8), a1 = *(const f32x4*)(lds + L_QS + lane * 8 + 4);
#pragma unroll
        for (int p = 1; p < 4; ++p) { a0 += *(const f32x4*)(lds + L_QS + (p * 64 + lane) * 8); a1 += *(const f32x4*)(lds + L_QS + (p * 64 + lane) * 8 + 4); }
        unsigned* qb = (unsigned*)(lds + L_QB) + 32 * (lane >> 5) + 2 * c + ((lane >> 4) & 1);
        qb[0] = pack2(a0[0], a0[1]); qb[64] = pack2(a0[2], a0[3]); qb[128] = pack2(a1[0], a1[1]); qb[192] = pack2(a1[2], a1[3]);
        signal(CN(C_QRDY), lane);
      }
    }
    const bool hgat = pw == 3 && has_mlp;
    const unsigned hoff = WS_HP + ((fs >> 2) * 4 + g) * 2048 + (16 * (c >> 2) + 4 * (fs & 3) + (c & 3)) * 16;
    v4u h0, h1;
    unsigned b1raw;
    {
      const __amdgpu_buffer_rsrc_t rsh = __builtin_amdgcn_make_buffer_rsrc((void*)ws, 0, hgat ? (int)WS_END : 0, 0x00020000);
      const __amdgpu_buffer_rsrc_t rb1 = __builtin_amdgcn_make_buffer_rsrc((void*)b1, 0, hgat ? FF * 2 : 0, 0x00020000);
      h0 = __builtin_amdgcn_raw_buffer_load_b128(rsh, hoff, 0, AUX_SC1);
      h1 = __builtin_amdgcn_raw_buffer_load_b128(rsh, hoff + 1024, 0, AUX_SC1);
      b1raw = __builtin_amdgcn_raw_buffer_load_b16(rb1, (f0 + 4 * (c & 3) + g) * 2, 0, 0);
      PIN();
      const __amdgpu_buffer_rsrc_t rw2 = __builtin_amdgcn_make_buffer_rsrc((void*)w2, 0, has_mlp ? DM * FF * 2 : 0, 0x00020000);
      const u16* p = wo + (size_t)(32 * kg + 8 * g) * DM + 256 * cg + 64 * pw + 4 * c;
      const int o2 = ((f0 + 4 * g) * DM + 256 * cg + 64 * pw + 4 * c) * 2;
#pragma unroll
      for (int j = 0; j < 8; ++j) wov[j] = ldnt<v2u>(p + j * DM);
#pragma unroll
      for (int j = 0; j < 4; ++j) w2v[j] = __builtin_amdgcn_raw_buffer_load_b64(rw2, o2 + j * DM * 2, 0, AUX_NT);
    }
    PIN();
    if (pw == 3) {
      float hv = 0.f;
      if (has_mlp) {
        if (!__all((tagbad(h0, T) | tagbad(h1, T)) == 0)) {
#pragma clang loop unroll(disable)
          for (unsigned spins = 0; spins < SPIN_CAP; ++spins) {
            const unsigned bad = tagbad(h0, T) | tagbad(h1, T);
            if (__all(bad == 0)) break;
            asm volatile("" ::: "memory");
            if (bad) {
              h0 = __builtin_amdgcn_raw_buffer_load_b128(rs, hoff, 0, AUX_SC1);
              h1 = __builtin_amdgcn_raw_buffer_load_b128(rs, hoff + 1024, 0, AUX_SC1);
            }
          }
        }
        float a[4] = {__uint_as_float(h0[0]), __uint_as_float(h0[2]), __uint_as_float(h1[0]), __uint_as_float(h1[2])};
#pragma unroll
        for (int i = 0; i < 4; ++i) a[i] = x32_sum(x16_sum(a[i]));
        const float av = g == 0 ? a[0] : g == 1 ? a[1] : g == 2 ? a[2] : a[3];
        const float u = bfr(bfr(av) + lo2f(b1raw));
        const float inner = 0.7978845608028654f * (u + 0.044715f * u * u * u);
        hv = 0.5f * u * (1.0f + tanhf(inner));
      }
      ((u16*)(lds + L_HB))[(c >> 2) * 16 + 4 * (c & 3) + g] = (u16)pack2(hv, 0.f);
      signal(CN(C_HRDY), lane);
    }
    const int h2 = 4 * hk + (kgl >> 2);
    const unsigned p2off = WS_P2 + (((h2 * 4 + (kgl & 3)) * 4 + pw) * 8 + (lane >> 3)) * 256 + (lane & 7) * 32;
    const unsigned mloff = WS_ML + ((h2 * 4 + pw) * 8 + (lane >> 3)) * 128 + (lane & 7) * 16;
    await<1>(CN(C_PUB2), 4u);
    v4u a0 = __builtin_amdgcn_raw_buffer_load_b128(rs, p2off, 0, AUX_SC1);
    v4u a1 = __builtin_amdgcn_raw_buffer_load_b128(rs, p2off + 16, 0, AUX_SC1);
    v4u am = __builtin_amdgcn_raw_buffer_load_b128(rs, mloff, 0, AUX_SC1);
    PIN();
    v4u wof[4];
    float accm[4];
    {
      v2u w2f[4];
#pragma unroll
      for (int i = 0; i < 4; ++i) {
        wof[i] = tr8x4(wov, i);
#pragma unroll
        for (int jj = 0; jj < 2; ++jj)
          w2f[i][jj] = (i & 1) ? perm_hi(w2v[2 * jj][i >> 1], w2v[2 * jj + 1][i >> 1]) : perm_lo(w2v[2 * jj][i >> 1], w2v[2 * jj + 1][i >> 1]);
        KEEP(wof[i]);
      }
      await<1>(CN(C_HRDY), 1u);
      const v2u ha = *(const v2u*)((const u16*)(lds + L_HB) + (c >> 2) * 16 + 4 * g);
#pragma unroll
      for (int i = 0; i < 4; ++i) {
        const f32x4 e = mfma16(ha, w2f[i], f32x4{0.f, 0.f, 0.f, 0.f});
        accm[i] = stg[3] * e[0];
        KEEP(accm[i]);
      }
    }
    PIN();
#pragma clang loop unroll(disable)
    for (unsigned spins = 0; spins < SPIN_CAP; ++spins) {
      const unsigned bad = tagbad(a0, T) | tagbad(a1, T) | tagbad(am, T);
      if (__all(bad == 0)) break;
      asm volatile("" ::: "memory");
      if (bad) {
        a0 = __builtin_amdgcn_raw_buffer_load_b128(rs, p2off, 0, AUX_SC1);
        a1 = __builtin_amdgcn_raw_buffer_load_b128(rs, p2off + 16, 0, AUX_SC1);
        am = __builtin_amdgcn_raw_buffer_load_b128(rs, mloff, 0, AUX_SC1);
      }
    }
    {
      const float m = __uint_as_float(am[0]), l = __uint_as_float(am[2]);
      float M = __builtin_fmaxf(m, dpp<ROR8>(m)); M = x32_max(x16_max(M));
      const float f = __builtin_amdgcn_exp2f(m - M);
      float v[5] = {f * __uint_as_float(a0[0]), f * __uint_as_float(a0[2]), f * __uint_as_float(a1[0]), f * __uint_as_float(a1[2]), f * l};
#pragma unroll
      for (int i = 0; i < 5; ++i) v[i] += dpp<ROR8>(v[i]);
#pragma unroll
      for (int i = 0; i < 5; ++i) v[i] = x16_sum(v[i]);
#pragma unroll
      for (int i = 0; i < 5; ++i) v[i] = x32_sum(v[i]);
      const float inv = __builtin_amdgcn_rcpf(v[4]);
      if (lane < 8) *(v2u*)((u16*)(lds + L_OB) + pw * 32 + 4 * lane) = v2u{pack2(v[0] * inv, v[1] * inv), pack2(v[2] * inv, v[3] * inv)};
    }
    if (arrive(CN(C_O), lane) != 3u) await<0>(CN(C_O), 4u);
    {
      const v4u oa = *(const v4u*)((const u16*)(lds + L_OB) + (c >> 2) * 32 + 8 * g);
      f32x4 d[4];
#pragma unroll
      for (int i = 0; i < 4; ++i) d[i] = mfma32(oa, wof[i], f32x4{0.f, 0.f, 0.f, 0.f});
#pragma unroll
      for (int i = 0; i < 4; ++i) KEEP(d[i]);
      const unsigned off = WS_P3 + ((kg * 2 + cg) * 4 + pw) * 2048 + lane * 16;
      const v4u s0 = {__float_as_uint(fmaf(stg[2], d[0][0], accm[0])), T, __float_as_uint(fmaf(stg[2], d[1][0], accm[1])), T};
      const v4u s1 = {__float_as_uint(fmaf(stg[2], d[2][0], accm[2])), T, __float_as_uint(fmaf(stg[2], d[3][0], accm[3])), T};
      __builtin_amdgcn_raw_buffer_store_b128(s0, rs, off, 0, AUX_SC1);
      __builtin_amdgcn_raw_buffer_store_b128(s1, rs, off + 1024, 0, AUX_SC1);
    }
    PIN();
    __builtin_amdgcn_s_sleep(DLY3);
    signal(CN(C_PUB3), lane);
  }
}

extern "C" void kernel_launch(void* const* d_in, const int* in_sizes, int n_in,
                              void* d_out, int out_size, void* d_ws, size_t ws_size,
                              hipStream_t stream) {
  (void)in_sizes; (void)n_in; (void)out_size; (void)ws_size;
  fused_kernel<<<256, 512, 0, stream>>>(
      (const u16*)d_in[0], (const u16*)d_in[1], (const u16*)d_in[2], (const u16*)d_in[3], (const u16*)d_in[4],
      (const u16*)d_in[6], (const u16*)d_in[7], (const u16*)d_in[8], (const u16*)d_in[9], (const u16*)d_in[10],
      (u16*)d_out, (unsigned char*)d_ws);
}
```

```cpp
#include <hip/hip_runtime.h>
#include <hip/hip_bf16.h>
#include <stdint.h>

#define GAS __attribute__((address_space(1)))
#define LAS __attribute__((address_space(3)))
typedef unsigned short u16;
typedef unsigned v4u __attribute__((ext_vector_type(4)));
typedef unsigned v2u __attribute__((ext_vector_type(2)));
typedef float f32x4 __attribute__((ext_vector_type(4)));
typedef float f32x2 __attribute__((ext_vector_type(2)));
typedef __bf16 bf16x8 __attribute__((ext_vector_type(8)));
typedef __bf16 bf16x2 __attribute__((ext_vector_type(2)));
typedef short s16x4 __attribute__((ext_vector_type(4)));
typedef GAS unsigned gu32;

constexpr int DM = 512, SK = 4096, DH = 128, WINDOW = 1024, FF = 1792, NQ = 4096;
constexpr unsigned WS_QP = 4096;
constexpr unsigned WS_HP = WS_QP + (512u << 10);
constexpr unsigned WS_P2 = WS_HP + (256u << 10);
constexpr unsigned WS_ML = WS_P2 + (1u << 20);
constexpr unsigned WS_P3 = WS_ML + 131072;
constexpr unsigned WS_END = WS_P3 + (2u << 20);
constexpr unsigned SPIN_CAP = 1u << 17;
constexpr unsigned LSPIN_CAP = 1u << 22;
constexpr int AUX_SC1 = 16, AUX_NT = 2;
constexpr float SCALE = 0.08838834764831845f, LOG2E = 1.4426950408889634f;

constexpr int DLY2 = 17, DLY3 = 19;
constexpr int QGATE = 8;
#define SETPRIO(n) __builtin_amdgcn_s_setprio(n)

__device__ __forceinline__ unsigned pack2(float lo, float hi) {
  return __builtin_bit_cast(unsigned, __builtin_convertvector(f32x2{lo, hi}, bf16x2));
}
__device__ __forceinline__ float bfr(float f) { return (float)(__bf16)f; }
__device__ __forceinline__ float lo2f(unsigned u) { return __builtin_bit_cast(float, u << 16); }
__device__ __forceinline__ float hi2f(unsigned u) { return __builtin_bit_cast(float, u & 0xffff0000u); }
__device__ __forceinline__ unsigned perm_lo(unsigned a, unsigned b) { return __builtin_amdgcn_perm(b, a, 0x05040100u); }
__device__ __forceinline__ unsigned perm_hi(unsigned a, unsigned b) { return __builtin_amdgcn_perm(b, a, 0x07060302u); }

template <int CTRL> __device__ __forceinline__ float dpp(float x) {
  return __builtin_bit_cast(float, __builtin_amdgcn_mov_dpp(__builtin_bit_cast(int, x), CTRL, 0xf, 0xf, true));
}
constexpr int XOR1 = 0xB1, XOR2 = 0x4E, ROR8 = 0x128, ROR4 = 0x124;
__device__ __forceinline__ float x16_max(float x) {
  auto s = __builtin_amdgcn_permlane16_swap(__float_as_uint(x), __float_as_uint(x), false, false);
  return __builtin_fmaxf(__uint_as_float(s[0]), __uint_as_float(s[1]));
}
__device__ __forceinline__ float x32_max(float x) {
  auto s = __builtin_amdgcn_permlane32_swap(__float_as_uint(x), __float_as_uint(x), false, false);
  return __builtin_fmaxf(__uint_as_float(s[0]), __uint_as_float(s[1]));
}
__device__ __forceinline__ float x16_sum(float x) {
  auto s = __builtin_amdgcn_permlane16_swap(__float_as_uint(x), __float_as_uint(x), false, false);
  return __uint_as_float(s[0]) + __uint_as_float(s[1]);
}
__device__ __forceinline__ float x32_sum(float x) {
  auto s = __builtin_amdgcn_permlane32_swap(__float_as_uint(x), __float_as_uint(x), false, false);
  return __uint_as_float(s[0]) + __uint_as_float(s[1]);
}
__device__ __forceinline__ float wave_sum(float x) {
  x += dpp<XOR1>(x); x += dpp<XOR2>(x); x += dpp<ROR4>(x); x += dpp<ROR8>(x);
  return x32_sum(x16_sum(x));
}
__device__ __forceinline__ f32x4 mfma32(v4u a, v4u b, f32x4 c) {
  return __builtin_amdgcn_mfma_f32_16x16x32_bf16(__builtin_bit_cast(bf16x8, a), __builtin_bit_cast(bf16x8, b), c, 0, 0, 0);
}
__device__ __forceinline__ f32x4 mfma16(v2u a, v2u b, f32x4 c) {
  return __builtin_amdgcn_mfma_f32_16x16x16bf16_1k(__builtin_bit_cast(s16x4, a), __builtin_bit_cast(s16x4, b), c, 0, 0, 0);
}
#define PIN() __builtin_amdgcn_sched_barrier(0)
#define BAR() do { PIN(); asm volatile("s_waitcnt lgkmcnt(0)" ::: "memory"); __builtin_amdgcn_s_barrier(); asm volatile("" ::: "memory"); PIN(); } while (0)
#define KEEP(x) asm volatile("" : "+v"(x))

template <typename T> __device__ __forceinline__ T ldg(const u16* p) { return *(const GAS T*)p; }
template <typename T> __device__ __forceinline__ T ldnt(const u16* p) { return __builtin_nontemporal_load((const GAS T*)p); }

constexpr int L_CNT = 0;
constexpr int L_ST = 16;
constexpr int L_RQ = 32;
constexpr int L_RH = L_RQ + 1024;
constexpr int L_QS = L_RH + 1024;
constexpr int L_HB = L_QS + 2048;
constexpr int L_MO = L_HB + 32;
constexpr int L_ML = L_MO + 2048;
constexpr int L_OB = L_ML + 32;
constexpr int L_QB = L_OB + 64;
constexpr int L_FR = L_QB + 256;
constexpr int L_KS = L_FR + 32;
constexpr int L_END = L_KS + 8192;
enum { C_Q = 0, C_QG, C_QRDY, C_ISS, C_ATT, C_PUB2, C_H, C_HRDY, C_O, C_PUB3, C_FIN, C_ST };

__device__ __forceinline__ unsigned arrive(unsigned addr, int lane) {
  unsigned old = 0;
  if (lane == 0)
    asm volatile("s_waitcnt lgkmcnt(0)\n\tds_add_rtn_u32 %0, %1, %2\n\ts_waitcnt lgkmcnt(0)" : "=v"(old) : "v"(addr), "v"(1u) : "memory");
  return __builtin_amdgcn_readfirstlane(old);
}
__device__ __forceinline__ void signal(unsigned addr, int lane) {
  if (lane == 0) asm volatile("s_waitcnt lgkmcnt(0)\n\tds_add_u32 %0, %1" :: "v"(addr), "v"(1u) : "memory");
}
template <int SLEEP> __device__ __forceinline__ void await(unsigned addr, unsigned n) {
#pragma clang loop unroll(disable)
  for (unsigned s = 0; s < LSPIN_CAP; ++s) {
    unsigned v;
    asm volatile("ds_read_b32 %0, %1\n\ts_waitcnt lgkmcnt(0)" : "=v"(v) : "v"(addr) : "memory");
    if ((unsigned)__builtin_amdgcn_readfirstlane(v) >= n) break;
    if (SLEEP) __builtin_amdgcn_s_sleep(SLEEP);
  }
}

__device__ __forceinline__ v4u tr8x4(const v2u (&W)[8], int i) {
  v4u f;
#pragma unroll
  for (int jj = 0; jj < 4; ++jj)
    f[jj] = (i & 1) ? perm_hi(W[2 * jj][i >> 1], W[2 * jj + 1][i >> 1]) : perm_lo(W[2 * jj][i >> 1], W[2 * jj + 1][i >> 1]);
  return f;
}
__device__ __forceinline__ unsigned tagbad(v4u v, unsigned T) { return (v[1] ^ T) | (v[3] ^ T); }

__global__ void __launch_bounds__(512, 2)
fused_kernel(const u16* __restrict__ x, const u16* __restrict__ kcache, const u16* __restrict__ vcache,
             const u16* __restrict__ wq, const u16* __restrict__ wo, const u16* __restrict__ w1,
             const u16* __restrict__ b1, const u16* __restrict__ w2, const u16* __restrict__ b2,
             const u16* __restrict__ gw, u16* __restrict__ out, unsigned char* ws) {
  __shared__ __attribute__((aligned(16))) float lds[L_END];
  asm volatile("" :: "s"(x), "s"(kcache), "s"(vcache), "s"(wq), "s"(wo), "s"(w1), "s"(b1), "s"(w2), "s"(b2), "s"(gw), "s"(out), "s"(ws));
  const int tid = threadIdx.x, lane = tid & 63, w = __builtin_amdgcn_readfirstlane(tid >> 6);
  const int g = lane >> 4, c = lane & 15;
  const int bid = blockIdx.x, hk = bid & 7, idx = bid >> 3;
  const int ab = idx >> 3, as = idx & 7;
  const int kgl = idx >> 1, cg = idx & 1, kg = hk * 16 + kgl;
  const __amdgpu_buffer_rsrc_t rs = __builtin_amdgcn_make_buffer_rsrc((void*)ws, 0, (int)WS_END, 0x00020000);
  const unsigned cn = (unsigned)(size_t)(LAS float*)(lds + L_CNT);
#define CN(i) (cn + 4u * (i))

  if (w >= 4) {
    const int aw = w - 4;
    BAR();
    unsigned tt;
    {
      const unsigned char* tp = ws;
      const int fst = 16 * (14 * hk + (kgl < 14 ? kgl : 13));
      tp = lane == 0 ? (const unsigned char*)(out + (bid >> 6) * DM) : tp;
      tp = lane == 1 ? ws + WS_END + 64 : tp;
      tp = lane == 2 ? (const unsigned char*)b2 : tp;
      tp = lane == 3 ? (const unsigned char*)(wo + (size_t)(32 * kg) * DM) : tp;
      tp = lane == 4 ? (const unsigned char*)(wo + (size_t)(32 * kg + 31) * DM + 510) : tp;
      tp = lane == 5 ? (const unsigned char*)(w2 + (size_t)fst * DM + 256 * cg) : tp;
      tp = lane == 6 ? (const unsigned char*)(w2 + (size_t)(fst + 15) * DM + 256 * cg) : tp;
      tp = lane == 7 ? (const unsigned char*)(b1 + fst) : tp;
      tt = *(const GAS unsigned*)tp;
    }
    PIN();
    v4u kr[8], vf[8];
    {
      const size_t kvoff = ((size_t)(ab * 8 + hk) * SK + (SK - WINDOW) + 128 * as + 32 * aw) * DH;
      const u16* kp = kcache + kvoff + 8 * lane;
      const u16* vp = vcache + kvoff + 4 * g * DH + 8 * c;
#pragma unroll
      for (int i = 0; i < 8; ++i) kr[i] = ldnt<v4u>(kp + i * 512);
#pragma unroll
      for (int j = 0; j < 8; ++j) vf[j] = ldnt<v4u>(vp + (16 * (j >> 2) + (j & 3)) * DH);
    }
    PIN();
    SETPRIO(2);
    const unsigned T = (unsigned)__builtin_amdgcn_readlane(tt, 63) + 1u;
    v4u kf[8];
    {
      unsigned char* ks_ = (unsigned char*)(lds + L_KS) + aw * 8192;
#pragma unroll
      for (int i = 0; i < 8; ++i) { const int row = 4 * i + g; *(v4u*)(ks_ + row * 256 + ((c ^ (row & 15)) << 4)) = kr[i]; }
      asm volatile("s_waitcnt lgkmcnt(0)" ::: "memory");
#pragma unroll
      for (int t = 0; t < 2; ++t)
#pragma unroll
        for (int ks = 0; ks < 4; ++ks) kf[t * 4 + ks] = *(const v4u*)(ks_ + (16 * t + c) * 256 + (((4 * ks + g) ^ c) << 4));
#pragma unroll
      for (int i = 0; i < 8; ++i) KEEP(kf[i]);
    }
    PIN();
    asm volatile("s_waitcnt vmcnt(%0)" :: "n"(QGATE) : "memory");
    signal(CN(C_ISS), lane);
    await<0>(CN(C_QRDY), 1u);
    v4u pf;
    {
      f32x4 s0 = {0.f, 0.f, 0.f, 0.f}, s1 = {0.f, 0.f, 0.f, 0.f};
      const u16* qb = (const u16*)(lds + L_QB) + (c >> 2) * 128 + 8 * g;
      v4u qf[4];
#pragma unroll
      for (int ks = 0; ks < 4; ++ks) qf[ks] = *(const v4u*)(qb + 32 * ks);
#pragma unroll
      for (int ks = 0; ks < 4; ++ks) {
        s0 = mfma32(kf[ks], qf[ks], s0);
        s1 = mfma32(kf[4 + ks], qf[ks], s1);
      }
      float sc[8];
#pragma unroll
      for (int j = 0; j < 2; ++j) {
        const unsigned u0 = pack2(s0[2 * j], s0[2 * j + 1]), u1 = pack2(s1[2 * j], s1[2 * j + 1]);
        const unsigned v0 = pack2(lo2f(u0) * SCALE, hi2f(u0) * SCALE), v1 = pack2(lo2f(u1) * SCALE, hi2f(u1) * SCALE);
        sc[2 * j] = lo2f(v0); sc[2 * j + 1] = hi2f(v0); sc[4 + 2 * j] = lo2f(v1); sc[5 + 2 * j] = hi2f(v1);
      }
      float m = __builtin_fmaxf(__builtin_fmaxf(__builtin_fmaxf(sc[0], sc[1]), __builtin_fmaxf(sc[2], sc[3])),
                                __builtin_fmaxf(__builtin_fmaxf(sc[4], sc[5]), __builtin_fmaxf(sc[6], sc[7])));
      m = x32_max(x16_max(m));
      float p[8];
#pragma unroll
      for (int j = 0; j < 8; ++j) p[j] = __builtin_amdgcn_exp2f((sc[j] - m) * LOG2E);
      pf = v4u{pack2(p[0], p[1]), pack2(p[2], p[3]), pack2(p[4], p[5]), pack2(p[6], p[7])};
      float l = ((p[0] + p[1]) + (p[2] + p[3])) + ((p[4] + p[5]) + (p[6] + p[7]));
      l = x32_sum(x16_sum(l));
      if (g == 0 && (c & 3) == 0) *(f32x2*)(lds + L_ML + (aw * 4 + (c >> 2)) * 2) = f32x2{m * LOG2E, l};
      KEEP(pf);
    }
    PIN();
    {
      v4u vt[8];
#pragma unroll
      for (int i = 0; i < 8; ++i)
#pragma unroll
        for (int jj = 0; jj < 4; ++jj)
          vt[i][jj] = (i & 1) ? perm_hi(vf[2 * jj][i >> 1], vf[2 * jj + 1][i >> 1]) : perm_lo(vf[2 * jj][i >> 1], vf[2 * jj + 1][i >> 1]);
      f32x4 o[8];
#pragma unroll
      for (int i = 0; i < 8; ++i) o[i] = mfma32(pf, vt[i], f32x4{0.f, 0.f, 0.f, 0.f});
#pragma unroll
      for (int i = 0; i < 8; ++i) KEEP(o[i]);
      float* mo = lds + L_MO + (aw * 4 + g) * 128 + 8 * c;
      *(f32x4*)(mo) = f32x4{o[0][0], o[1][0], o[2][0], o[3][0]};
      *(f32x4*)(mo + 4) = f32x4{o[4][0], o[5][0], o[6][0], o[7][0]};
    }
    if (arrive(CN(C_ATT), lane) != 3u) await<0>(CN(C_ATT), 4u);
    {
      f32x2 ml[4], ov[4];
#pragma unroll
      for (int u = 0; u < 4; ++u) { ml[u] = *(const f32x2*)(lds + L_ML + (u * 4 + aw) * 2); ov[u] = *(const f32x2*)(lds + L_MO + (u * 4 + aw) * 128 + 2 * lane); }
      const float M = __builtin_fmaxf(__builtin_fmaxf(ml[0][0], ml[1][0]), __builtin_fmaxf(ml[2][0], ml[3][0]));
      float L = 0.f, o0 = 0.f, o1 = 0.f;
#pragma unroll
      for (int u = 0; u < 4; ++u) {
        const float f = __builtin_amdgcn_exp2f(ml[u][0] - M);
        L = fmaf(f, ml[u][1], L); o0 = fmaf(f, ov[u][0], o0); o1 = fmaf(f, ov[u][1], o1);
      }
      const int hg = 4 * hk + aw;
      const v4u pv = {__float_as_uint(o0), T, __float_as_uint(o1), T};
      __builtin_amdgcn_raw_buffer_store_b128(pv, rs, WS_P2 + (((hg * 4 + (lane >> 4)) * 4 + ab) * 8 + as) * 256 + (lane & 15) * 16, 0, AUX_SC1);
      if (lane < 8) {
        const v4u mv = {__float_as_uint(M), T, __float_as_uint(L), T};
        __builtin_amdgcn_raw_buffer_store_b128(mv, rs, WS_ML + ((hg * 4 + ab) * 8 + as) * 128 + lane * 16, 0, AUX_SC1);
      }
    }
    PIN();
    __builtin_amdgcn_s_sleep(DLY2);
    signal(CN(C_PUB2), lane);
    const int ob = bid >> 6, ocw = bid & 7, om = (bid >> 4) & 3, oh = (bid >> 3) & 1;
    const int col = 64 * ocw + 4 * (4 * om + (lane & 3)) + 2 * oh;
    const unsigned b2v = ldg<unsigned>(b2 + col);
    const unsigned off = WS_P3 + ((aw * 32 + (lane >> 2)) * 8 + ocw) * 2048 + oh * 1024 + (ob * 16 + 4 * om + (lane & 3)) * 16;
    await<1>(CN(C_PUB3), 4u);
    v4u pv0 = __builtin_amdgcn_raw_buffer_load_b128(rs, off, 0, AUX_SC1);
    v4u pv1 = __builtin_amdgcn_raw_buffer_load_b128(rs, off + 16 * 8 * 2048, 0, AUX_SC1);
#pragma clang loop unroll(disable)
    for (unsigned spins = 0; spins < SPIN_CAP; ++spins) {
      const unsigned bad0 = tagbad(pv0, T), bad1 = tagbad(pv1, T);
      if (__all((bad0 | bad1) == 0)) break;
      asm volatile("" ::: "memory");
      if (bad0) pv0 = __builtin_amdgcn_raw_buffer_load_b128(rs, off, 0, AUX_SC1);
      if (bad1) pv1 = __builtin_amdgcn_raw_buffer_load_b128(rs, off + 16 * 8 * 2048, 0, AUX_SC1);
    }
    float sa = __uint_as_float(pv0[0]) + __uint_as_float(pv1[0]), sb = __uint_as_float(pv0[2]) + __uint_as_float(pv1[2]);
    sa += dpp<ROR4>(sa); sb += dpp<ROR4>(sb); sa += dpp<ROR8>(sa); sb += dpp<ROR8>(sb);
    sa = x16_sum(sa); sb = x16_sum(sb); sa = x32_sum(sa); sb = x32_sum(sb);
    if (lane < 4) *(f32x2*)(lds + L_FR + aw * 8 + 2 * lane) = f32x2{sa, sb};
    if (arrive(CN(C_FIN), lane) == 3u) {
      if (lane < 4) {
        const float omg = lds[L_ST + 4 * ob + 3];
        f32x2 t = *(const f32x2*)(lds + L_FR + 2 * lane);
#pragma unroll
        for (int u = 1; u < 4; ++u) t += *(const f32x2*)(lds + L_FR + u * 8 + 2 * lane);
        *(GAS unsigned*)(out + ob * DM + col) = pack2(fmaf(omg, lo2f(b2v), t[0]), fmaf(omg, hi2f(b2v), t[1]));
      }
      if (bid == 0 && lane == 0) __hip_atomic_store((gu32*)ws, T, __ATOMIC_RELAXED, __HIP_MEMORY_SCOPE_AGENT);
    }
  } else {
    const int pw = w;
    SETPRIO(3);
    const int kq = idx & 3, cq = idx >> 2;
    const bool has_ht = idx < 14;
    const int th = 14 * hk + idx;
    const int qrow = 128 * kq + 32 * pw + 8 * g, hrow = 128 * (th & 3) + 32 * pw + 8 * g;
    v2u wv[8], w1v[8];
    {
      const u16* p = wq + (size_t)qrow * NQ + hk * 512 + 64 * cq + 4 * c;
#pragma unroll
      for (int j = 0; j < 8; ++j) wv[j] = ldnt<v2u>(p + j * NQ);
    }
    const v4u xa = ldg<v4u>(x + (c >> 2) * DM + qrow);
    const unsigned ev = __builtin_amdgcn_raw_buffer_load_b32(rs, 0, 0, 0);
    PIN();
    const v4u xs = ldg<v4u>(x + pw * DM + 8 * lane);
    const v4u gwv = ldg<v4u>(gw + 8 * lane);
    const v4u xh = ldg<v4u>(x + (c >> 2) * DM + hrow);
    {
      const __amdgpu_buffer_rsrc_t rw1 = __builtin_amdgcn_make_buffer_rsrc((void*)w1, 0, has_ht ? DM * FF * 2 : 0, 0x00020000);
      const int o = (hrow * FF + 64 * (th >> 2) + 4 * c) * 2;
#pragma unroll
      for (int j = 0; j < 8; ++j) w1v[j] = __builtin_amdgcn_raw_buffer_load_b64(rw1, o + j * FF * 2, 0, AUX_NT);
    }
    if (tid < 16) ((LAS unsigned*)(lds + L_CNT))[tid] = 0u;
    BAR();
    PIN();
    unsigned T;
    {
      f32x4 d[4];
#pragma unroll
      for (int i = 0; i < 4; ++i) d[i] = mfma32(xa, tr8x4(wv, i), f32x4{0.f, 0.f, 0.f, 0.f});
      *(f32x4*)(lds + L_RQ + (pw * 64 + lane) * 4) = f32x4{d[0][0], d[1][0], d[2][0], d[3][0]};
      T = (unsigned)__builtin_amdgcn_readfirstlane(ev) + 1u;
      if (arrive(CN(C_Q), lane) == 3u) {
        f32x4 a = *(const f32x4*)(lds + L_RQ + lane * 4);
#pragma unroll
        for (int p = 1; p < 4; ++p) a += *(const f32x4*)(lds + L_RQ + (p * 64 + lane) * 4);
        const unsigned off = WS_QP + ((hk * 8 + cq) * 4 + kq) * 2048 + lane * 16;
        __builtin_amdgcn_raw_buffer_store_b128(v4u{__float_as_uint(a[0]), T, __float_as_uint(a[1]), T}, rs, off, 0, AUX_SC1);
        __builtin_amdgcn_raw_buffer_store_b128(v4u{__float_as_uint(a[2]), T, __float_as_uint(a[3]), T}, rs, off + 1024, 0, AUX_SC1);
      }
    }
    PIN();
    {
      float xv[8], gv[8];
#pragma unroll
      for (int k = 0; k < 4; ++k) { xv[2 * k] = lo2f(xs[k]); xv[2 * k + 1] = hi2f(xs[k]); gv[2 * k] = lo2f(gwv[k]); gv[2 * k + 1] = hi2f(gwv[k]); }
      float s = 0.f, gs = 0.f;
#pragma unroll
      for (int k = 0; k < 8; ++k) { s += xv[k]; gs += bfr(xv[k] * gv[k]); }
      s = wave_sum(s); gs = wave_sum(gs);
      const float mean = s * (1.0f / DM);
      float vs = 0.f;
#pragma unroll
      for (int k = 0; k < 8; ++k) { const float d = xv[k] - mean; vs = fmaf(d, d, vs); }
      vs = wave_sum(vs);
      if (lane == 0) {
        const float var_b = bfr(vs * (1.0f / DM));
        const float den = bfr(sqrtf(bfr(var_b + 1e-5f)));
        const float gt = bfr(1.0f / (1.0f + __expf(-bfr(gs))));
        *(f32x4*)(lds + L_ST + 4 * pw) = f32x4{bfr(mean), den, gt, bfr(1.0f - gt)};
      }
      if (arrive(CN(C_ST), lane) != 3u) await<0>(CN(C_ST), 4u);
    }
    if (has_ht) {
      const f32x4 st = *(const f32x4*)(lds + L_ST + 4 * (c >> 2));
      v4u ya;
#pragma unroll
      for (int k = 0; k < 4; ++k) ya[k] = pack2(bfr(lo2f(xh[k]) - st[0]) / st[1], bfr(hi2f(xh[k]) - st[0]) / st[1]);
      f32x4 d[4];
#pragma unroll
      for (int i = 0; i < 4; ++i) d[i] = mfma32(ya, tr8x4(w1v, i), f32x4{0.f, 0.f, 0.f, 0.f});
      *(f32x4*)(lds + L_RH + (pw * 64 + lane) * 4) = f32x4{d[0][0], d[1][0], d[2][0], d[3][0]};
      if (arrive(CN(C_H), lane) == 3u) {
        f32x4 a = *(const f32x4*)(lds + L_RH + lane * 4);
#pragma unroll
        for (int p = 1; p < 4; ++p) a += *(const f32x4*)(lds + L_RH + (p * 64 + lane) * 4);
        const unsigned off = WS_HP + th * 2048 + lane * 16;
        __builtin_amdgcn_raw_buffer_store_b128(v4u{__float_as_uint(a[0]), T, __float_as_uint(a[1]), T}, rs, off, 0, AUX_SC1);
        __builtin_amdgcn_raw_buffer_store_b128(v4u{__float_as_uint(a[2]), T, __float_as_uint(a[3]), T}, rs, off + 1024, 0, AUX_SC1);
      }
    }
    f32x4 stg = *(const f32x4*)(lds + L_ST + 4 * g);
    KEEP(stg);
    PIN();
    await<2>(CN(C_ISS), 4u);
    const bool has_mlp = kgl < 14;
    const int fs = 14 * hk + kgl, f0 = 16 * fs;
    v2u wov[8], w2v[4];
    {
      const unsigned qoff = WS_QP + ((hk * 8 + (lane >> 5)) * 4 + pw) * 2048 + ((lane >> 4) & 1) * 1024 + (16 * ab + c) * 16;
      v4u qv[4];
#pragma unroll
      for (int i = 0; i < 4; ++i) qv[i] = __builtin_amdgcn_raw_buffer_load_b128(rs, qoff + i * 16384, 0, AUX_SC1);
      PIN();
      unsigned bad0 = tagbad(qv[0], T) | tagbad(qv[1], T) | tagbad(qv[2], T) | tagbad(qv[3], T);
      if (!__all(bad0 == 0)) {
#pragma clang loop unroll(disable)
        for (unsigned spins = 0; spins < SPIN_CAP; ++spins) {
          unsigned bad[4];
#pragma unroll
          for (int i = 0; i < 4; ++i) bad[i] = tagbad(qv[i], T);
          if (__all((bad[0] | bad[1] | bad[2] | bad[3]) == 0)) break;
          asm volatile("" ::: "memory");
#pragma unroll
          for (int i = 0; i < 4; ++i) if (bad[i]) qv[i] = __builtin_amdgcn_raw_buffer_load_b128(rs, qoff + i * 16384, 0, AUX_SC1);
        }
      }
      float* qs = lds + L_QS + (pw * 64 + lane) * 8;
      *(v4u*)(qs) = v4u{qv[0][0], qv[0][2], qv[1][0], qv[1][2]};
      *(v4u*)(qs + 4) = v4u{qv[2][0], qv[2][2], qv[3][0], qv[3][2]};
      if (arrive(CN(C_QG), lane) == 3u) {
        f32x4 a0 = *(const f32x4*)(lds + L_QS + lane * 8), a1 = *(const f32x4*)(lds + L_QS + lane * 8 + 4);
#pragma unroll
        for (int p = 1; p < 4; ++p) { a0 += *(const f32x4*)(lds + L_QS + (p * 64 + lane) * 8); a1 += *(const f32x4*)(lds + L_QS + (p * 64 + lane) * 8 + 4); }
        unsigned* qb = (unsigned*)(lds + L_QB) + 32 * (lane >> 5) + 2 * c + ((lane >> 4) & 1);
        qb[0] = pack2(a0[0], a0[1]); qb[64] = pack2(a0[2], a0[3]); qb[128] = pack2(a1[0], a1[1]); qb[192] = pack2(a1[2], a1[3]);
        signal(CN(C_QRDY), lane);
      }
    }
    const bool hgat = pw == 3 && has_mlp;
    const unsigned hoff = WS_HP + ((fs >> 2) * 4 + g) * 2048 + (16 * (c >> 2) + 4 * (fs & 3) + (c & 3)) * 16;
    v4u h0, h1;
    unsigned b1raw;
    {
      const __amdgpu_buffer_rsrc_t rsh = __builtin_amdgcn_make_buffer_rsrc((void*)ws, 0, hgat ? (int)WS_END : 0, 0x00020000);
      const __amdgpu_buffer_rsrc_t rb1 = __builtin_amdgcn_make_buffer_rsrc((void*)b1, 0, hgat ? FF * 2 : 0, 0x00020000);
      h0 = __builtin_amdgcn_raw_buffer_load_b128(rsh, hoff, 0, AUX_SC1);
      h1 = __builtin_amdgcn_raw_buffer_load_b128(rsh, hoff + 1024, 0, AUX_SC1);
      b1raw = __builtin_amdgcn_raw_buffer_load_b16(rb1, (f0 + 4 * (c & 3) + g) * 2, 0, 0);
      PIN();
      const __amdgpu_buffer_rsrc_t rw2 = __builtin_amdgcn_make_buffer_rsrc((void*)w2, 0, has_mlp ? DM * FF * 2 : 0, 0x00020000);
      const u16* p = wo + (size_t)(32 * kg + 8 * g) * DM + 256 * cg + 64 * pw + 4 * c;
      const int o2 = ((f0 + 4 * g) * DM + 256 * cg + 64 * pw + 4 * c) * 2;
#pragma unroll
      for (int j = 0; j < 8; ++j) wov[j] = ldnt<v2u>(p + j * DM);
#pragma unroll
      for (int j = 0; j < 4; ++j) w2v[j] = __builtin_amdgcn_raw_buffer_load_b64(rw2, o2 + j * DM * 2, 0, AUX_NT);
    }
    PIN();
    if (pw == 3) {
      float hv = 0.f;
      if (has_mlp) {
        if (!__all((tagbad(h0, T) | tagbad(h1, T)) == 0)) {
#pragma clang loop unroll(disable)
          for (unsigned spins = 0; spins < SPIN_CAP; ++spins) {
            const unsigned bad = tagbad(h0, T) | tagbad(h1, T);
            if (__all(bad == 0)) break;
            asm volatile("" ::: "memory");
            if (bad) {
              h0 = __builtin_amdgcn_raw_buffer_load_b128(rs, hoff, 0, AUX_SC1);
              h1 = __builtin_amdgcn_raw_buffer_load_b128(rs, hoff + 1024, 0, AUX_SC1);
            }
          }
        }
        float a[4] = {__uint_as_float(h0[0]), __uint_as_float(h0[2]), __uint_as_float(h1[0]), __uint_as_float(h1[2])};
#pragma unroll
        for (int i = 0; i < 4; ++i) a[i] = x32_sum(x16_sum(a[i]));
        const float av = g == 0 ? a[0] : g == 1 ? a[1] : g == 2 ? a[2] : a[3];
        const float u = bfr(bfr(av) + lo2f(b1raw));
        const float inner = 0.7978845608028654f * (u + 0.044715f * u * u * u);
        hv = 0.5f * u * (1.0f + tanhf(inner));
      }
      ((u16*)(lds + L_HB))[(c >> 2) * 16 + 4 * (c & 3) + g] = (u16)pack2(hv, 0.f);
      signal(CN(C_HRDY), lane);
    }
    const int h2 = 4 * hk + (kgl >> 2);
    const unsigned p2off = WS_P2 + (((h2 * 4 + (kgl & 3)) * 4 + pw) * 8 + (lane >> 3)) * 256 + (lane & 7) * 32;
    const unsigned mloff = WS_ML + ((h2 * 4 + pw) * 8 + (lane >> 3)) * 128 + (lane & 7) * 16;
    await<1>(CN(C_PUB2), 4u);
    v4u a0 = __builtin_amdgcn_raw_buffer_load_b128(rs, p2off, 0, AUX_SC1);
    v4u a1 = __builtin_amdgcn_raw_buffer_load_b128(rs, p2off + 16, 0, AUX_SC1);
    v4u am = __builtin_amdgcn_raw_buffer_load_b128(rs, mloff, 0, AUX_SC1);
    PIN();
    v4u wof[4];
    float accm[4];
    {
      v2u w2f[4];
#pragma unroll
      for (int i = 0; i < 4; ++i) {
        wof[i] = tr8x4(wov, i);
#pragma unroll
        for (int jj = 0; jj < 2; ++jj)
          w2f[i][jj] = (i & 1) ? perm_hi(w2v[2 * jj][i >> 1], w2v[2 * jj + 1][i >> 1]) : perm_lo(w2v[2 * jj][i >> 1], w2v[2 * jj + 1][i >> 1]);
        KEEP(wof[i]);
      }
      await<1>(CN(C_HRDY), 1u);
      const v2u ha = *(const v2u*)((const u16*)(lds + L_HB) + (c >> 2) * 16 + 4 * g);
#pragma unroll
      for (int i = 0; i < 4; ++i) {
        const f32x4 e = mfma16(ha, w2f[i], f32x4{0.f, 0.f, 0.f, 0.f});
        accm[i] = stg[3] * e[0];
        KEEP(accm[i]);
      }
    }
    PIN();
#pragma clang loop unroll(disable)
    for (unsigned spins = 0; spins < SPIN_CAP; ++spins) {
      const unsigned bad = tagbad(a0, T) | tagbad(a1, T) | tagbad(am, T);
      if (__all(bad == 0)) break;
      asm volatile("" ::: "memory");
      if (bad) {
        a0 = __builtin_amdgcn_raw_buffer_load_b128(rs, p2off, 0, AUX_SC1);
        a1 = __builtin_amdgcn_raw_buffer_load_b128(rs, p2off + 16, 0, AUX_SC1);
        am = __builtin_amdgcn_raw_buffer_load_b128(rs, mloff, 0, AUX_SC1);
      }
    }
    {
      const float m = __uint_as_float(am[0]), l = __uint_as_float(am[2]);
      float M = __builtin_fmaxf(m, dpp<ROR8>(m)); M = x32_max(x16_max(M));
      const float f = __builtin_amdgcn_exp2f(m - M);
      float v[5] = {f * __uint_as_float(a0[0]), f * __uint_as_float(a0[2]), f * __uint_as_float(a1[0]), f * __uint_as_float(a1[2]), f * l};
#pragma unroll
      for (int i = 0; i < 5; ++i) v[i] += dpp<ROR8>(v[i]);
#pragma unroll
      for (int i = 0; i < 5; ++i) v[i] = x16_sum(v[i]);
#pragma unroll
      for (int i = 0; i < 5; ++i) v[i] = x32_sum(v[i]);
      const float inv = __builtin_amdgcn_rcpf(v[4]);
      if (lane < 8) *(v2u*)((u16*)(lds + L_OB) + pw * 32 + 4 * lane) = v2u{pack2(v[0] * inv, v[1] * inv), pack2(v[2] * inv, v[3] * inv)};
    }
    if (arrive(CN(C_O), lane) != 3u) await<0>(CN(C_O), 4u);
    {
      const v4u oa = *(const v4u*)((const u16*)(lds + L_OB) + (c >> 2) * 32 + 8 * g);
      f32x4 d[4];
#pragma unroll
      for (int i = 0; i < 4; ++i) d[i] = mfma32(oa, wof[i], f32x4{0.f, 0.f, 0.f, 0.f});
#pragma unroll
      for (int i = 0; i < 4; ++i) KEEP(d[i]);
      const unsigned off = WS_P3 + ((kg * 2 + cg) * 4 + pw) * 2048 + lane * 16;
      const v4u s0 = {__float_as_uint(fmaf(stg[2], d[0][0], accm[0])), T, __float_as_uint(fmaf(stg[2], d[1][0], accm[1])), T};
      const v4u s1 = {__float_as_uint(fmaf(stg[2], d[2][0], accm[2])), T, __float_as_uint(fmaf(stg[2], d[3][0], accm[3])), T};
      __builtin_amdgcn_raw_buffer_store_b128(s0, rs, off, 0, AUX_SC1);
      __builtin_amdgcn_raw_buffer_store_b128(s1, rs, off + 1024, 0, AUX_SC1);
    }
    PIN();
    __builtin_amdgcn_s_sleep(DLY3);
    signal(CN(C_PUB3), lane);
  }
}

extern "C" void kernel_launch(void* const* d_in, const int* in_sizes, int n_in,
                              void* d_out, int out_size, void* d_ws, size_t ws_size,
                              hipStream_t stream) {
  (void)in_sizes; (void)n_in; (void)out_size; (void)ws_size;
  fused_kernel<<<256, 512, 0, stream>>>(
      (const u16*)d_in[0], (const u16*)d_in[1], (const u16*)d_in[2], (const u16*)d_in[3], (const u16*)d_in[4],
      (const u16*)d_in[6], (const u16*)d_in[7], (const u16*)d_in[8], (const u16*)d_in[9], (const u16*)d_in[10],
      (u16*)d_out, (unsigned char*)d_ws);
}
```
